```python
import torch

def _softmax(x, axis=-1):
    return torch.softmax(x, dim=axis)
def _gelu(x):
    return torch.nn.functional.gelu(x, approximate="tanh")
def _silu(x):
    return torch.nn.functional.silu(x)
def _sigmoid(x):
    return torch.sigmoid(x)
def _layernorm(x, axis=-1, eps=1e-5):
    m = x.mean(dim=axis, keepdim=True)
    v = x.var(dim=axis, keepdim=True, unbiased=False)
    return (x - m) / torch.sqrt(v + eps)
def _scatter_add(tgt, idx, src, axis=0):
    return tgt.clone().index_add_(axis, idx, src)
def _segment_sum(src, seg, n):
    out = torch.zeros((n,) + src.shape[1:], dtype=src.dtype)
    return out.index_add_(0, seg, src)
def _pad(x, pw):
    flat = tuple(v for pair in reversed(pw) for v in pair)
    return torch.nn.functional.pad(x, flat)

kernel_name = 'sequential_preLN__ssm_conv_s4d__glu_gated_geglu__prompt'

def setup_inputs():

    _w = (lambda a: (lambda t: t.to(torch.bfloat16) if t.is_floating_point() else t)(torch.as_tensor(a)))
    def _A_setup():
        Bsz, L, D, N = 1, 2048, 512, 128
        return {
            "u": _w((lambda *s: torch.randn(*s, dtype=torch.bfloat16) * 0.05)(Bsz, L, D)),
            "log_A_real": _w((lambda *s: torch.randn(*s, dtype=torch.bfloat16) * 6.4)(D, N) * 0.5),
            "A_imag": _w((lambda *s: torch.randn(*s, dtype=torch.bfloat16) * 0.05)(D, N) * 3.14),
            "C_real": _w((lambda *s: torch.randn(*s, dtype=torch.bfloat16) * 0.05)(D, N) / N ** 0.5),
            "C_imag": _w((lambda *s: torch.randn(*s, dtype=torch.bfloat16) * 0.05)(D, N) / N ** 0.5),
            "log_dt": _w((lambda *s: torch.randn(*s, dtype=torch.bfloat16) * 0.05)(D) * 0.5 - 4.0),
            "D_skip": _w((lambda *s: torch.randn(*s, dtype=torch.bfloat16) * 0.05)(D)),
        }
    _A_in = _A_setup()

    def _B_setup():
        B, S, Dm, Df = 1, 2048, 512, 1792
        return {
            "x": _w((lambda *s: torch.randn(*s, dtype=torch.bfloat16) * 0.05)(B, S, Dm)),
            "w_gate": _w((lambda *s: torch.randn(*s, dtype=torch.bfloat16) * 0.4)(Dm, Df) * (Dm ** -0.5)),
            "w_up": _w((lambda *s: torch.randn(*s, dtype=torch.bfloat16) * 0.4)(Dm, Df) * (Dm ** -0.5)),
            "w_down": _w((lambda *s: torch.randn(*s, dtype=torch.bfloat16) * 0.4)(Df, Dm) * (Df ** -0.5)),
        }
    _B_in = _B_setup()

    return {
        "A_u": _A_in["u"],
        "A_log_A_real": _A_in["log_A_real"],
        "A_A_imag": _A_in["A_imag"],
        "A_C_real": _A_in["C_real"],
        "A_C_imag": _A_in["C_imag"],
        "A_log_dt": _A_in["log_dt"],
        "A_D_skip": _A_in["D_skip"],
        "B_x": _B_in["x"],
        "B_w_gate": _B_in["w_gate"],
        "B_w_up": _B_in["w_up"],
        "B_w_down": _B_in["w_down"],
    }

def reference(A_u, A_log_A_real, A_A_imag, A_C_real, A_C_imag, A_log_dt, A_D_skip, B_x, B_w_gate, B_w_up, B_w_down):
    def _A_ref(u, log_A_real, A_imag, C_real, C_imag, log_dt, D_skip):
        L = u.shape[1]
        dt = torch.exp(log_dt)
        A = -torch.exp(log_A_real) + 1j * A_imag
        C = (C_real + 1j * C_imag) * dt[:, None]
        pos = (lambda x, dt: x.to(dt))(torch.arange(L), log_dt.dtype)
        P = torch.exp((A * dt[:, None])[:, :, None] * pos[None, None, :])
        K = 2.0 * torch.einsum("dn,dnl->dl", C, P).real
        L2 = 2 * L
        u_t = (lambda x, *d: x.permute(*d))(u, 0, 2, 1)
        Kf = (lambda x, *a, **k: torch.fft.rfft(x.float(), *a, **k))(K, L2)
        Uf = (lambda x, *a, **k: torch.fft.rfft(x.float(), *a, **k))(u_t, L2)
        y = (lambda x, *a, **k: torch.fft.irfft(x, *a, **k).to(torch.bfloat16))(Uf * Kf[None, :, :], L2)[:, :, :L]
        y = (lambda x, *d: x.permute(*d))(y, 0, 2, 1)
        return y + u * D_skip[None, None, :]

    def _B_ref(x, w_gate, w_up, w_down):
        g = torch.einsum("bsd,df->bsf", x, w_gate)
        u = torch.einsum("bsd,df->bsf", x, w_up)
        h = _gelu(g) * u
        return torch.einsum("bsf,fd->bsd", h, w_down)

    x = A_u
    h = x + _A_ref(_layernorm(x, axis=-1), A_log_A_real, A_A_imag, A_C_real, A_C_imag, A_log_dt, A_D_skip)
    h2 = h + _B_ref(_layernorm(h, axis=-1), B_w_gate, B_w_up, B_w_down)
    return h2
```

```cpp
#include <hip/hip_runtime.h>
#include <hip/hip_bf16.h>
#include <stdint.h>

typedef unsigned short u16;
__device__ __forceinline__ float bf2f(u16 v){ return __uint_as_float(((unsigned)v)<<16); }
__device__ __forceinline__ u16 f2bf(float f){ unsigned u=__float_as_uint(f); u += 0x7FFF + ((u>>16)&1); return (u16)(u>>16); }
__device__ __forceinline__ float rbf(float f){ return bf2f(f2bf(f)); }

constexpr int L=2048, D=512, N=128, F=1792;

__global__ void ln_kernel(const u16* x, u16* u){
  int row=blockIdx.x; int t=threadIdx.x;
  float v[8]; float s=0;
  for(int j=0;j<8;j++){ v[j]=bf2f(x[row*D+t*8+j]); s+=v[j]; }
  for(int o=32;o;o>>=1) s+=__shfl_xor(s,o);
  float mean=s/D;
  float q=0; for(int j=0;j<8;j++){ float d=v[j]-mean; q+=d*d; }
  for(int o=32;o;o>>=1) q+=__shfl_xor(q,o);
  float var=rbf(q/D); float m=rbf(mean);
  float sd=rbf(sqrtf(rbf(var+1e-5f)));
  for(int j=0;j<8;j++) u[row*D+t*8+j]=f2bf(rbf(v[j]-m)/sd);
}
__global__ void kgen_kernel(const u16* logA,const u16* Aim,const u16* Cr,const u16* Ci,const u16* logdt,float* K){
  int d=blockIdx.x;
  float dt=rbf(expf(bf2f(logdt[d])));
  for(int l=threadIdx.x;l<L;l+=blockDim.x){
    float p=rbf((float)l);
    float acc=0;
    for(int n=0;n<N;n++){
      float a=-rbf(expf(bf2f(logA[d*N+n])))*dt; float b=bf2f(Aim[d*N+n])*dt;
      float cr=bf2f(Cr[d*N+n])*dt, ci=bf2f(Ci[d*N+n])*dt;
      float e=expf(a*p); float sn,cs; sincosf(b*p,&sn,&cs);
      acc+= cr*e*cs - ci*e*sn;
    }
    K[d*L+l]=2.f*acc;
  }
}
__global__ void conv_kernel(const u16* x,const u16* u,const float* K,const u16* Dsk,u16* h){
  __shared__ float su[L]; __shared__ float sk[L];
  int d=blockIdx.x;
  for(int l=threadIdx.x;l<L;l+=blockDim.x){ su[l]=bf2f(u[l*D+d]); sk[l]=K[d*L+l]; }
  __syncthreads();
  float ds=bf2f(Dsk[d]);
  for(int t=threadIdx.x;t<L;t+=blockDim.x){
    float acc=0; for(int l=0;l<=t;l++) acc+=sk[l]*su[t-l];
    float y=rbf(acc); float s=rbf(y+rbf(su[t]*ds));
    h[t*D+d]=f2bf(bf2f(x[t*D+d])+s);
  }
}
__global__ void ffn1_kernel(const u16* xn,const u16* wg,const u16* wu,u16* H){
  __shared__ float sx[8][D];
  int r0=blockIdx.x*8; int f=blockIdx.y*256+threadIdx.x;
  for(int i=threadIdx.x;i<8*D;i+=256) sx[i/D][i%D]=bf2f(xn[r0*D+i]);
  __syncthreads();
  float g[8]={},uu[8]={};
  for(int k=0;k<D;k++){ float a=bf2f(wg[k*F+f]), b=bf2f(wu[k*F+f]); for(int r=0;r<8;r++){ g[r]+=sx[r][k]*a; uu[r]+=sx[r][k]*b; } }
  for(int r=0;r<8;r++){ float gg=rbf(g[r]); float u2=rbf(uu[r]);
    float ge=rbf(0.5f*gg*(1.f+tanhf(0.7978845608f*(gg+0.044715f*gg*gg*gg))));
    H[(r0+r)*F+f]=f2bf(ge*u2); }
}
__global__ void ffn2_kernel(const u16* H,const u16* wd,const u16* h,u16* out){
  __shared__ float sx[8][F];
  int r0=blockIdx.x*8; int c=blockIdx.y*256+threadIdx.x;
  for(int i=threadIdx.x;i<8*F;i+=256) sx[i/F][i%F]=bf2f(H[r0*F+i]);
  __syncthreads();
  float g[8]={};
  for(int k=0;k<F;k++){ float a=bf2f(wd[k*D+c]); for(int r=0;r<8;r++) g[r]+=sx[r][k]*a; }
  for(int r=0;r<8;r++) out[(r0+r)*D+c]=f2bf(bf2f(h[(r0+r)*D+c])+rbf(g[r]));
}

extern "C" void kernel_launch(void* const* d_in, const int* in_sizes, int n_in, void* d_out, int out_size, void* d_ws, size_t ws_size, hipStream_t stream) {
  const u16* x=(const u16*)d_in[0];
  char* ws=(char*)d_ws;
  u16* u=(u16*)ws; float* K=(float*)(ws+(4<<20)); u16* h=(u16*)(ws+(12<<20)); u16* xn=(u16*)(ws+(16<<20)); u16* H=(u16*)(ws+(20<<20));
  ln_kernel<<<L,64,0,stream>>>(x,u);
  kgen_kernel<<<D,256,0,stream>>>((const u16*)d_in[1],(const u16*)d_in[2],(const u16*)d_in[3],(const u16*)d_in[4],(const u16*)d_in[5],K);
  conv_kernel<<<D,256,0,stream>>>(x,u,K,(const u16*)d_in[6],h);
  ln_kernel<<<L,64,0,stream>>>(h,xn);
  ffn1_kernel<<<dim3(L/8,F/256),256,0,stream>>>(xn,(const u16*)d_in[8],(const u16*)d_in[9],H);
  ffn2_kernel<<<dim3(L/8,D/256),256,0,stream>>>(H,(const u16*)d_in[10],h,(u16*)d_out);
}
```

```cpp
#include <hip/hip_runtime.h>
#include <stdint.h>

typedef unsigned short u16;
typedef unsigned long long u64;
typedef unsigned v4u __attribute__((ext_vector_type(4)));
typedef unsigned v2u __attribute__((ext_vector_type(2)));
typedef float f32x2 __attribute__((ext_vector_type(2)));
typedef float f32x4 __attribute__((ext_vector_type(4)));
typedef float f32x16 __attribute__((ext_vector_type(16)));
typedef short s16x4 __attribute__((ext_vector_type(4)));
typedef _Float16 f16x8 __attribute__((ext_vector_type(8)));
typedef _Float16 f16x2 __attribute__((ext_vector_type(2)));
typedef __bf16 b16x8 __attribute__((ext_vector_type(8)));
typedef __bf16 b16x2 __attribute__((ext_vector_type(2)));
#define LAS __attribute__((address_space(3)))
#define GAS __attribute__((address_space(1)))
typedef LAS unsigned char* lptr;

#define STAMP 0

constexpr int SEQ = 2048, DM = 512, NMODE = 128, FF = 1792;
constexpr int SC1 = 16;
constexpr int SPIN_CAP = 20000;
constexpr int R4 = 8;

constexpr int WS_EPOCH = 0;
constexpr int WS_ST1 = 4096;
constexpr int WS_F2 = 32768;
constexpr int WS_F3 = 49152;
constexpr int WS_F4 = 65536;
constexpr int WS_F6 = WS_F4 + R4 * 32768;
constexpr int WS_CS = WS_F6 + 65536;
constexpr int WS_HT = 1 << 20;
constexpr int WS_W1F = 4 << 20;
constexpr int WS_WDF = 8 << 20;
constexpr int WS_HF = 10 << 20;
constexpr int WS_DBG = 20 << 20;
constexpr int WS_BYTES = 24 << 20;
static_assert(WS_CS + 112 * 16 * 32 * 4 <= WS_HT, "ws map");

constexpr int L_B0 = 0, L_B1 = 32768, L_B2 = 65536, L_B3 = 98304;
constexpr int L_YF = 0, L_PT = 16384, L_CT = 24576, L_RT = 32768, L_RT2 = 49152;
constexpr int L_KX = 65536, KX_CP = 4160, KX_CH = 4 * KX_CP;
constexpr int L_U = L_KX + 2 * KX_CH, U_CH = 4576;
constexpr int L_XB = L_U + 2 * U_CH, L_UD = L_XB + 8192, L_RED = L_UD + 8192;
constexpr int L_KC = L_RED + 24576, KC_CH = 1312;
constexpr int L_CNT = L_KC + 2 * KC_CH;
constexpr int L_ST2 = L_CNT + 64;
constexpr int L_CSW = L_ST2 + 1024;
constexpr int L_HX = L_CSW + 1024;
constexpr int L_STAMP = L_HX + 4096;
constexpr int L_BYTES = L_STAMP + 1024;
constexpr int L_RED2 = 0;
static_assert(L_BYTES <= 163840, "lds");
static_assert(L_U % 16 == 0 && L_XB % 16 == 0 && L_RED % 16 == 0 && L_KC % 16 == 0 && L_CNT % 16 == 0, "align");

struct Params {
  const u16 *x, *logA, *aim, *cre, *cim, *logdt, *dskip, *wg, *wu, *wd;
  u16* out;
  unsigned char* ws;
};

__device__ __forceinline__ float asf(unsigned v) { return __builtin_bit_cast(float, v); }
__device__ __forceinline__ unsigned asu(float v) { return __builtin_bit_cast(unsigned, v); }
__device__ __forceinline__ float bflo(unsigned v) { return asf(v << 16); }
__device__ __forceinline__ float bfhi(unsigned v) { return asf(v & 0xffff0000u); }
__device__ __forceinline__ unsigned pkbf(float lo, float hi) {
  b16x2 r = {(__bf16)lo, (__bf16)hi};
  return __builtin_bit_cast(unsigned, r);
}
__device__ __forceinline__ float rbf(float f) { return bflo(pkbf(f, f)); }
__device__ __forceinline__ unsigned pkh(float lo, float hi) {
  f16x2 r = {(_Float16)lo, (_Float16)hi};
  return __builtin_bit_cast(unsigned, r);
}
template <int C> __device__ __forceinline__ float dppf(float v) {
  return asf((unsigned)__builtin_amdgcn_update_dpp(0, (int)asu(v), C, 0xf, 0xf, true));
}
__device__ __forceinline__ float x16_add(float v) {
  auto s = __builtin_amdgcn_permlane16_swap(asu(v), asu(v), false, false);
  return asf(s[0]) + asf(s[1]);
}
__device__ __forceinline__ float x32_add(float v) {
  auto s = __builtin_amdgcn_permlane32_swap(asu(v), asu(v), false, false);
  return asf(s[0]) + asf(s[1]);
}
__device__ __forceinline__ float wave_sum(float v) {
  v += dppf<0xB1>(v); v += dppf<0x4E>(v); v += dppf<0x141>(v); v += dppf<0x140>(v);
  return x32_add(x16_add(v));
}
__device__ __forceinline__ float wave_max(float v) {
  v = fmaxf(v, dppf<0xB1>(v)); v = fmaxf(v, dppf<0x4E>(v)); v = fmaxf(v, dppf<0x141>(v)); v = fmaxf(v, dppf<0x140>(v));
  auto s = __builtin_amdgcn_permlane16_swap(asu(v), asu(v), false, false);
  v = fmaxf(asf(s[0]), asf(s[1]));
  auto t = __builtin_amdgcn_permlane32_swap(asu(v), asu(v), false, false);
  return fmaxf(asf(t[0]), asf(t[1]));
}
__device__ __forceinline__ float dot2acc(unsigned a, unsigned b, float c) {
  return __builtin_amdgcn_fdot2_f32_bf16(__builtin_bit_cast(b16x2, a), __builtin_bit_cast(b16x2, b), c, false);
}
#define BAR() asm volatile("s_waitcnt lgkmcnt(0)\n\ts_barrier" ::: "memory")
#define VMWAIT(N) asm volatile("s_waitcnt vmcnt(" #N ")" ::: "memory")
#define SBAR() __builtin_amdgcn_sched_barrier(0)

__device__ __forceinline__ void glds1(const void* g, unsigned dst) {
  unsigned sv;
  asm volatile("s_mov_b32 %0, m0\n\ts_mov_b32 m0, %2\n\ts_nop 0\n\tglobal_load_lds_dwordx4 %1, off\n\ts_mov_b32 m0, %0"
               : "=&s"(sv) : "v"(g), "s"(dst) : "memory");
}
__device__ __forceinline__ void glds4_sc1(const void* g, unsigned dst) {
  unsigned sv;
  asm volatile("s_mov_b32 %0, m0\n\ts_mov_b32 m0, %2\n\ts_nop 0\n\t"
               "global_load_lds_dwordx4 %1, off sc1\n\tglobal_load_lds_dwordx4 %1, off offset:1024 sc1\n\t"
               "global_load_lds_dwordx4 %1, off offset:2048 sc1\n\tglobal_load_lds_dwordx4 %1, off offset:3072 sc1\n\t"
               "s_mov_b32 m0, %0"
               : "=&s"(sv) : "v"(g), "s"(dst) : "memory");
}
__device__ __forceinline__ v2u trd(lptr p) {
  return __builtin_bit_cast(v2u, __builtin_amdgcn_ds_read_tr16_b64_v4i16((LAS s16x4*)p));
}
__device__ __forceinline__ v4u cat(v2u a, v2u b) { return v4u{a.x, a.y, b.x, b.y}; }
__device__ __forceinline__ f32x4 mfma16h(v4u a, v4u b, f32x4 c) {
  return __builtin_amdgcn_mfma_f32_16x16x32_f16(__builtin_bit_cast(f16x8, a), __builtin_bit_cast(f16x8, b), c, 0, 0, 0);
}
__device__ __forceinline__ f32x4 mfma16b(v4u a, v4u b, f32x4 c) {
  return __builtin_amdgcn_mfma_f32_16x16x32_bf16(__builtin_bit_cast(b16x8, a), __builtin_bit_cast(b16x8, b), c, 0, 0, 0);
}
__device__ __forceinline__ f32x16 mfma32b(v4u a, v4u b, f32x16 c) {
  return __builtin_amdgcn_mfma_f32_32x32x16_bf16(__builtin_bit_cast(b16x8, a), __builtin_bit_cast(b16x8, b), c, 0, 0, 0);
}
typedef __amdgpu_buffer_rsrc_t rsrc_t;
__device__ __forceinline__ v4u ld16(rsrc_t rs, int voff, int soff) { return __builtin_amdgcn_raw_buffer_load_b128(rs, voff, soff, SC1); }
__device__ __forceinline__ v2u ld8(rsrc_t rs, int voff, int soff) { return __builtin_amdgcn_raw_buffer_load_b64(rs, voff, soff, SC1); }
__device__ __forceinline__ void st16(v4u v, rsrc_t rs, int voff) { __builtin_amdgcn_raw_buffer_store_b128(v, rs, voff, 0, SC1); }
__device__ __forceinline__ void st8(v2u v, rsrc_t rs, int voff) { __builtin_amdgcn_raw_buffer_store_b64(v, rs, voff, 0, SC1); }
__device__ __forceinline__ void st4(unsigned v, rsrc_t rs, int voff) { __builtin_amdgcn_raw_buffer_store_b32(v, rs, voff, 0, SC1); }

__device__ __forceinline__ void wait_flags(rsrc_t rs, int voff, bool active, v2u tag) {
#pragma unroll 1
  for (int it = 0; it < SPIN_CAP; ++it) {
    asm volatile("" ::: "memory");
    v2u f = ld8(rs, voff, 0);
    bool ok = !active || (f.x == tag.x && f.y == tag.y);
    if (__all(ok)) break;
    __builtin_amdgcn_s_sleep(1);
  }
  asm volatile("" ::: "memory");
}
__device__ __forceinline__ void lds_arrive(lptr L, int c) {
  if ((threadIdx.x & 63) == 0) __hip_atomic_fetch_add((LAS unsigned*)(L + L_CNT) + c, 1u, __ATOMIC_RELAXED, __HIP_MEMORY_SCOPE_WORKGROUP);
}
__device__ __forceinline__ void lds_wait(lptr L, int c, unsigned n) {
#pragma unroll 1
  for (int it = 0; it < (1 << 22); ++it) {
    unsigned v = *(volatile LAS unsigned*)(L + L_CNT + 4 * c);
    if (__builtin_amdgcn_readfirstlane(v) >= n) break;
    __builtin_amdgcn_s_sleep(1);
  }
  asm volatile("" ::: "memory");
}

#define TS(i) do {} while (0)

__global__ void __launch_bounds__(512, 2) fused_kernel(Params p) {
  extern __shared__ __attribute__((aligned(16))) unsigned char smem[];
  lptr L = (lptr)smem;
  const unsigned lds0 = (unsigned)(uintptr_t)L;
  const int tid = threadIdx.x, l = tid & 63, w = __builtin_amdgcn_readfirstlane(tid >> 6);
  const int bid = blockIdx.x, xcd = bid & 7, idx = bid >> 3;
  const int cp = xcd * 32 + idx, cw = w >> 2, rg = w & 3, ch = 2 * cp + cw;
  const int g4 = l >> 4, q4 = (l >> 2) & 3, p4 = l & 3, n16 = l & 15;
  rsrc_t rs = __builtin_amdgcn_make_buffer_rsrc((void*)p.ws, (short)0, WS_BYTES, 0x00020000);
  TS(0);

  v2u ep = ld8(rs, WS_EPOCH, 0);
  const v4u xrow = *(const GAS v4u*)(p.x + (size_t)(8 * bid + w) * DM + 8 * l);
  const unsigned pA = *(const GAS unsigned*)(p.logA + ch * NMODE + 2 * l);
  const unsigned pB = *(const GAS unsigned*)(p.aim + ch * NMODE + 2 * l);
  const unsigned pCr = *(const GAS unsigned*)(p.cre + ch * NMODE + 2 * l);
  const unsigned pCi = *(const GAS unsigned*)(p.cim + ch * NMODE + 2 * l);
  const unsigned pdt = *(const GAS unsigned*)(p.logdt + 2 * cp);
  const unsigned pds = *(const GAS unsigned*)(p.dskip + 2 * cp);
  SBAR();
  const bool retile = bid < 224;
  const int cgp = bid >> 3, kb = bid & 7, rfg = bid >> 1, rdh = bid & 1;
  if (retile) {
    const size_t o1 = (size_t)(64 * kb + 8 * w + (l >> 3)) * FF + 64 * cgp + 8 * (l & 7);
    glds1(p.wg + o1, lds0 + L_RT + w * 1024);
    glds1(p.wu + o1, lds0 + L_RT + 8192 + w * 1024);
    glds1(p.wd + (size_t)(16 * rfg + 2 * w + (l >> 5)) * DM + 256 * rdh + 8 * (l & 31), lds0 + L_RT2 + w * 1024);
  }
  unsigned xs[4];
#pragma unroll
  for (int i = 0; i < 4; ++i) xs[i] = *(const GAS unsigned*)(p.x + (size_t)(4 * tid + i) * DM + 2 * cp);
  SBAR();
  if (tid < 16) *(LAS unsigned*)(L + L_CNT + 4 * tid) = 0u;
  if (tid < 64) *(LAS v4u*)(L + L_ST2 + 16 * tid) = v4u{0u, 0u, 0u, 0u};
  if (tid >= 64 && tid < 124) { const int i = tid - 64; *(LAS v4u*)(L + L_U + (i / 30) * U_CH + (i % 30) * 16) = v4u{0u, 0u, 0u, 0u}; }
  if (tid >= 128 && tid < 144) { const int i = tid - 128; *(LAS v4u*)(L + L_KX + (i >> 1) * KX_CP + 4096 + (i & 1) * 16) = v4u{0u, 0u, 0u, 0u}; }
  if (tid >= 192 && tid < 194) *(LAS v4u*)(L + L_KC + (tid - 192) * KC_CH) = v4u{0u, 0u, 0u, 0u};

  v2u tag;
  {
    u64 e = ((u64)ep.y << 32 | ep.x) + 1ull;
    tag.x = __builtin_amdgcn_readfirstlane((unsigned)e);
    tag.y = __builtin_amdgcn_readfirstlane((unsigned)(e >> 32));
  }
  {
    float v[8];
#pragma unroll
    for (int j = 0; j < 4; ++j) { v[2 * j] = bflo(xrow[j]); v[2 * j + 1] = bfhi(xrow[j]); }
    float s = ((v[0] + v[1]) + (v[2] + v[3])) + ((v[4] + v[5]) + (v[6] + v[7]));
    const float mean = wave_sum(s) * (1.f / DM);
    float q = 0.f;
#pragma unroll
    for (int j = 0; j < 8; ++j) { const float d = v[j] - mean; q += d * d; }
    const float var = rbf(wave_sum(q) * (1.f / DM));
    const float sd = __builtin_sqrtf(rbf(var + 1e-5f));
    if (l == 0) st8(v2u{pkbf(mean, sd), tag.x}, rs, WS_ST1 + (8 * bid + w) * 8);
  }
  TS(1);
  VMWAIT(5);
  BAR();
  if (retile) {
    const int fgl = w & 3, ks2 = w >> 2, fgG = 4 * cgp + fgl;
    lptr a0 = L + L_RT + (g4 & 1) * 8192 + (32 * ks2 + 8 * (g4 >> 1) + q4) * 128 + (16 * fgl + 4 * p4) * 2;
    const v4u f0 = cat(trd(a0), trd(a0 + 512)), f1 = cat(trd(a0 + 2048), trd(a0 + 2560));
    lptr a1 = L + L_RT2 + (4 * (g4 & 1) + q4) * 512 + (16 * (2 * w + (g4 >> 1)) + 4 * p4) * 2;
    const v4u f2 = cat(trd(a1), trd(a1 + 4096));
    const int o = WS_W1F + (fgG * 32 + 4 * kb + 2 * ks2) * 1024 + 16 * l;
    st16(f0, rs, o);
    st16(f1, rs, o + 1024);
    const int dtg = 16 * rdh + 2 * w + (g4 >> 1);
    st16(f2, rs, WS_WDF + (((dtg >> 2) * 56 + (rfg >> 1)) * 4 + (dtg & 3)) * 1024 + ((rfg & 1) * 32 + (l & 31)) * 16);
    float cs = 0.f;
#pragma unroll
    for (int j = 0; j < 4; ++j) { cs = dot2acc(f0[j], 0x3f803f80u, cs); cs = dot2acc(f1[j], 0x3f803f80u, cs); }
    cs = x32_add(cs);
    if (l < 32) st4(asu(cs), rs, WS_CS + ((fgG * 16 + 2 * kb + ks2) * 32 + l) * 4);
  }
  TS(2);
  {
    const float dt = rbf(expf(cw ? bfhi(pdt) : bflo(pdt)));
    const float LOG2E = 1.4426950408889634f, INV2PI = 0.15915494309189535f;
    f32x4 t;
    t.x = -rbf(expf(bflo(pA))) * dt * LOG2E; t.y = bflo(pB) * dt * INV2PI;
    t.z = -rbf(expf(bfhi(pA))) * dt * LOG2E; t.w = bfhi(pB) * dt * INV2PI;
    *(LAS f32x4*)(L + L_PT + w * 1024 + 16 * l) = t;
    lptr pt = L + L_PT + w * 1024 + g4 * 32;
    v4u xf[8];
    if (rg < 3) {
      const int i = n16;
      int base = 1024 + 128 * i;
      if (rg == 0) base = 128 * (i >> 3) + (i & 7);
      if (rg == 1) base = i < 8 ? 256 + 128 * (i >> 2) + 2 * (i & 3) : 512 + 128 * ((i - 8) >> 1) + 4 * (i & 1);
      const float fb = (float)base;
#pragma unroll
      for (int ks = 0; ks < 8; ++ks) {
        const f32x4 z0 = *(LAS f32x4*)(pt + ks * 128), z1 = *(LAS f32x4*)(pt + ks * 128 + 16);
        const float a2[4] = {z0.x, z0.z, z1.x, z1.z}, bt[4] = {z0.y, z0.w, z1.y, z1.w};
#pragma unroll
        for (int m = 0; m < 4; ++m) {
          const float e = __builtin_amdgcn_exp2f(a2[m] * fb);
          const float fr = __builtin_amdgcn_fractf(bt[m] * fb);
          xf[ks][m] = pkh(e * __builtin_amdgcn_cosf(fr), e * __builtin_amdgcn_sinf(fr));
        }
      }
    } else {
      const float c0r = bflo(pCr) * dt, c0i = bflo(pCi) * dt, c1r = bfhi(pCr) * dt, c1i = bfhi(pCi) * dt;
      const float mx = wave_max(fmaxf(fabsf(c0r) + fabsf(c0i), fabsf(c1r) + fabsf(c1i)));
      int es = 258 - (int)(asu(mx) >> 23);
      es = es > 220 ? 220 : (es < 34 ? 34 : es);
      const float S2 = asf((unsigned)(es + 1) << 23);
      if (l == 0) *(LAS unsigned*)(L + L_CNT + 32 + 4 * cw) = (unsigned)(254 - es) << 23;
      *(LAS f32x4*)(L + L_CT + cw * 1024 + 16 * l) = f32x4{c0r * S2, c0i * S2, c1r * S2, c1i * S2};
      lptr ct = L + L_CT + cw * 1024 + g4 * 32;
      const float fb = (float)(8 * n16);
#pragma unroll 2
      for (int ks = 0; ks < 8; ++ks) {
        const f32x4 z0 = *(LAS f32x4*)(pt + ks * 128), z1 = *(LAS f32x4*)(pt + ks * 128 + 16);
        const f32x4 c0 = *(LAS f32x4*)(ct + ks * 128), c1 = *(LAS f32x4*)(ct + ks * 128 + 16);
        const float a2[4] = {z0.x, z0.z, z1.x, z1.z}, bt[4] = {z0.y, z0.w, z1.y, z1.w};
        const float cr[4] = {c0.x, c0.z, c1.x, c1.z}, ci[4] = {c0.y, c0.w, c1.y, c1.w};
        v4u y;
#pragma unroll
        for (int m = 0; m < 4; ++m) {
          const float e = __builtin_amdgcn_exp2f(a2[m] * fb);
          const float fr = __builtin_amdgcn_fractf(bt[m] * fb);
          const float er = e * __builtin_amdgcn_cosf(fr), ei = e * __builtin_amdgcn_sinf(fr);
          y[m] = pkh(cr[m] * er - ci[m] * ei, -(cr[m] * ei + ci[m] * er));
        }
        *(LAS v4u*)(L + L_YF + cw * 8192 + ks * 1024 + 16 * l) = y;
      }
    }
    TS(3);
    BAR();
    if (rg < 3) {
      f32x4 acc = {0.f, 0.f, 0.f, 0.f};
#pragma unroll
      for (int ks = 0; ks < 8; ++ks) acc = mfma16h(xf[ks], *(LAS v4u*)(L + L_YF + cw * 8192 + ks * 1024 + 16 * l), acc);
#pragma unroll
      for (int r = 0; r < 4; ++r) {
        const int i = 4 * g4 + r;
        int ci = 512 + 16 * i + n16;
        if (rg == 0) ci = 128 * (i >> 3) + (i & 7) + 8 * n16;
        if (rg == 1) ci = i < 8 ? 256 + 64 * (i >> 2) + (i & 3) + 4 * n16 : 384 + 32 * ((i - 8) >> 1) + (i & 1) + 2 * n16;
        if (ci <= 640) *(LAS _Float16*)(L + L_KC + cw * KC_CH + 16 + 2 * ci) = (_Float16)acc[r];
      }
    }
    BAR();
    {
      const int c2 = tid >> 8, chi = tid & 255;
      lptr kc = L + L_KC + c2 * KC_CH + 16;
      unsigned e[12];
#pragma unroll
      for (int j = 0; j < 11; ++j) {
        const int li = 2047 - 8 * chi - j;
        const int cf = (int)(pkbf((float)li, 0.f) & 0xffffu) - 17024;
        const int ci = li < 128 ? li : cf;
        e[j] = *(LAS u16*)(kc + 2 * ci);
      }
      e[11] = 0;
      unsigned P[6], Q[5];
#pragma unroll
      for (int j = 0; j < 6; ++j) P[j] = e[2 * j] | (e[2 * j + 1] << 16);
#pragma unroll
      for (int j = 0; j < 5; ++j) Q[j] = __builtin_amdgcn_alignbit(P[j + 1], P[j], 16);
      lptr kx = L + L_KX + c2 * KX_CH + 16 * chi;
      *(LAS v4u*)(kx) = v4u{P[0], P[1], P[2], P[3]};
      *(LAS v4u*)(kx + KX_CP) = v4u{Q[0], Q[1], Q[2], Q[3]};
      *(LAS v4u*)(kx + 2 * KX_CP) = v4u{P[1], P[2], P[3], P[4]};
      *(LAS v4u*)(kx + 3 * KX_CP) = v4u{Q[1], Q[2], Q[3], Q[4]};
    }
  }
  TS(4);
  VMWAIT(0);
  if (retile && l < 2) {
    const int o = l == 0 ? WS_F2 + ((4 * cgp + (w & 3)) * 16 + 2 * kb + (w >> 2)) * 8 : WS_F3 + (((4 * rdh + (w >> 1)) * 112 + rfg) * 2 + (w & 1)) * 8;
    st8(tag, rs, o);
  }
  {
    v4u s0, s1;
#pragma unroll 1
  for (int it = 0; it < SPIN_CAP; ++it) {
      asm volatile("" ::: "memory");
      s0 = ld16(rs, WS_ST1 + 32 * tid, 0);
      s1 = ld16(rs, WS_ST1 + 32 * tid + 16, 0);
      const bool ok = s0.y == tag.x && s0.w == tag.x && s1.y == tag.x && s1.w == tag.x;
      if (__all(ok)) break;
      __builtin_amdgcn_s_sleep(1);
    }
    const unsigned st[4] = {s0.x, s0.z, s1.x, s1.z};
    const float d0 = bflo(pds), d1 = bfhi(pds);
    float u0[4], u1[4];
#pragma unroll
    for (int i = 0; i < 4; ++i) {
      const float m = bflo(st[i]), rsd = __builtin_amdgcn_rcpf(bfhi(st[i]));
      const unsigned c = pkbf(bflo(xs[i]) - m, bfhi(xs[i]) - m);
      const unsigned u = pkbf(bflo(c) * rsd, bfhi(c) * rsd);
      u0[i] = bflo(u); u1[i] = bfhi(u);
    }
    *(LAS v2u*)(L + L_U + 480 + 8 * tid) = v2u{pkh(u0[0], u0[1]), pkh(u0[2], u0[3])};
    *(LAS v2u*)(L + L_U + U_CH + 480 + 8 * tid) = v2u{pkh(u1[0], u1[1]), pkh(u1[2], u1[3])};
    *(LAS v2u*)(L + L_UD + 8 * tid) = v2u{pkbf(u0[0] * d0, u0[1] * d0), pkbf(u0[2] * d0, u0[3] * d0)};
    *(LAS v2u*)(L + L_UD + 4096 + 8 * tid) = v2u{pkbf(u1[0] * d1, u1[1] * d1), pkbf(u1[2] * d1, u1[3] * d1)};
    *(LAS v2u*)(L + L_XB + 8 * tid) = v2u{(xs[0] & 0xffffu) | (xs[1] << 16), (xs[2] & 0xffffu) | (xs[3] << 16)};
    *(LAS v2u*)(L + L_XB + 4096 + 8 * tid) = v2u{(xs[0] >> 16) | (xs[1] & 0xffff0000u), (xs[2] >> 16) | (xs[3] & 0xffff0000u)};
  }
  TS(5);
  const int rb = (xcd >> 1) * 4 + (idx >> 3), cb = (xcd & 1) * 8 + (idx & 7);
  const int fgW = 7 * cb + (w < 7 ? w : 0);
  v4u wa[32];
  v4u csp0, csp1;
  if (w < 7) {
    wait_flags(rs, WS_F2 + (fgW * 16 + n16) * 8, true, tag);
#pragma unroll
    for (int ks = 0; ks < 16; ++ks) wa[ks] = ld16(rs, 16 * l, WS_W1F + (fgW * 32 + ks) * 1024);
  }
  SBAR();
  TS(6);
  BAR();
  if (bid == 0 && tid == 0) st8(tag, rs, WS_EPOCH);
  lptr kA = L + L_KX + cw * KX_CH + (3 - (n16 & 3)) * KX_CP + 2 * (12 - 4 * (n16 >> 2) + 8 * g4) + 64 * (6 - 2 * rg);
  lptr uB = L + L_U + cw * U_CH + 32 * n16 + 16 * g4 + 384 - 128 * rg;
#define AFRAG(a, rr) cat(*(LAS v2u*)(kA + 64 * (57 - 8 * (a) - (rr))), *(LAS v2u*)(kA + 64 * (57 - 8 * (a) - (rr)) + 8))
#define BFRAG(b, rr) (*(LAS v4u*)(uB + 512 * (b) + 64 * (1 - (rr))))
  const float invS = *(LAS float*)(L + L_CNT + 32 + 4 * cw);
  auto conv_finish = [&](f32x4 (&acc)[4], int q0) {
    lptr red = L + L_RED + cw * 12288 + 16 * l;
    f32x4 own;
#pragma unroll
    for (int t = 0; t < 4; ++t) {
      if (t != rg) *(LAS f32x4*)(red + t * 3072 + ((rg - t - 1) & 3) * 1024) = acc[t];
      else own = acc[t];
    }
    BAR();
    lptr rr = red + rg * 3072;
    own += *(LAS f32x4*)(rr) + *(LAS f32x4*)(rr + 1024) + *(LAS f32x4*)(rr + 2048);
    const int q = q0 + rg, t0 = 256 * q + 16 * n16 + 4 * g4;
    const v2u xb = *(LAS v2u*)(L + L_XB + cw * 4096 + 2 * t0), ud = *(LAS v2u*)(L + L_UD + cw * 4096 + 2 * t0);
    const unsigned y0 = pkbf(own.x * invS, own.y * invS), y1 = pkbf(own.z * invS, own.w * invS);
    const unsigned s0 = pkbf(bflo(y0) + bflo(ud.x), bfhi(y0) + bfhi(ud.x)), s1 = pkbf(bflo(y1) + bflo(ud.y), bfhi(y1) + bfhi(ud.y));
    const v2u h = {pkbf(bflo(xb.x) + bflo(s0), bfhi(xb.x) + bfhi(s0)), pkbf(bflo(xb.y) + bflo(s1), bfhi(xb.y) + bfhi(s1))};
    st8(h, rs, WS_HT + (t0 >> 5) * 32768 + ch * 64 + (t0 & 31) * 2);
  };
  {
    f32x4 acc[4];
#pragma unroll
    for (int t = 0; t < 4; ++t) acc[t] = f32x4{0.f, 0.f, 0.f, 0.f};
#pragma unroll
    for (int rr = 0; rr < 2; ++rr) {
      v4u bf[4];
#pragma unroll
      for (int b = 0; b < 4; ++b) bf[b] = BFRAG(b, rr);
#pragma unroll
      for (int a = 0; a < 4; ++a) {
        const v4u af = AFRAG(a, rr);
#pragma unroll
        for (int b = 0; b + a < 4; ++b) acc[a + b] = mfma16h(af, bf[b], acc[a + b]);
      }
    }
    conv_finish(acc, 0);
  }
  TS(7);
  {
    f32x4 acc[4];
#pragma unroll
    for (int t = 0; t < 4; ++t) acc[t] = f32x4{0.f, 0.f, 0.f, 0.f};
#pragma unroll
    for (int rr = 0; rr < 2; ++rr) {
      v4u bf[8];
#pragma unroll
      for (int b = 0; b < 8; ++b) bf[b] = BFRAG(b, rr);
#pragma unroll
      for (int a = 0; a < 8; ++a) {
        const v4u af = AFRAG(a, rr);
#pragma unroll
        for (int b = 0; b < 8; ++b)
          if (a + b >= 4 && a + b < 8) acc[a + b - 4] = mfma16h(af, bf[b], acc[a + b - 4]);
      }
      if (rr == 0) {
        SBAR();
        VMWAIT(0);
        if (l < R4) st8(tag, rs, WS_F4 + l * 32768 + (rg * 512 + ch) * 8);
        BAR();
      }
    }
    TS(8);
    conv_finish(acc, 4);
  }
  TS(9);
  const int qh = rb >> 3;
  const unsigned char* htg = p.ws + WS_HT + (size_t)rb * 32768 + w * 4096 + 16 * l;
  const int f4o = WS_F4 + xcd * 32768 + (qh * 512 + 64 * w + l) * 8;
  wait_flags(rs, f4o, true, tag);
  glds4_sc1(htg, lds0 + L_B0 + w * 4096);
  if (l < R4) st8(tag, rs, WS_F4 + l * 32768 + ((4 + rg) * 512 + ch) * 8);
  if (w < 7) {
#pragma unroll
    for (int ks = 16; ks < 32; ++ks) wa[ks] = ld16(rs, 16 * l, WS_W1F + (fgW * 32 + ks) * 1024);
    csp0 = v4u{0, 0, 0, 0};
#pragma unroll
    for (int i = 0; i < 4; ++i) {
      csp0[i] = __builtin_amdgcn_raw_buffer_load_b32(rs, 4 * l, WS_CS + fgW * 2048 + i * 256, SC1);
      csp1[i] = __builtin_amdgcn_raw_buffer_load_b32(rs, 4 * l, WS_CS + fgW * 2048 + 1024 + i * 256, SC1);
    }
    SBAR();
    VMWAIT(25);
  } else {
    VMWAIT(1);
  }
  asm volatile("s_waitcnt lgkmcnt(0)" ::: "memory");
  lds_arrive(L, 0);
  TS(10);
  const int trb = (8 * (g4 >> 1) + q4) * 64 + 32 * (g4 & 1) + 8 * p4;
  const int bslot[4] = {L_B0, L_B1, L_B2, L_B3};
  if (w < 7) {
    float csr[16];
#pragma unroll
    for (int s = 0; s < 4; ++s) {
      lds_wait(L, s, 8);
      if (s == 0) TS(11);
      lptr bt = L + bslot[s] + trb;
      f32x16 acc;
#pragma unroll
      for (int r = 0; r < 16; ++r) acc[r] = 0.f;
#pragma unroll
      for (int ks = 0; ks < 32; ++ks) acc = mfma32b(wa[ks], cat(trd(bt + ks * 1024), trd(bt + ks * 1024 + 256)), acc);
      SBAR();
      if (s > 0) {
        VMWAIT(0);
        if (l == 0) st8(tag, rs, WS_F6 + ((16 * (s - 1) + rb) * 128 + fgW) * 8);
      }
      if (s < 3) {
        wait_flags(rs, f4o + (s + 1) * 8192, true, tag);
        glds4_sc1(htg + (size_t)(s + 1) * 16 * 32768, lds0 + bslot[s + 1] + w * 4096);
      }
      if (s == 0) {
        float c = 0.f;
#pragma unroll
        for (int i = 0; i < 4; ++i) c += asf(csp0[i]) + asf(csp1[i]);
        c = x32_add(c);
        if (l < 32) *(LAS float*)(L + L_CSW + w * 128 + 4 * l) = c;
#pragma unroll
        for (int i = 0; i < 4; ++i) {
          const f32x4 v = *(LAS f32x4*)(L + L_CSW + w * 128 + 32 * i + 16 * (l >> 5));
          csr[4 * i] = v.x; csr[4 * i + 1] = v.y; csr[4 * i + 2] = v.z; csr[4 * i + 3] = v.w;
        }
      }
      f32x2 st;
#pragma unroll 1
  for (int it = 0; it < (1 << 22); ++it) {
        st = *(volatile LAS f32x2*)(L + L_ST2 + s * 256 + 8 * (l & 31));
        if (__all(st.y != 0.f)) break;
        __builtin_amdgcn_s_sleep(1);
      }
      const float nm = -st.x, rstd = st.y;
      v4u hv;
#pragma unroll
      for (int j = 0; j < 4; ++j) {
        const unsigned gp = pkbf(__builtin_fmaf(nm, csr[2 * j], acc[2 * j]) * rstd, __builtin_fmaf(nm, csr[2 * j + 1], acc[2 * j + 1]) * rstd);
        const unsigned up = pkbf(__builtin_fmaf(nm, csr[8 + 2 * j], acc[8 + 2 * j]) * rstd, __builtin_fmaf(nm, csr[9 + 2 * j], acc[9 + 2 * j]) * rstd);
        const float g0 = bflo(gp), g1 = bfhi(gp);
        const float C1 = -2.f * 0.7978845608028654f * 1.4426950408889634f, C2 = C1 * 0.044715f;
        const float e0 = __builtin_amdgcn_exp2f(g0 * __builtin_fmaf(g0 * g0, C2, C1));
        const float e1 = __builtin_amdgcn_exp2f(g1 * __builtin_fmaf(g1 * g1, C2, C1));
        const unsigned ge = pkbf(g0 * __builtin_amdgcn_rcpf(1.f + e0), g1 * __builtin_amdgcn_rcpf(1.f + e1));
        hv[j] = pkbf(bflo(ge) * bflo(up), bfhi(ge) * bfhi(up));
      }
      if (s < 3) { VMWAIT(0); lds_arrive(L, s + 1); }
      st16(hv, rs, WS_HF + ((16 * s + rb) * 112 + fgW) * 1024 + 16 * l);
      TS(12 + s);
    }
    VMWAIT(0);
    if (l == 0) st8(tag, rs, WS_F6 + ((48 + rb) * 128 + fgW) * 8);
  } else {
#pragma unroll 1
    for (int s = 0; s < 4; ++s) {
      lds_wait(L, s, 8);
      if (s < 3) {
        wait_flags(rs, f4o + (s + 1) * 8192, true, tag);
        glds4_sc1(htg + (size_t)(s + 1) * 16 * 32768, lds0 + (s + 1) * 32768 + w * 4096);
      }
      lptr bt = L + s * 32768 + trb;
      float s1 = 0.f, s2 = 0.f, s1b = 0.f, s2b = 0.f;
#pragma unroll 8
      for (int ks = 0; ks < 32; ++ks) {
        const v2u a = trd(bt + ks * 1024), b = trd(bt + ks * 1024 + 256);
        s1 = dot2acc(a.x, 0x3f803f80u, s1); s2 = dot2acc(a.x, a.x, s2);
        s1b = dot2acc(a.y, 0x3f803f80u, s1b); s2b = dot2acc(a.y, a.y, s2b);
        s1 = dot2acc(b.x, 0x3f803f80u, s1); s2 = dot2acc(b.x, b.x, s2);
        s1b = dot2acc(b.y, 0x3f803f80u, s1b); s2b = dot2acc(b.y, b.y, s2b);
      }
      const float mean = x32_add(s1 + s1b) * (1.f / DM);
      const float var = rbf(fmaxf(x32_add(s2 + s2b) * (1.f / DM) - mean * mean, 0.f));
      const float sd = rbf(__builtin_sqrtf(rbf(var + 1e-5f)));
      if (l < 32) *(LAS f32x2*)(L + L_ST2 + s * 256 + 8 * l) = f32x2{rbf(mean), 1.f / sd};
      if (s < 3) { VMWAIT(0); lds_arrive(L, s + 1); }
      TS(12 + s);
    }
  }
  TS(16);
  {
    const int g2r = xcd * 4 + (idx >> 3), db = idx & 7, tbo = g2r >> 1, th = g2r & 1;
    wait_flags(rs, WS_F3 + ((db * 112 + 14 * w) * 2 + (l < 28 ? l : 0)) * 8, true, tag);
    v4u wd[7][4];
#pragma unroll
    for (int ks = 0; ks < 7; ++ks)
#pragma unroll
      for (int dt = 0; dt < 4; ++dt) wd[ks][dt] = ld16(rs, 16 * l, WS_WDF + ((db * 56 + 7 * w + ks) * 4 + dt) * 1024);
    SBAR();
    const int hvo = (g4 >> 1) * 1024 + ((g4 & 1) * 32 + th * 16 + n16) * 16;
    const int fw = w & 3;
    TS(17);
#pragma unroll
    for (int s = 0; s < 4; ++s) {
      const int tb = 16 * s + tbo;
      wait_flags(rs, WS_F6 + (tb * 128 + 14 * w + (n16 < 14 ? n16 : 0)) * 8, true, tag);
      v4u hb[7];
#pragma unroll
      for (int ks = 0; ks < 7; ++ks) hb[ks] = ld16(rs, hvo, WS_HF + (tb * 112 + 14 * w + 2 * ks) * 1024);
      const bool fin = (w >> 2) == (s & 1);
      v2u hres = ld8(rs, WS_HT + tb * 32768 + (64 * db + 16 * fw + (l >> 2)) * 64 + (th * 16 + 4 * p4) * 2, 0);
      SBAR();
      f32x4 acc[4];
#pragma unroll
      for (int dt = 0; dt < 4; ++dt) acc[dt] = f32x4{0.f, 0.f, 0.f, 0.f};
#pragma unroll
      for (int ks = 0; ks < 7; ++ks)
#pragma unroll
        for (int dt = 0; dt < 4; ++dt) acc[dt] = mfma16b(wd[ks][dt], hb[ks], acc[dt]);
      if (s == 0) BAR();
      lptr red = L + L_RED2 + (s & 1) * 32768 + 16 * l;
#pragma unroll
      for (int dt = 0; dt < 4; ++dt) *(LAS f32x4*)(red + w * 4096 + dt * 1024) = acc[dt];
      *(LAS v2u*)(L + L_HX + w * 512 + 8 * l) = hres;
      BAR();
      const v2u ht = trd(L + L_HX + w * 512 + 8 * l);
      if (fin) {
        f32x4 sum = *(LAS f32x4*)(red + fw * 1024);
#pragma unroll
        for (int v = 1; v < 8; ++v) sum += *(LAS f32x4*)(red + v * 4096 + fw * 1024);
        const unsigned a0 = pkbf(sum.x, sum.y), a1 = pkbf(sum.z, sum.w);
        const v2u o = {pkbf(bflo(ht.x) + bflo(a0), bfhi(ht.x) + bfhi(a0)), pkbf(bflo(ht.y) + bflo(a1), bfhi(ht.y) + bfhi(a1))};
        *(GAS v2u*)(p.out + (size_t)(512 * s + 16 * g2r + n16) * DM + 64 * db + 16 * fw + 4 * g4) = o;
      }
      TS(18 + s);
    }
  }
}

extern "C" void kernel_launch(void* const* d_in, const int* in_sizes, int n_in, void* d_out, int out_size, void* d_ws,
                              size_t ws_size, hipStream_t stream) {
  Params p;
  p.x = (const u16*)d_in[0]; p.logA = (const u16*)d_in[1]; p.aim = (const u16*)d_in[2]; p.cre = (const u16*)d_in[3];
  p.cim = (const u16*)d_in[4]; p.logdt = (const u16*)d_in[5]; p.dskip = (const u16*)d_in[6];
  p.wg = (const u16*)d_in[8]; p.wu = (const u16*)d_in[9]; p.wd = (const u16*)d_in[10];
  p.out = (u16*)d_out; p.ws = (unsigned char*)d_ws;
  (void)hipFuncSetAttribute((const void*)fused_kernel, hipFuncAttributeMaxDynamicSharedMemorySize, L_BYTES);
  fused_kernel<<<dim3(256), dim3(512), L_BYTES, stream>>>(p);
}
```

```cpp
#include <hip/hip_runtime.h>
#include <stdint.h>

typedef unsigned short u16;
typedef unsigned long long u64;
typedef unsigned v4u __attribute__((ext_vector_type(4)));
typedef unsigned v2u __attribute__((ext_vector_type(2)));
typedef float f32x2 __attribute__((ext_vector_type(2)));
typedef float f32x4 __attribute__((ext_vector_type(4)));
typedef float f32x16 __attribute__((ext_vector_type(16)));
typedef short s16x4 __attribute__((ext_vector_type(4)));
typedef _Float16 f16x8 __attribute__((ext_vector_type(8)));
typedef _Float16 f16x2 __attribute__((ext_vector_type(2)));
typedef __bf16 b16x8 __attribute__((ext_vector_type(8)));
typedef __bf16 b16x2 __attribute__((ext_vector_type(2)));
#define LAS __attribute__((address_space(3)))
#define GAS __attribute__((address_space(1)))
typedef LAS unsigned char* lptr;

#define STAMP 0

constexpr int SEQ = 2048, DM = 512, NMODE = 128, FF = 1792;
constexpr int SC1 = 16;
constexpr int SPIN_CAP = 20000;

constexpr int WS_EPOCH = 0;
constexpr int WS_ST1 = 4096;
constexpr int WS_F2 = 32768;
constexpr int WS_F3 = 40960;
constexpr int WS_F4 = 65536;
constexpr int WS_F6 = 131072;
constexpr int WS_HT = 1 << 20;
constexpr int WS_W1F = 3 << 20;
constexpr int WS_WDF = 7 << 20;
constexpr int WS_HF = 10 << 20;
constexpr int WS_XT = 17 << 20;
constexpr int XT_CP = 16384 + 256;
constexpr int WS_DBG = 22 << 20;
constexpr int WS_BYTES = 24 << 20;
static_assert(WS_XT + 256 * XT_CP <= WS_DBG, "ws map");

constexpr int L_T = 0, T_ROW = 528, T_CH = 64 * T_ROW;
constexpr int L_RED = 65536;
constexpr int L_RT = L_RED + 4096, L_DUMP = L_RED + 28672, L_KC = L_RED + 36864, KC_CH = 1312;
constexpr int L_KX = L_RED + 49152, KX_CP = 4160, KX_CH = 4 * KX_CP;
constexpr int L_U = L_KX + 2 * KX_CH, U_CH = 4576;
constexpr int L_CNT = L_U + 2 * U_CH;
constexpr int L_ST2 = L_CNT + 64;
constexpr int L_CSW = L_ST2 + 1024;
constexpr int L_STAMP = L_CSW + 1024;
constexpr int L_BYTES = L_STAMP + 1024;
constexpr int L_RED2 = 0;
constexpr int L_HX = 131072;
static_assert(L_BYTES <= 163840, "lds");
static_assert(2 * T_CH <= L_RT && L_KC + 2 * KC_CH <= L_KX, "lds");
static_assert(L_U % 16 == 0 && L_KC % 16 == 0 && L_CNT % 16 == 0, "align");

struct Params {
  const u16 *x, *logA, *aim, *cre, *cim, *logdt, *dskip, *wg, *wu, *wd;
  u16* out;
  unsigned char* ws;
};

__device__ __forceinline__ float asf(unsigned v) { return __builtin_bit_cast(float, v); }
__device__ __forceinline__ unsigned asu(float v) { return __builtin_bit_cast(unsigned, v); }
__device__ __forceinline__ float bflo(unsigned v) { return asf(v << 16); }
__device__ __forceinline__ float bfhi(unsigned v) { return asf(v & 0xffff0000u); }
__device__ __forceinline__ unsigned pkbf(float lo, float hi) {
  b16x2 r = {(__bf16)lo, (__bf16)hi};
  return __builtin_bit_cast(unsigned, r);
}
__device__ __forceinline__ float rbf(float f) { return bflo(pkbf(f, f)); }
__device__ __forceinline__ unsigned pkh(float lo, float hi) {
  f16x2 r = {(_Float16)lo, (_Float16)hi};
  return __builtin_bit_cast(unsigned, r);
}
template <int C> __device__ __forceinline__ unsigned dppu(unsigned v) {
  return (unsigned)__builtin_amdgcn_update_dpp(0, (int)v, C, 0xf, 0xf, true);
}
template <int C> __device__ __forceinline__ float dppf(float v) { return asf(dppu<C>(asu(v))); }
__device__ __forceinline__ float x16_add(float v) {
  auto s = __builtin_amdgcn_permlane16_swap(asu(v), asu(v), false, false);
  return asf(s[0]) + asf(s[1]);
}
__device__ __forceinline__ float x32_add(float v) {
  auto s = __builtin_amdgcn_permlane32_swap(asu(v), asu(v), false, false);
  return asf(s[0]) + asf(s[1]);
}
__device__ __forceinline__ float wave_sum(float v) {
  v += dppf<0xB1>(v); v += dppf<0x4E>(v); v += dppf<0x141>(v); v += dppf<0x140>(v);
  return x32_add(x16_add(v));
}
__device__ __forceinline__ float wave_max(float v) {
  v = fmaxf(v, dppf<0xB1>(v)); v = fmaxf(v, dppf<0x4E>(v)); v = fmaxf(v, dppf<0x141>(v)); v = fmaxf(v, dppf<0x140>(v));
  auto s = __builtin_amdgcn_permlane16_swap(asu(v), asu(v), false, false);
  v = fmaxf(asf(s[0]), asf(s[1]));
  auto t = __builtin_amdgcn_permlane32_swap(asu(v), asu(v), false, false);
  return fmaxf(asf(t[0]), asf(t[1]));
}
__device__ __forceinline__ float dot2acc(unsigned a, unsigned b, float c) {
  return __builtin_amdgcn_fdot2_f32_bf16(__builtin_bit_cast(b16x2, a), __builtin_bit_cast(b16x2, b), c, false);
}
#define BAR() asm volatile("s_waitcnt lgkmcnt(0)\n\ts_barrier" ::: "memory")
#define VMWAIT(N) asm volatile("s_waitcnt vmcnt(" #N ")" ::: "memory")
#define SBAR() __builtin_amdgcn_sched_barrier(0)
#define FENCE() do { asm volatile("" ::: "memory"); __builtin_amdgcn_sched_barrier(0); } while (0)

__device__ __forceinline__ void glds1(const void* g, unsigned dst) {
  unsigned sv;
  asm volatile("s_mov_b32 %0, m0\n\ts_mov_b32 m0, %2\n\ts_nop 0\n\tglobal_load_lds_dwordx4 %1, off\n\ts_mov_b32 m0, %0"
               : "=&s"(sv) : "v"(g), "s"(dst) : "memory");
}
__device__ __forceinline__ void glds4_sc1(const void* g, unsigned dst) {
  unsigned sv;
  asm volatile("s_mov_b32 %0, m0\n\ts_mov_b32 m0, %2\n\ts_nop 0\n\t"
               "global_load_lds_dwordx4 %1, off sc1\n\tglobal_load_lds_dwordx4 %1, off offset:1024 sc1\n\t"
               "global_load_lds_dwordx4 %1, off offset:2048 sc1\n\tglobal_load_lds_dwordx4 %1, off offset:3072 sc1\n\t"
               "s_mov_b32 m0, %0"
               : "=&s"(sv) : "v"(g), "s"(dst) : "memory");
}
__device__ __forceinline__ v2u trd(lptr p) {
  return __builtin_bit_cast(v2u, __builtin_amdgcn_ds_read_tr16_b64_v4i16((LAS s16x4*)p));
}
__device__ __forceinline__ v4u cat(v2u a, v2u b) { return v4u{a.x, a.y, b.x, b.y}; }
__device__ __forceinline__ f32x4 mfma16h(v4u a, v4u b, f32x4 c) {
  return __builtin_amdgcn_mfma_f32_16x16x32_f16(__builtin_bit_cast(f16x8, a), __builtin_bit_cast(f16x8, b), c, 0, 0, 0);
}
__device__ __forceinline__ f32x4 mfma16b(v4u a, v4u b, f32x4 c) {
  return __builtin_amdgcn_mfma_f32_16x16x32_bf16(__builtin_bit_cast(b16x8, a), __builtin_bit_cast(b16x8, b), c, 0, 0, 0);
}
__device__ __forceinline__ f32x16 mfma32b(v4u a, v4u b, f32x16 c) {
  return __builtin_amdgcn_mfma_f32_32x32x16_bf16(__builtin_bit_cast(b16x8, a), __builtin_bit_cast(b16x8, b), c, 0, 0, 0);
}
typedef __amdgpu_buffer_rsrc_t rsrc_t;
__device__ __forceinline__ v4u ld16(rsrc_t rs, int voff, int soff) { return __builtin_amdgcn_raw_buffer_load_b128(rs, voff, soff, SC1); }
__device__ __forceinline__ v2u ld8(rsrc_t rs, int voff, int soff) { return __builtin_amdgcn_raw_buffer_load_b64(rs, voff, soff, SC1); }
__device__ __forceinline__ unsigned ld4(rsrc_t rs, int voff, int soff) { return __builtin_amdgcn_raw_buffer_load_b32(rs, voff, soff, SC1); }
__device__ __forceinline__ void st16(v4u v, rsrc_t rs, int voff) { __builtin_amdgcn_raw_buffer_store_b128(v, rs, voff, 0, SC1); }
__device__ __forceinline__ void st8(v2u v, rsrc_t rs, int voff) { __builtin_amdgcn_raw_buffer_store_b64(v, rs, voff, 0, SC1); }
__device__ __forceinline__ void st4(unsigned v, rsrc_t rs, int voff) { __builtin_amdgcn_raw_buffer_store_b32(v, rs, voff, 0, SC1); }

__device__ __forceinline__ bool ok8(v2u f, unsigned tag) { return f.x == tag && f.y == tag; }
__device__ __forceinline__ bool ok16(v4u f, unsigned tag) { return f.x == tag && f.y == tag && f.z == tag && f.w == tag; }
__device__ __forceinline__ void wait8(rsrc_t rs, int voff, v2u f, unsigned tag) {
#pragma unroll 1
  for (int it = 0; it < SPIN_CAP; ++it) {
    if (__all(ok8(f, tag))) break;
    asm volatile("" ::: "memory");
    f = ld8(rs, voff, 0);
  }
  asm volatile("" ::: "memory");
}
__device__ __forceinline__ void lds_arrive(lptr L, int c) {
  if ((threadIdx.x & 63) == 0) __hip_atomic_fetch_add((LAS unsigned*)(L + L_CNT) + c, 1u, __ATOMIC_RELAXED, __HIP_MEMORY_SCOPE_WORKGROUP);
}
__device__ __forceinline__ void lds_wait(lptr L, int c, unsigned n) {
#pragma unroll 1
  for (int it = 0; it < (1 << 22); ++it) {
    unsigned v = *(volatile LAS unsigned*)(L + L_CNT + 4 * c);
    if (__builtin_amdgcn_readfirstlane(v) >= n) break;
    __builtin_amdgcn_s_sleep(1);
  }
  asm volatile("" ::: "memory");
}

struct C2 { f32x2 r, i; };
__device__ __forceinline__ C2 cmul(C2 a, C2 b) { return C2{a.r * b.r - a.i * b.i, a.r * b.i + a.i * b.r}; }
__device__ __forceinline__ C2 cexp2(f32x2 a2, f32x2 bt, float p) {
  const float e0 = __builtin_amdgcn_exp2f(a2.x * p), e1 = __builtin_amdgcn_exp2f(a2.y * p);
  const float f0 = __builtin_amdgcn_fractf(bt.x * p), f1 = __builtin_amdgcn_fractf(bt.y * p);
  return C2{f32x2{e0 * __builtin_amdgcn_cosf(f0), e1 * __builtin_amdgcn_cosf(f1)},
            f32x2{e0 * __builtin_amdgcn_sinf(f0), e1 * __builtin_amdgcn_sinf(f1)}};
}
__device__ __forceinline__ void put(lptr t, int row, C2 v) {
  *(LAS v2u*)(t + row * T_ROW) = v2u{pkh(v.r.x, v.i.x), pkh(v.r.y, v.i.y)};
}

#define TS(i) do {} while (0)

__global__ void __launch_bounds__(512, 2) fused_kernel(Params p) {
  extern __shared__ __attribute__((aligned(16))) unsigned char smem[];
  lptr L = (lptr)smem;
  const unsigned lds0 = (unsigned)(uintptr_t)L;
  const int tid = threadIdx.x, l = tid & 63, w = __builtin_amdgcn_readfirstlane(tid >> 6);
  const int bid = blockIdx.x, xcd = bid & 7, idx = bid >> 3;
  const int cp = bid, cw = w >> 2, rg = w & 3, ch = 2 * cp + cw;
  const int g4 = l >> 4, q4 = (l >> 2) & 3, p4 = l & 3, n16 = l & 15;
  rsrc_t rs = __builtin_amdgcn_make_buffer_rsrc((void*)p.ws, (short)0, WS_BYTES, 0x00020000);
  TS(0);

  const unsigned ep = ld4(rs, WS_EPOCH, 0);
  const v4u xblk = *(const GAS v4u*)(p.x + (size_t)(8 * bid + (l >> 3)) * DM + 64 * w + 8 * (l & 7));
  const v4u xrow = *(const GAS v4u*)(p.x + (size_t)(8 * bid + w) * DM + 8 * l);
  const unsigned pdt = *(const GAS unsigned*)(p.logdt + 2 * cp);
  const unsigned pA = *(const GAS unsigned*)(p.logA + ch * NMODE + 2 * l);
  const unsigned pB = *(const GAS unsigned*)(p.aim + ch * NMODE + 2 * l);
  const unsigned pCr = *(const GAS unsigned*)(p.cre + ch * NMODE + 2 * l);
  const unsigned pCi = *(const GAS unsigned*)(p.cim + ch * NMODE + 2 * l);
  const unsigned pds = *(const GAS unsigned*)(p.dskip + 2 * cp);
  FENCE();
  {
    u64 pc; unsigned len;
    asm volatile("s_getpc_b64 %0\n.Lcode_here:\n\ts_mov_b32 %1, .Lcode_end-.Lcode_here" : "=s"(pc), "=s"(len));
    const unsigned o = (unsigned)(((idx & 3) * 8 + w) * 1024 + 16 * l);
    glds1((const void*)((pc & ~15ull) + (o < len ? o : 0u)), lds0 + L_DUMP + w * 1024);
  }
  const int role = cw ? 3 - rg : rg, rj = (role >> 1) | (cw << 1), un = 4 * bid + rj;
  const bool retile = bid < 224, rtW1 = (role & 1) == 0;
  if (retile) {
    if (rtW1) {
      const size_t o1 = (size_t)(16 * (un & 31) + (l >> 3)) * FF + 64 * (un >> 5) + 8 * (l & 7);
      glds1(p.wg + o1, lds0 + L_RT + rj * 4096);
      glds1(p.wg + o1 + 8 * FF, lds0 + L_RT + rj * 4096 + 1024);
      glds1(p.wu + o1, lds0 + L_RT + rj * 4096 + 2048);
      glds1(p.wu + o1 + 8 * FF, lds0 + L_RT + rj * 4096 + 3072);
    } else {
      const size_t o2 = (size_t)(32 * (un >> 4) + 16 * (un & 1) + (l >> 3)) * DM + 64 * ((un >> 1) & 7) + 8 * (l & 7);
      glds1(p.wd + o2, lds0 + L_RT + 16384 + rj * 2048);
      glds1(p.wd + o2 + 8 * DM, lds0 + L_RT + 16384 + rj * 2048 + 1024);
    }
  }
  FENCE();
  if (tid < 16) *(LAS unsigned*)(L + L_CNT + 4 * tid) = 0u;
  if (tid < 64) *(LAS v4u*)(L + L_ST2 + 16 * tid) = v4u{0u, 0u, 0u, 0u};
  if (tid >= 64 && tid < 124) { const int i = tid - 64; *(LAS v4u*)(L + L_U + (i / 30) * U_CH + (i % 30) * 16) = v4u{0u, 0u, 0u, 0u}; }
  if (tid >= 128 && tid < 144) { const int i = tid - 128; *(LAS v4u*)(L + L_KX + (i >> 1) * KX_CP + 4096 + (i & 1) * 16) = v4u{0u, 0u, 0u, 0u}; }
  if (tid >= 192 && tid < 194) *(LAS v4u*)(L + L_KC + (tid - 192) * KC_CH) = v4u{0u, 0u, 0u, 0u};
  unsigned eo[11];
#pragma unroll
  for (int j = 0; j < 11; ++j) {
    const int li = 2047 - 8 * (tid & 255) - j;
    const int cf = (int)(pkbf((float)li, 0.f) & 0xffffu) - 17024;
    eo[j] = lds0 + L_KC + (tid >> 8) * KC_CH + 16 + 2 * (li < 128 ? li : cf);
    asm volatile("" : "+v"(eo[j]));
  }
  FENCE();
  const unsigned tag = __builtin_amdgcn_readfirstlane(ep) + 1u;
  const v2u tag2 = {tag, tag};
  {
    const bool odd = (l >> 3) & 1;
    const unsigned r0 = dppu<0x128>(odd ? xblk.x : xblk.z), r1 = dppu<0x128>(odd ? xblk.y : xblk.w);
    const int o = WS_XT + (32 * w + 4 * (l & 7) + (odd ? 2 : 0)) * XT_CP + (4 * bid + (l >> 4)) * 16;
    st16(v4u{odd ? r0 : xblk.x, tag, odd ? xblk.z : r0, tag}, rs, o);
    st16(v4u{odd ? r1 : xblk.y, tag, odd ? xblk.w : r1, tag}, rs, o + XT_CP);
  }
  {
    float v[8];
#pragma unroll
    for (int j = 0; j < 4; ++j) { v[2 * j] = bflo(xrow[j]); v[2 * j + 1] = bfhi(xrow[j]); }
    float s = ((v[0] + v[1]) + (v[2] + v[3])) + ((v[4] + v[5]) + (v[6] + v[7]));
    const float mean = wave_sum(s) * (1.f / DM);
    float q = 0.f;
#pragma unroll
    for (int j = 0; j < 8; ++j) { const float d = v[j] - mean; q += d * d; }
    const float var = rbf(wave_sum(q) * (1.f / DM));
    const float sd = __builtin_sqrtf(rbf(var + 1e-5f));
    st8(v2u{pkbf(mean, sd), tag}, rs, l == 0 ? WS_ST1 + (8 * bid + w) * 8 : 0x7ffffff0);
  }
  FENCE();
  TS(1);
  {
    const float dt = rbf(expf(cw ? bfhi(pdt) : bflo(pdt)));
    const float LOG2E = 1.4426950408889634f, INV2PI = 0.15915494309189535f;
    const f32x2 a2 = {-rbf(expf(bflo(pA))) * dt * LOG2E, -rbf(expf(bfhi(pA))) * dt * LOG2E};
    const f32x2 bt = {bflo(pB) * dt * INV2PI, bfhi(pB) * dt * INV2PI};
    lptr t = L + L_T + cw * T_CH + 8 * l;
    const C2 one = {f32x2{1.f, 1.f}, f32x2{0.f, 0.f}};
    if (role == 0) {
      const C2 e1 = cexp2(a2, bt, 1.f), e128 = cexp2(a2, bt, 128.f);
      C2 v = e1;
      put(t, 0, one); put(t, 8, e128);
#pragma unroll
      for (int i = 1; i < 8; ++i) {
        put(t, i, v); put(t, 8 + i, cmul(v, e128));
        if (i < 7) v = cmul(v, e1);
      }
    } else if (role == 1) {
      const C2 e2 = cexp2(a2, bt, 2.f), e128 = cexp2(a2, bt, 128.f), e256 = cexp2(a2, bt, 256.f);
      const C2 e4 = cmul(e2, e2), e6 = cmul(e4, e2), e384 = cmul(e256, e128);
      put(t, 16, e256); put(t, 17, cmul(e256, e2)); put(t, 18, cmul(e256, e4)); put(t, 19, cmul(e256, e6));
      put(t, 20, e384); put(t, 21, cmul(e384, e2)); put(t, 22, cmul(e384, e4)); put(t, 23, cmul(e384, e6));
      C2 v = cmul(e256, e256);
#pragma unroll
      for (int j = 0; j < 4; ++j) {
        put(t, 24 + 2 * j, v); put(t, 25 + 2 * j, cmul(v, e4));
        if (j < 3) v = cmul(v, e128);
      }
    } else if (role == 2) {
      const C2 e128 = cexp2(a2, bt, 128.f);
      C2 v = cexp2(a2, bt, 1024.f);
#pragma unroll
      for (int i = 0; i < 9; ++i) {
        put(t, 32 + i, v);
        if (i < 8) v = cmul(v, e128);
      }
    } else {
      const f32x2 cr = {bflo(pCr) * dt, bfhi(pCr) * dt}, ci = {bflo(pCi) * dt, bfhi(pCi) * dt};
      const float mx = wave_max(fmaxf(fabsf(cr.x) + fabsf(ci.x), fabsf(cr.y) + fabsf(ci.y)));
      int es = 258 - (int)(asu(mx) >> 23);
      es = es > 220 ? 220 : (es < 34 ? 34 : es);
      const float S2 = asf((unsigned)(es + 1) << 23);
      if (l == 0) *(LAS unsigned*)(L + L_CNT + 32 + 4 * cw) = (unsigned)(254 - es) << 23;
      const C2 e8 = cexp2(a2, -bt, 8.f), e64 = cexp2(a2, -bt, 64.f);
      C2 v = {cr * S2, ci * -S2};
      C2 v2 = cmul(v, e64);
#pragma unroll
      for (int j = 0; j < 8; ++j) {
        put(t, 48 + j, v); put(t, 56 + j, v2);
        if (j < 7) { v = cmul(v, e8); v2 = cmul(v2, e8); }
      }
    }
  }
  FENCE();
  TS(2);
  VMWAIT(3);
  if (retile) {
    if (rtW1) {
      lptr a0 = L + L_RT + rj * 4096 + (g4 & 1) * 2048 + (8 * (g4 >> 1) + q4) * 128 + 8 * p4;
      const int o = WS_W1F + ((4 * (un >> 5)) * 32 + (un & 31)) * 1024 + 16 * l;
#pragma unroll
      for (int f = 0; f < 4; ++f) st16(cat(trd(a0 + 32 * f), trd(a0 + 32 * f + 512)), rs, o + f * 32768);
    } else {
      lptr a1 = L + L_RT + 16384 + rj * 2048 + (4 * (g4 & 1) + q4) * 128 + 32 * (g4 >> 1) + 8 * p4;
      const int o = WS_WDF + ((((un >> 1) & 7) * 56 + (un >> 4)) * 4 + (g4 >> 1)) * 1024 + ((un & 1) * 32 + (l & 31)) * 16;
#pragma unroll
      for (int f = 0; f < 2; ++f) st16(cat(trd(a1 + 64 * f), trd(a1 + 64 * f + 1024)), rs, o + f * 2048);
    }
  }
  FENCE();
  TS(3);
  const int t0 = 256 * w + 16 * n16 + 4 * g4;
  const int gxo = WS_XT + cp * XT_CP + 8 * t0, gso = WS_ST1 + 8 * t0;
  BAR();
  v4u xa = ld16(rs, gxo, 0), xb = ld16(rs, gxo + 16, 0), s0 = ld16(rs, gso, 0), s1 = ld16(rs, gso + 16, 0);
  FENCE();
  if (role < 3) {
    lptr ta = L + L_T + cw * T_CH + (16 * role + n16) * T_ROW + 16 * g4, tb = L + L_T + cw * T_CH + (48 + n16) * T_ROW + 16 * g4;
    f32x4 acc = {0.f, 0.f, 0.f, 0.f};
#pragma unroll
    for (int ks = 0; ks < 8; ++ks) acc = mfma16h(*(LAS v4u*)(ta + 64 * ks), *(LAS v4u*)(tb + 64 * ks), acc);
#pragma unroll
    for (int r = 0; r < 4; ++r) {
      const int i = 4 * g4 + r;
      int ci = 512 + 16 * i + n16;
      if (role == 0) ci = 128 * (i >> 3) + (i & 7) + 8 * n16;
      if (role == 1) ci = i < 8 ? 256 + 64 * (i >> 2) + (i & 3) + 4 * n16 : 384 + 32 * ((i - 8) >> 1) + (i & 1) + 2 * n16;
      if (ci <= 640) *(LAS _Float16*)(L + L_KC + cw * KC_CH + 16 + 2 * ci) = (_Float16)acc[r];
    }
  }
  BAR();
  TS(4);
  {
    unsigned e[12];
#pragma unroll
    for (int j = 0; j < 11; ++j) e[j] = *(LAS u16*)(uintptr_t)eo[j];
    e[11] = 0;
    unsigned P[6], Q[5];
#pragma unroll
    for (int j = 0; j < 6; ++j) P[j] = e[2 * j] | (e[2 * j + 1] << 16);
#pragma unroll
    for (int j = 0; j < 5; ++j) Q[j] = __builtin_amdgcn_alignbit(P[j + 1], P[j], 16);
    lptr kx = L + L_KX + (tid >> 8) * KX_CH + 16 * (tid & 255);
    *(LAS v4u*)(kx) = v4u{P[0], P[1], P[2], P[3]};
    *(LAS v4u*)(kx + KX_CP) = v4u{Q[0], Q[1], Q[2], Q[3]};
    *(LAS v4u*)(kx + 2 * KX_CP) = v4u{P[1], P[2], P[3], P[4]};
    *(LAS v4u*)(kx + 3 * KX_CP) = v4u{Q[1], Q[2], Q[3], Q[4]};
  }
  FENCE();
  TS(5);
  VMWAIT(4);
  st8(tag2, rs, (retile && l == 0) ? (rtW1 ? WS_F2 + un * 8 : WS_F3 + ((((un >> 1) & 7) * 56 + (un >> 4)) * 2 + (un & 1)) * 8) : 0x7ffffff0);
#pragma unroll 1
  for (int it = 0; it < SPIN_CAP; ++it) {
    const bool ok = xa.y == tag && xa.w == tag && xb.y == tag && xb.w == tag && s0.y == tag && s0.w == tag && s1.y == tag && s1.w == tag;
    if (__all(ok)) break;
    asm volatile("" ::: "memory");
    xa = ld16(rs, gxo, 0); xb = ld16(rs, gxo + 16, 0); s0 = ld16(rs, gso, 0); s1 = ld16(rs, gso + 16, 0);
  }
  FENCE();
  TS(6);
  const int rb = (xcd & 1) * 8 + (idx >> 2), cb = (xcd >> 1) * 4 + (idx & 3);
  const int fgW = 7 * cb + (w > 0 ? w - 1 : 0);
  const int f2o = WS_F2 + ((fgW >> 2) * 32 + (l & 31)) * 8;
  const v2u f2s = ld8(rs, f2o, 0);
  FENCE();
  const unsigned xs[4] = {xa.x, xa.z, xb.x, xb.z};
  unsigned ud[4];
  {
    const unsigned st[4] = {s0.x, s0.z, s1.x, s1.z};
    const float d0 = bflo(pds), d1 = bfhi(pds);
    float u0[4], u1[4];
#pragma unroll
    for (int i = 0; i < 4; ++i) {
      const float m = bflo(st[i]), rsd = __builtin_amdgcn_rcpf(bfhi(st[i]));
      const unsigned c = pkbf(bflo(xs[i]) - m, bfhi(xs[i]) - m);
      const unsigned u = pkbf(bflo(c) * rsd, bfhi(c) * rsd);
      u0[i] = bflo(u); u1[i] = bfhi(u);
      ud[i] = pkbf(u0[i] * d0, u1[i] * d1);
    }
    *(LAS v2u*)(L + L_U + 480 + 2 * t0) = v2u{pkh(u0[0], u0[1]), pkh(u0[2], u0[3])};
    *(LAS v2u*)(L + L_U + U_CH + 480 + 2 * t0) = v2u{pkh(u1[0], u1[1]), pkh(u1[2], u1[3])};
  }
  BAR();
  TS(7);
  st4(tag, rs, (bid == 0 && tid == 0) ? WS_EPOCH : 0x7ffffff0);
  lptr kA = L + L_KX + cw * KX_CH + (3 - (n16 & 3)) * KX_CP + 2 * (12 - 4 * (n16 >> 2) + 8 * g4) + 64 * (6 - 2 * rg);
  lptr uB = L + L_U + cw * U_CH + 32 * n16 + 16 * g4 + 384 - 128 * rg;
#define AFRAG(a, rr) cat(*(LAS v2u*)(kA + 64 * (57 - 8 * (a) - (rr))), *(LAS v2u*)(kA + 64 * (57 - 8 * (a) - (rr)) + 8))
#define BFRAG(b, rr) (*(LAS v4u*)(uB + 512 * (b) + 64 * (1 - (rr))))
  const f32x2 invS = *(LAS f32x2*)(L + L_CNT + 32);
  lptr redw = L + L_RED + w * 1024 + 16 * l;
  const int wvo = w > 0 ? 16 * l : 0x7ffffff0;
  const int wso = WS_W1F + fgW * 32768;
  const int f4w = l < 4 ? WS_F4 + l * 16384 + (w * 256 + cp) * 8 : 0x7ffffff0;
  v4u wa[32];
  auto conv_out = [&]() {
    lptr rr = L + L_RED + (w < 6 ? w : w - 6) * 8192 + 16 * l;
    const f32x4 y0 = (*(LAS f32x4*)(rr) + *(LAS f32x4*)(rr + 1024)) + (*(LAS f32x4*)(rr + 2048) + *(LAS f32x4*)(rr + 3072));
    const f32x4 y1 = (*(LAS f32x4*)(rr + 4096) + *(LAS f32x4*)(rr + 5120)) + (*(LAS f32x4*)(rr + 6144) + *(LAS f32x4*)(rr + 7168));
    float h0[4], h1[4];
#pragma unroll
    for (int i = 0; i < 4; ++i) {
      const unsigned y = pkbf(y0[i] * invS.x, y1[i] * invS.y);
      const unsigned s = pkbf(bflo(y) + bflo(ud[i]), bfhi(y) + bfhi(ud[i]));
      h0[i] = bflo(xs[i]) + bflo(s); h1[i] = bfhi(xs[i]) + bfhi(s);
    }
    st16(v4u{pkbf(h0[0], h0[1]), pkbf(h0[2], h0[3]), pkbf(h1[0], h1[1]), pkbf(h1[2], h1[3])}, rs,
         WS_HT + (t0 >> 5) * 32768 + cp * 128 + (t0 & 31) * 4);
  };
  {
    f32x4 acc[2] = {{0.f, 0.f, 0.f, 0.f}, {0.f, 0.f, 0.f, 0.f}};
#pragma unroll
    for (int rr = 0; rr < 2; ++rr) {
      const v4u b0 = BFRAG(0, rr), b1 = BFRAG(1, rr), a0 = AFRAG(0, rr), a1 = AFRAG(1, rr);
      acc[0] = mfma16h(a0, b0, acc[0]); acc[1] = mfma16h(a0, b1, acc[1]); acc[1] = mfma16h(a1, b0, acc[1]);
    }
    *(LAS f32x4*)(redw) = acc[0]; *(LAS f32x4*)(redw + 8192) = acc[1];
  }
  BAR();
  if (w < 2) conv_out();
  FENCE();
  wait8(rs, f2o, f2s, tag);
  TS(8);
  {
    f32x4 acc[2] = {{0.f, 0.f, 0.f, 0.f}, {0.f, 0.f, 0.f, 0.f}};
#pragma unroll
    for (int rr = 0; rr < 2; ++rr) {
      v4u bf[4];
#pragma unroll
      for (int b = 0; b < 4; ++b) bf[b] = BFRAG(b, rr);
#pragma unroll
      for (int a = 0; a < 4; ++a) {
        const v4u af = AFRAG(a, rr);
        if (a < 3) { acc[0] = mfma16h(af, bf[2 - a], acc[0]); wa[3 * rr + a] = ld16(rs, wvo, wso + (3 * rr + a) * 1024); }
        acc[1] = mfma16h(af, bf[3 - a], acc[1]);
      }
    }
    *(LAS f32x4*)(redw + 16384) = acc[0]; *(LAS f32x4*)(redw + 24576) = acc[1];
  }
  BAR();
  if (w < 2) { VMWAIT(6); st8(tag2, rs, f4w); }
  else if (w < 4) conv_out();
  FENCE();
  TS(9);
  const int qh = xcd & 1;
  const int f4r = WS_F4 + (xcd >> 1) * 16384 + qh * 2048;
  v2u f4s;
  {
    f32x4 acc[4];
#pragma unroll
    for (int t = 0; t < 4; ++t) acc[t] = f32x4{0.f, 0.f, 0.f, 0.f};
#pragma unroll
    for (int rr = 0; rr < 2; ++rr) {
      v4u bf[8];
#pragma unroll
      for (int b = 0; b < 8; ++b) bf[b] = BFRAG(b, rr);
#pragma unroll
      for (int a = 0; a < 8; ++a) {
        const v4u af = AFRAG(a, rr);
#pragma unroll
        for (int b = 0; b < 8; ++b)
          if (a + b >= 4 && a + b < 8) acc[a + b - 4] = mfma16h(af, bf[b], acc[a + b - 4]);
        if (a < 6) wa[6 + 6 * rr + a] = ld16(rs, wvo, wso + (6 + 6 * rr + a) * 1024);
      }
      if (rr == 0) {
        FENCE();
        if (w == 2 || w == 3) { VMWAIT(6); st8(tag2, rs, f4w); }
        FENCE();
      }
    }
    FENCE();
    f4s = ld8(rs, f4r + (64 * rg + l) * 8, 0);
    *(LAS f32x4*)(redw + 32768) = acc[0]; *(LAS f32x4*)(redw + 40960) = acc[1];
    *(LAS f32x4*)(redw) = acc[2]; *(LAS f32x4*)(redw + 8192) = acc[3];
  }
  BAR();
  TS(10);
  const unsigned char* htg = p.ws + WS_HT + (size_t)rb * 32768 + 16 * l;
  if (w < 4) {
    wait8(rs, f4r + (64 * rg + l) * 8, f4s, tag);
    glds4_sc1(htg + w * 8192, lds0 + w * 8192);
    glds4_sc1(htg + w * 8192 + 4096, lds0 + w * 8192 + 4096);
  } else {
    conv_out();
    asm volatile("s_waitcnt lgkmcnt(0)" ::: "memory");
    lds_arrive(L, 4);
  }
  FENCE();
#pragma unroll
  for (int ks = 18; ks < 22; ++ks) wa[ks] = ld16(rs, wvo, wso + ks * 1024);
  FENCE();
  TS(11);
  VMWAIT(4);
  if (w < 4) lds_arrive(L, 0);
  else st8(tag2, rs, f4w);
  FENCE();
  TS(12);
  const int trb = (4 * (g4 >> 1) + (q4 >> 1)) * 128 + (4 * (g4 & 1) + p4) * 16 + (q4 & 1) * 8;
  const int g2r = xcd * 4 + (idx >> 3), db = idx & 7, tbo = g2r >> 1, th = g2r & 1;
  const int f3o = WS_F3 + ((db * 56 + 7 * w) * 2 + (n16 < 14 ? n16 : 0)) * 8;
  const int wdo = WS_WDF + (db * 56 + 7 * w) * 4096;
  v4u wd[28];
#pragma unroll
  for (int i = 0; i < 16; ++i) wd[12 + i] = wa[i];
  if (w > 0) {
    v2u f3s;
#pragma unroll
    for (int s = 0; s < 4; ++s) {
      lds_wait(L, s, s == 0 ? 4 : 1);
      if (s == 0) TS(13);
      lptr bt = L + s * 32768 + trb;
      f32x16 acc;
#pragma unroll
      for (int r = 0; r < 16; ++r) acc[r] = 0.f;
      float cs0 = 0.f, cs1 = 0.f;
#pragma unroll
      for (int ks = 0; ks < 32; ++ks) {
        acc = mfma32b(wa[ks], cat(trd(bt + ks * 1024), trd(bt + ks * 1024 + 256)), acc);
        if (s == 0) {
          if (ks < 10) wa[22 + ks] = ld16(rs, wvo, wso + (22 + ks) * 1024);
          if (ks == 10) f3s = ld8(rs, f3o, 0);
          cs0 = dot2acc(wa[ks][0], 0x3f803f80u, cs0); cs1 = dot2acc(wa[ks][1], 0x3f803f80u, cs1);
          cs0 = dot2acc(wa[ks][2], 0x3f803f80u, cs0); cs1 = dot2acc(wa[ks][3], 0x3f803f80u, cs1);
        }
        if ((s == 1 || s == 2) && (ks & 3) == 0 && ks >= 8) wd[6 * (s - 1) + (ks >> 2) - 2] = ld16(rs, 16 * l, wdo + (6 * (s - 1) + (ks >> 2) - 2) * 1024);
        if (s == 3 && ks >= 8 && ks < 24) wd[4 + ks] = ld16(rs, 16 * l, wdo + (4 + ks) * 1024);
        if (s > 0 && ks == 7) {
          FENCE();
          VMWAIT(0);
          st8(tag2, rs, l == 0 ? WS_F6 + ((16 * (s - 1) + rb) * 128 + fgW) * 8 : 0x7ffffff0);
          FENCE();
        }
        if ((ks & 3) == 3) SBAR();
      }
      FENCE();
      if (s == 0) {
        wait8(rs, f3o, f3s, tag);
        const float c = x32_add(cs0 + cs1);
        if (l < 32) *(LAS float*)(L + L_CSW + w * 128 + 4 * l) = c;
      }
      FENCE();
      float csr[16];
#pragma unroll
      for (int i = 0; i < 4; ++i) {
        const f32x4 v = *(volatile LAS f32x4*)(L + L_CSW + w * 128 + 32 * i + 16 * (l >> 5));
        csr[4 * i] = v.x; csr[4 * i + 1] = v.y; csr[4 * i + 2] = v.z; csr[4 * i + 3] = v.w;
      }
      TS(14 + 2 * s);
      f32x2 st;
#pragma unroll 1
      for (int it = 0; it < (1 << 22); ++it) {
        st = *(volatile LAS f32x2*)(L + L_ST2 + s * 256 + 8 * (l & 31));
        if (__all(st.y != 0.f)) break;
        __builtin_amdgcn_s_sleep(1);
      }
      const float nm = -st.x, rstd = st.y;
      v4u hv;
#pragma unroll
      for (int j = 0; j < 4; ++j) {
        const unsigned gp = pkbf(__builtin_fmaf(nm, csr[2 * j], acc[2 * j]) * rstd, __builtin_fmaf(nm, csr[2 * j + 1], acc[2 * j + 1]) * rstd);
        const unsigned up = pkbf(__builtin_fmaf(nm, csr[8 + 2 * j], acc[8 + 2 * j]) * rstd, __builtin_fmaf(nm, csr[9 + 2 * j], acc[9 + 2 * j]) * rstd);
        const float g0 = bflo(gp), g1 = bfhi(gp);
        const float C1 = -2.f * 0.7978845608028654f * 1.4426950408889634f, C2c = C1 * 0.044715f;
        const float e0 = __builtin_amdgcn_exp2f(g0 * __builtin_fmaf(g0 * g0, C2c, C1));
        const float e1 = __builtin_amdgcn_exp2f(g1 * __builtin_fmaf(g1 * g1, C2c, C1));
        const unsigned ge = pkbf(g0 * __builtin_amdgcn_rcpf(1.f + e0), g1 * __builtin_amdgcn_rcpf(1.f + e1));
        hv[j] = pkbf(bflo(ge) * bflo(up), bfhi(ge) * bfhi(up));
      }
      st16(hv, rs, WS_HF + ((16 * s + rb) * 112 + fgW) * 1024 + 16 * l);
      FENCE();
      TS(15 + 2 * s);
    }
  } else {
    auto stats = [&](int s) {
      lptr bt = L + s * 32768 + trb;
      float s1 = 0.f, s2 = 0.f, s1b = 0.f, s2b = 0.f;
#pragma unroll 8
      for (int ks = 0; ks < 32; ++ks) {
        const v2u a = trd(bt + ks * 1024), b = trd(bt + ks * 1024 + 256);
        s1 = dot2acc(a.x, 0x3f803f80u, s1); s2 = dot2acc(a.x, a.x, s2);
        s1b = dot2acc(a.y, 0x3f803f80u, s1b); s2b = dot2acc(a.y, a.y, s2b);
        s1 = dot2acc(b.x, 0x3f803f80u, s1); s2 = dot2acc(b.x, b.x, s2);
        s1b = dot2acc(b.y, 0x3f803f80u, s1b); s2b = dot2acc(b.y, b.y, s2b);
      }
      const float mean = x32_add(s1 + s1b) * (1.f / DM);
      const float var = rbf(fmaxf(x32_add(s2 + s2b) * (1.f / DM) - mean * mean, 0.f));
      const float sd = rbf(__builtin_sqrtf(rbf(var + 1e-5f)));
      if (l < 32) *(LAS f32x2*)(L + L_ST2 + s * 256 + 8 * l) = f32x2{rbf(mean), 1.f / sd};
    };
#pragma unroll 1
    for (int s = 0; s < 4; ++s) {
      const int fo = f4r + (s < 3 ? s + 1 : 0) * 4096 + 16 * l;
      v4u fa = ld16(rs, fo, 0), fb = ld16(rs, fo + 1024, 0);
      FENCE();
      if (s == 0) lds_wait(L, 0, 4);
      else { VMWAIT(2); lds_arrive(L, s); }
      if (s == 0) stats(0);
      if (s < 3) {
#pragma unroll 1
        for (int it = 0; it < SPIN_CAP; ++it) {
          if (__all(ok16(fa, tag) && ok16(fb, tag))) break;
          asm volatile("" ::: "memory");
          fa = ld16(rs, fo, 0); fb = ld16(rs, fo + 1024, 0);
        }
        lds_wait(L, 4, 4);
#pragma unroll 1
        for (int j = 0; j < 8; ++j) glds4_sc1(htg + (size_t)(s + 1) * 16 * 32768 + j * 4096, lds0 + (s + 1) * 32768 + j * 4096);
      }
      TS(14 + 2 * s);
      if (s > 0) stats(s);
      TS(15 + 2 * s);
    }
    wait8(rs, f3o, ld8(rs, f3o, 0), tag);
  }
  FENCE();
  int wq = w;
  asm volatile("" : "+s"(wq));
  if (wq == 0) {
#pragma unroll
    for (int j = 0; j < 28; ++j) wd[j] = ld16(rs, 16 * l, wdo + j * 1024);
  }
  FENCE();
  {
    const int f6o = WS_F6 + ((16 * g4 + tbo) * 128 + 14 * w + (n16 < 14 ? n16 : 0)) * 8;
    v2u f6s = ld8(rs, f6o, 0);
    FENCE();
    VMWAIT(1);
    st8(tag2, rs, (l == 0 && w > 0) ? WS_F6 + ((48 + rb) * 128 + fgW) * 8 : 0x7ffffff0);
    FENCE();
    TS(22);
    const int hvo = (g4 >> 1) * 1024 + ((g4 & 1) * 32 + th * 16 + n16) * 16;
    const int fw = w & 3, rch = 64 * db + 16 * fw + (l >> 2);
    const int hro = WS_HT + tbo * 32768 + (rch >> 1) * 128 + (4 * th + p4) * 16 + (rch & 1) * 8;
#pragma unroll 1
    for (int it = 0; it < SPIN_CAP; ++it) {
      if (__all(ok8(f6s, tag) || g4 == 3)) break;
      asm volatile("" ::: "memory");
      f6s = ld8(rs, f6o, 0);
    }
    FENCE();
    v4u hb[4][7];
    v2u hres[2];
#pragma unroll
    for (int s = 0; s < 3; ++s)
#pragma unroll
      for (int ks = 0; ks < 7; ++ks) hb[s][ks] = ld16(rs, hvo, WS_HF + ((16 * s + tbo) * 112 + 14 * w + 2 * ks) * 1024);
    hres[0] = ld8(rs, hro, (w >> 2) * 16 * 32768);
    hres[1] = ld8(rs, hro, ((w >> 2) + 2) * 16 * 32768);
    FENCE();
    TS(23);
#pragma unroll
    for (int s = 0; s < 4; ++s) {
      f32x4 acc[4];
#pragma unroll
      for (int dt = 0; dt < 4; ++dt) acc[dt] = f32x4{0.f, 0.f, 0.f, 0.f};
#pragma unroll
      for (int ks = 0; ks < 7; ++ks)
#pragma unroll
        for (int dt = 0; dt < 4; ++dt) acc[dt] = mfma16b(wd[4 * ks + dt], hb[s][ks], acc[dt]);
      FENCE();
      if (s == 0) {
        f6s = ld8(rs, f6o, 0);
        BAR();
      }
      if (s == 1) {
        wait8(rs, f6o, f6s, tag);
#pragma unroll
        for (int ks = 0; ks < 7; ++ks) hb[3][ks] = ld16(rs, hvo, WS_HF + ((48 + tbo) * 112 + 14 * w + 2 * ks) * 1024);
        FENCE();
      }
      const bool fin = (w >> 2) == (s & 1);
      lptr red = L + L_RED2 + (s & 1) * 32768 + 16 * l;
#pragma unroll
      for (int dt = 0; dt < 4; ++dt) *(LAS f32x4*)(red + w * 4096 + dt * 1024) = acc[dt];
      if (fin) *(LAS v2u*)(L + L_HX + w * 512 + 8 * l) = hres[s >> 1];
      BAR();
      if (fin) {
        const v2u ht = trd(L + L_HX + w * 512 + 8 * l);
        f32x4 sum = *(LAS f32x4*)(red + fw * 1024);
#pragma unroll
        for (int v = 1; v < 8; ++v) sum += *(LAS f32x4*)(red + v * 4096 + fw * 1024);
        const unsigned a0 = pkbf(sum.x, sum.y), a1 = pkbf(sum.z, sum.w);
        const v2u o = {pkbf(bflo(ht.x) + bflo(a0), bfhi(ht.x) + bfhi(a0)), pkbf(bflo(ht.y) + bflo(a1), bfhi(ht.y) + bfhi(a1))};
        *(GAS v2u*)(p.out + (size_t)(512 * s + 16 * g2r + n16) * DM + 64 * db + 16 * fw + 4 * g4) = o;
      }
      TS(24 + s);
    }
  }
  asm volatile(".Lcode_end:");
}

extern "C" void kernel_launch(void* const* d_in, const int* in_sizes, int n_in, void* d_out, int out_size, void* d_ws,
                              size_t ws_size, hipStream_t stream) {
  Params p;
  p.x = (const u16*)d_in[0]; p.logA = (const u16*)d_in[1]; p.aim = (const u16*)d_in[2]; p.cre = (const u16*)d_in[3];
  p.cim = (const u16*)d_in[4]; p.logdt = (const u16*)d_in[5]; p.dskip = (const u16*)d_in[6];
  p.wg = (const u16*)d_in[8]; p.wu = (const u16*)d_in[9]; p.wd = (const u16*)d_in[10];
  p.out = (u16*)d_out; p.ws = (unsigned char*)d_ws;
  (void)hipFuncSetAttribute((const void*)fused_kernel, hipFuncAttributeMaxDynamicSharedMemorySize, L_BYTES);
  fused_kernel<<<dim3(256), dim3(512), L_BYTES, stream>>>(p);
}
```

```cpp
#include <hip/hip_runtime.h>
#include <stdint.h>

typedef unsigned short u16;
typedef unsigned long long u64;
typedef unsigned v4u __attribute__((ext_vector_type(4)));
typedef unsigned v2u __attribute__((ext_vector_type(2)));
typedef float f32x2 __attribute__((ext_vector_type(2)));
typedef float f32x4 __attribute__((ext_vector_type(4)));
typedef float f32x16 __attribute__((ext_vector_type(16)));
typedef short s16x4 __attribute__((ext_vector_type(4)));
typedef _Float16 f16x8 __attribute__((ext_vector_type(8)));
typedef _Float16 f16x2 __attribute__((ext_vector_type(2)));
typedef __bf16 b16x8 __attribute__((ext_vector_type(8)));
typedef __bf16 b16x2 __attribute__((ext_vector_type(2)));
#define LAS __attribute__((address_space(3)))
#define GAS __attribute__((address_space(1)))
typedef LAS unsigned char* lptr;

#define TOUCH 0
#define STAMP 0

constexpr int SEQ = 2048, DM = 512, NMODE = 128, FF = 1792;
constexpr int SC1 = 16;
constexpr int SPIN_CAP = 20000;

constexpr int WS_EPOCH = 0;
constexpr int WS_ST1 = 4096;
constexpr int WS_F2 = 32768;
constexpr int WS_F3 = 40960;
constexpr int WS_F4 = 65536;
constexpr int WS_F6 = 131072;
constexpr int WS_HT = 1 << 20;
constexpr int WS_W1F = 3 << 20;
constexpr int WS_WDF = 7 << 20;
constexpr int WS_HF = 10 << 20;
constexpr int WS_XT = 17 << 20;
constexpr int XT_CP = 16384 + 256;
constexpr int WS_DBG = 22 << 20;
constexpr int WS_BYTES = 24 << 20;
static_assert(WS_XT + 256 * XT_CP <= WS_DBG, "ws map");

constexpr int L_T = 0, T_ROW = 528, T_CH = 64 * T_ROW;
constexpr int L_RED = 65536;
constexpr int L_RT = L_RED + 4096, L_DUMP = L_RED + 28672, L_KC = L_RED + 36864, KC_CH = 1312;
constexpr int L_KX = L_RED + 49152, KX_CP = 4160, KX_CH = 4 * KX_CP;
constexpr int L_U = L_KX + 2 * KX_CH, U_CH = 4576;
constexpr int L_CNT = L_U + 2 * U_CH;
constexpr int L_ST2 = L_CNT + 64;
constexpr int L_CSW = L_ST2 + 1024;
constexpr int L_STAMP = L_CSW + 1024;
constexpr int L_BYTES = L_STAMP + 1024;
constexpr int L_RED2 = 0;
constexpr int L_HX = 131072;
static_assert(L_BYTES <= 163840, "lds");
static_assert(2 * T_CH <= L_RT && L_KC + 2 * KC_CH <= L_KX, "lds");
static_assert(L_U % 16 == 0 && L_KC % 16 == 0 && L_CNT % 16 == 0, "align");

struct Params {
  const u16 *x, *logA, *aim, *cre, *cim, *logdt, *dskip, *wg, *wu, *wd;
  u16* out;
  unsigned char* ws;
};

__device__ __forceinline__ float asf(unsigned v) { return __builtin_bit_cast(float, v); }
__device__ __forceinline__ unsigned asu(float v) { return __builtin_bit_cast(unsigned, v); }
__device__ __forceinline__ float bflo(unsigned v) { return asf(v << 16); }
__device__ __forceinline__ float bfhi(unsigned v) { return asf(v & 0xffff0000u); }
__device__ __forceinline__ unsigned pkbf(float lo, float hi) {
  b16x2 r = {(__bf16)lo, (__bf16)hi};
  return __builtin_bit_cast(unsigned, r);
}
__device__ __forceinline__ float rbf(float f) { return bflo(pkbf(f, f)); }
__device__ __forceinline__ unsigned pkh(float lo, float hi) {
  f16x2 r = {(_Float16)lo, (_Float16)hi};
  return __builtin_bit_cast(unsigned, r);
}
template <int C> __device__ __forceinline__ unsigned dppu(unsigned v) {
  return (unsigned)__builtin_amdgcn_update_dpp(0, (int)v, C, 0xf, 0xf, true);
}
template <int C> __device__ __forceinline__ float dppf(float v) { return asf(dppu<C>(asu(v))); }
__device__ __forceinline__ float x16_add(float v) {
  auto s = __builtin_amdgcn_permlane16_swap(asu(v), asu(v), false, false);
  return asf(s[0]) + asf(s[1]);
}
__device__ __forceinline__ float x32_add(float v) {
  auto s = __builtin_amdgcn_permlane32_swap(asu(v), asu(v), false, false);
  return asf(s[0]) + asf(s[1]);
}
__device__ __forceinline__ float wave_sum(float v) {
  v += dppf<0xB1>(v); v += dppf<0x4E>(v); v += dppf<0x141>(v); v += dppf<0x140>(v);
  return x32_add(x16_add(v));
}
__device__ __forceinline__ float wave_max(float v) {
  v = fmaxf(v, dppf<0xB1>(v)); v = fmaxf(v, dppf<0x4E>(v)); v = fmaxf(v, dppf<0x141>(v)); v = fmaxf(v, dppf<0x140>(v));
  auto s = __builtin_amdgcn_permlane16_swap(asu(v), asu(v), false, false);
  v = fmaxf(asf(s[0]), asf(s[1]));
  auto t = __builtin_amdgcn_permlane32_swap(asu(v), asu(v), false, false);
  return fmaxf(asf(t[0]), asf(t[1]));
}
__device__ __forceinline__ float dot2acc(unsigned a, unsigned b, float c) {
  return __builtin_amdgcn_fdot2_f32_bf16(__builtin_bit_cast(b16x2, a), __builtin_bit_cast(b16x2, b), c, false);
}
#define BAR() asm volatile("s_waitcnt lgkmcnt(0)\n\ts_barrier" ::: "memory")
#define VMWAIT(N) asm volatile("s_waitcnt vmcnt(" #N ")" ::: "memory")
#define SBAR() __builtin_amdgcn_sched_barrier(0)
#define FENCE() do { asm volatile("" ::: "memory"); __builtin_amdgcn_sched_barrier(0); } while (0)

__device__ __forceinline__ void glds1(const void* g, unsigned dst) {
  unsigned sv;
  asm volatile("s_mov_b32 %0, m0\n\ts_mov_b32 m0, %2\n\ts_nop 0\n\tglobal_load_lds_dwordx4 %1, off\n\ts_mov_b32 m0, %0"
               : "=&s"(sv) : "v"(g), "s"(dst) : "memory");
}
__device__ __forceinline__ void glds4_sc1(const void* g, unsigned dst) {
  unsigned sv;
  asm volatile("s_mov_b32 %0, m0\n\ts_mov_b32 m0, %2\n\ts_nop 0\n\t"
               "global_load_lds_dwordx4 %1, off sc1\n\tglobal_load_lds_dwordx4 %1, off offset:1024 sc1\n\t"
               "global_load_lds_dwordx4 %1, off offset:2048 sc1\n\tglobal_load_lds_dwordx4 %1, off offset:3072 sc1\n\t"
               "s_mov_b32 m0, %0"
               : "=&s"(sv) : "v"(g), "s"(dst) : "memory");
}
__device__ __forceinline__ v2u trd(lptr p) {
  return __builtin_bit_cast(v2u, __builtin_amdgcn_ds_read_tr16_b64_v4i16((LAS s16x4*)p));
}
__device__ __forceinline__ v4u cat(v2u a, v2u b) { return v4u{a.x, a.y, b.x, b.y}; }
__device__ __forceinline__ f32x4 mfma16h(v4u a, v4u b, f32x4 c) {
  return __builtin_amdgcn_mfma_f32_16x16x32_f16(__builtin_bit_cast(f16x8, a), __builtin_bit_cast(f16x8, b), c, 0, 0, 0);
}
__device__ __forceinline__ f32x4 mfma16b(v4u a, v4u b, f32x4 c) {
  return __builtin_amdgcn_mfma_f32_16x16x32_bf16(__builtin_bit_cast(b16x8, a), __builtin_bit_cast(b16x8, b), c, 0, 0, 0);
}
__device__ __forceinline__ f32x16 mfma32b(v4u a, v4u b, f32x16 c) {
  return __builtin_amdgcn_mfma_f32_32x32x16_bf16(__builtin_bit_cast(b16x8, a), __builtin_bit_cast(b16x8, b), c, 0, 0, 0);
}
typedef __amdgpu_buffer_rsrc_t rsrc_t;
__device__ __forceinline__ v4u ld16(rsrc_t rs, int voff, int soff) { return __builtin_amdgcn_raw_buffer_load_b128(rs, voff, soff, SC1); }
__device__ __forceinline__ v2u ld8(rsrc_t rs, int voff, int soff) { return __builtin_amdgcn_raw_buffer_load_b64(rs, voff, soff, SC1); }
__device__ __forceinline__ unsigned ld4(rsrc_t rs, int voff, int soff) { return __builtin_amdgcn_raw_buffer_load_b32(rs, voff, soff, SC1); }
__device__ __forceinline__ void st16(v4u v, rsrc_t rs, int voff) { __builtin_amdgcn_raw_buffer_store_b128(v, rs, voff, 0, SC1); }
__device__ __forceinline__ void st8(v2u v, rsrc_t rs, int voff) { __builtin_amdgcn_raw_buffer_store_b64(v, rs, voff, 0, SC1); }
__device__ __forceinline__ void st4(unsigned v, rsrc_t rs, int voff) { __builtin_amdgcn_raw_buffer_store_b32(v, rs, voff, 0, SC1); }

__device__ __forceinline__ bool ok8(v2u f, unsigned tag) { return f.x == tag && f.y == tag; }
__device__ __forceinline__ bool ok16(v4u f, unsigned tag) { return f.x == tag && f.y == tag && f.z == tag && f.w == tag; }
__device__ __forceinline__ void wait8(rsrc_t rs, int voff, v2u f, unsigned tag) {
#pragma unroll 1
  for (int it = 0; it < SPIN_CAP; ++it) {
    if (__all(ok8(f, tag))) break;
    asm volatile("" ::: "memory");
    f = ld8(rs, voff, 0);
  }
  asm volatile("" ::: "memory");
}
__device__ __forceinline__ void lds_arrive(lptr L, int c) {
  if ((threadIdx.x & 63) == 0) __hip_atomic_fetch_add((LAS unsigned*)(L + L_CNT) + c, 1u, __ATOMIC_RELAXED, __HIP_MEMORY_SCOPE_WORKGROUP);
}
__device__ __forceinline__ void lds_wait(lptr L, int c, unsigned n) {
#pragma unroll 1
  for (int it = 0; it < (1 << 22); ++it) {
    unsigned v = *(volatile LAS unsigned*)(L + L_CNT + 4 * c);
    if (__builtin_amdgcn_readfirstlane(v) >= n) break;
    __builtin_amdgcn_s_sleep(1);
  }
  asm volatile("" ::: "memory");
}

struct C2 { f32x2 r, i; };
__device__ __forceinline__ C2 cmul(C2 a, C2 b) { return C2{a.r * b.r - a.i * b.i, a.r * b.i + a.i * b.r}; }
__device__ __forceinline__ C2 cexp2(f32x2 a2, f32x2 bt, float p) {
  const float e0 = __builtin_amdgcn_exp2f(a2.x * p), e1 = __builtin_amdgcn_exp2f(a2.y * p);
  const float f0 = __builtin_amdgcn_fractf(bt.x * p), f1 = __builtin_amdgcn_fractf(bt.y * p);
  return C2{f32x2{e0 * __builtin_amdgcn_cosf(f0), e1 * __builtin_amdgcn_cosf(f1)},
            f32x2{e0 * __builtin_amdgcn_sinf(f0), e1 * __builtin_amdgcn_sinf(f1)}};
}
__device__ __forceinline__ void put(lptr t, int row, C2 v) {
  *(LAS v2u*)(t + row * T_ROW) = v2u{pkh(v.r.x, v.i.x), pkh(v.r.y, v.i.y)};
}

#define TX(i) do {} while (0)
#define TS(i) do { if (STAMP == 1 || (i) == 0 || (i) == 29) TX(i); } while (0)
#define TA(i) do { if (STAMP == 2) { FENCE(); TX(i); FENCE(); } } while (0)

__global__ void __launch_bounds__(512, 2) fused_kernel(Params p) {
  extern __shared__ __attribute__((aligned(16))) unsigned char smem[];
  lptr L = (lptr)smem;
  const unsigned lds0 = (unsigned)(uintptr_t)L;
  const int tid = threadIdx.x, l = tid & 63, w = __builtin_amdgcn_readfirstlane(tid >> 6);
  const int bid = blockIdx.x, xcd = bid & 7, idx = bid >> 3;
  const int cp = bid, cw = w >> 2, rg = w & 3, ch = 2 * cp + cw;
  const int g4 = l >> 4, q4 = (l >> 2) & 3, p4 = l & 3, n16 = l & 15;
  rsrc_t rs = __builtin_amdgcn_make_buffer_rsrc((void*)p.ws, (short)0, WS_BYTES, 0x00020000);
  TS(0);

  const unsigned ep = ld4(rs, WS_EPOCH, 0);
  const v4u xblk = *(const GAS v4u*)(p.x + (size_t)(8 * bid + (l >> 3)) * DM + 64 * w + 8 * (l & 7));
  const v4u xrow = *(const GAS v4u*)(p.x + (size_t)(8 * bid + w) * DM + 8 * l);
  const unsigned pdt = *(const GAS unsigned*)(p.logdt + 2 * cp);
  const unsigned pA = *(const GAS unsigned*)(p.logA + ch * NMODE + 2 * l);
  const unsigned pB = *(const GAS unsigned*)(p.aim + ch * NMODE + 2 * l);
  const unsigned pCr = *(const GAS unsigned*)(p.cre + ch * NMODE + 2 * l);
  const unsigned pCi = *(const GAS unsigned*)(p.cim + ch * NMODE + 2 * l);
  const unsigned pds = *(const GAS unsigned*)(p.dskip + 2 * cp);
  FENCE();
  {
    u64 pc; unsigned len;
    asm volatile("s_getpc_b64 %0\n.Lcode_here:\n\ts_mov_b32 %1, .Lcode_end-.Lcode_here" : "=s"(pc), "=s"(len));
    const unsigned o = (unsigned)(((idx & 3) * 8 + w) * 1024 + 16 * l);
    glds1((const void*)((pc & ~15ull) + (o < len ? o : 0u)), lds0 + L_DUMP + w * 1024);
  }
  const int role = cw ? 3 - rg : rg, rj = (role >> 1) | (cw << 1), un = 4 * bid + rj;
  const bool retile = bid < 224, rtW1 = (role & 1) == 0;
  if (retile) {
    if (rtW1) {
      const size_t o1 = (size_t)(16 * (un & 31) + (l >> 3)) * FF + 64 * (un >> 5) + 8 * (l & 7);
      glds1(p.wg + o1, lds0 + L_RT + rj * 4096);
      glds1(p.wg + o1 + 8 * FF, lds0 + L_RT + rj * 4096 + 1024);
      glds1(p.wu + o1, lds0 + L_RT + rj * 4096 + 2048);
      glds1(p.wu + o1 + 8 * FF, lds0 + L_RT + rj * 4096 + 3072);
    } else {
      const size_t o2 = (size_t)(32 * (un >> 4) + 16 * (un & 1) + (l >> 3)) * DM + 64 * ((un >> 1) & 7) + 8 * (l & 7);
      glds1(p.wd + o2, lds0 + L_RT + 16384 + rj * 2048);
      glds1(p.wd + o2 + 8 * DM, lds0 + L_RT + 16384 + rj * 2048 + 1024);
    }
  }
  FENCE();
  TA(1);
  if (tid < 16) *(LAS unsigned*)(L + L_CNT + 4 * tid) = 0u;
  if (tid < 64) *(LAS v4u*)(L + L_ST2 + 16 * tid) = v4u{0u, 0u, 0u, 0u};
  if (tid >= 64 && tid < 124) { const int i = tid - 64; *(LAS v4u*)(L + L_U + (i / 30) * U_CH + (i % 30) * 16) = v4u{0u, 0u, 0u, 0u}; }
  if (tid >= 128 && tid < 144) { const int i = tid - 128; *(LAS v4u*)(L + L_KX + (i >> 1) * KX_CP + 4096 + (i & 1) * 16) = v4u{0u, 0u, 0u, 0u}; }
  if (tid >= 192 && tid < 194) *(LAS v4u*)(L + L_KC + (tid - 192) * KC_CH) = v4u{0u, 0u, 0u, 0u};
  unsigned eo[11];
#pragma unroll
  for (int j = 0; j < 11; ++j) {
    const int li = 2047 - 8 * (tid & 255) - j;
    const int cf = (int)(pkbf((float)li, 0.f) & 0xffffu) - 17024;
    eo[j] = lds0 + L_KC + (tid >> 8) * KC_CH + 16 + 2 * (li < 128 ? li : cf);
    asm volatile("" : "+v"(eo[j]));
  }
  FENCE();
  TA(2);
  const unsigned tag = __builtin_amdgcn_readfirstlane(ep) + 1u;
  const v2u tag2 = {tag, tag};
  if (STAMP == 2) { unsigned tt = tag; asm volatile("" : "+s"(tt)); }
  TA(3);
  {
    const bool odd = (l >> 3) & 1;
    const unsigned r0 = dppu<0x128>(odd ? xblk.x : xblk.z), r1 = dppu<0x128>(odd ? xblk.y : xblk.w);
    const int o = WS_XT + (32 * w + 4 * (l & 7) + (odd ? 2 : 0)) * XT_CP + (4 * bid + (l >> 4)) * 16;
    st16(v4u{odd ? r0 : xblk.x, tag, odd ? xblk.z : r0, tag}, rs, o);
    st16(v4u{odd ? r1 : xblk.y, tag, odd ? xblk.w : r1, tag}, rs, o + XT_CP);
  }
  TA(4);
  {
    float v[8];
#pragma unroll
    for (int j = 0; j < 4; ++j) { v[2 * j] = bflo(xrow[j]); v[2 * j + 1] = bfhi(xrow[j]); }
    float s = ((v[0] + v[1]) + (v[2] + v[3])) + ((v[4] + v[5]) + (v[6] + v[7]));
    const float mean = wave_sum(s) * (1.f / DM);
    float q = 0.f;
#pragma unroll
    for (int j = 0; j < 8; ++j) { const float d = v[j] - mean; q += d * d; }
    const float var = rbf(wave_sum(q) * (1.f / DM));
    const float sd = __builtin_sqrtf(rbf(var + 1e-5f));
    st8(v2u{pkbf(mean, sd), tag}, rs, l == 0 ? WS_ST1 + (8 * bid + w) * 8 : 0x7ffffff0);
  }
  FENCE();
  TS(1);
  TA(5);
  {
    const float dt = rbf(expf(cw ? bfhi(pdt) : bflo(pdt)));
    const float LOG2E = 1.4426950408889634f, INV2PI = 0.15915494309189535f;
    const f32x2 a2 = {-rbf(expf(bflo(pA))) * dt * LOG2E, -rbf(expf(bfhi(pA))) * dt * LOG2E};
    const f32x2 bt = {bflo(pB) * dt * INV2PI, bfhi(pB) * dt * INV2PI};
    lptr t = L + L_T + cw * T_CH + 8 * l;
    const C2 one = {f32x2{1.f, 1.f}, f32x2{0.f, 0.f}};
    if (role == 0) {
      const C2 e1 = cexp2(a2, bt, 1.f), e128 = cexp2(a2, bt, 128.f);
      C2 v = e1;
      put(t, 0, one); put(t, 8, e128);
#pragma unroll
      for (int i = 1; i < 8; ++i) {
        put(t, i, v); put(t, 8 + i, cmul(v, e128));
        if (i < 7) v = cmul(v, e1);
      }
    } else if (role == 1) {
      const C2 e2 = cexp2(a2, bt, 2.f), e128 = cexp2(a2, bt, 128.f), e256 = cexp2(a2, bt, 256.f);
      const C2 e4 = cmul(e2, e2), e6 = cmul(e4, e2), e384 = cmul(e256, e128);
      put(t, 16, e256); put(t, 17, cmul(e256, e2)); put(t, 18, cmul(e256, e4)); put(t, 19, cmul(e256, e6));
      put(t, 20, e384); put(t, 21, cmul(e384, e2)); put(t, 22, cmul(e384, e4)); put(t, 23, cmul(e384, e6));
      C2 v = cmul(e256, e256);
#pragma unroll
      for (int j = 0; j < 4; ++j) {
        put(t, 24 + 2 * j, v); put(t, 25 + 2 * j, cmul(v, e4));
        if (j < 3) v = cmul(v, e128);
      }
    } else if (role == 2) {
      const C2 e128 = cexp2(a2, bt, 128.f);
      C2 v = cexp2(a2, bt, 1024.f);
#pragma unroll
      for (int i = 0; i < 9; ++i) {
        put(t, 32 + i, v);
        if (i < 8) v = cmul(v, e128);
      }
    } else {
      const f32x2 cr = {bflo(pCr) * dt, bfhi(pCr) * dt}, ci = {bflo(pCi) * dt, bfhi(pCi) * dt};
      const float mx = wave_max(fmaxf(fabsf(cr.x) + fabsf(ci.x), fabsf(cr.y) + fabsf(ci.y)));
      int es = 258 - (int)(asu(mx) >> 23);
      es = es > 220 ? 220 : (es < 34 ? 34 : es);
      const float S2 = asf((unsigned)(es + 1) << 23);
      if (l == 0) *(LAS unsigned*)(L + L_CNT + 32 + 4 * cw) = (unsigned)(254 - es) << 23;
      const C2 e8 = cexp2(a2, -bt, 8.f), e64 = cexp2(a2, -bt, 64.f);
      C2 v = {cr * S2, ci * -S2};
      C2 v2 = cmul(v, e64);
#pragma unroll
      for (int j = 0; j < 8; ++j) {
        put(t, 48 + j, v); put(t, 56 + j, v2);
        if (j < 7) { v = cmul(v, e8); v2 = cmul(v2, e8); }
      }
    }
  }
  FENCE();
  TS(2);
  TA(6);
  VMWAIT(3);
  TA(7);
  if (retile) {
    if (rtW1) {
      lptr a0 = L + L_RT + rj * 4096 + (g4 & 1) * 2048 + (8 * (g4 >> 1) + q4) * 128 + 8 * p4;
      const int o = WS_W1F + ((4 * (un >> 5)) * 32 + (un & 31)) * 1024 + 16 * l;
#pragma unroll
      for (int f = 0; f < 4; ++f) st16(cat(trd(a0 + 32 * f), trd(a0 + 32 * f + 512)), rs, o + f * 32768);
    } else {
      lptr a1 = L + L_RT + 16384 + rj * 2048 + (4 * (g4 & 1) + q4) * 128 + 32 * (g4 >> 1) + 8 * p4;
      const int o = WS_WDF + ((((un >> 1) & 7) * 56 + (un >> 4)) * 4 + (g4 >> 1)) * 1024 + ((un & 1) * 32 + (l & 31)) * 16;
#pragma unroll
      for (int f = 0; f < 2; ++f) st16(cat(trd(a1 + 64 * f), trd(a1 + 64 * f + 1024)), rs, o + f * 2048);
    }
  }
  FENCE();
  TS(3);
  TA(8);
  const int t0 = 256 * w + 16 * n16 + 4 * g4;
  const int gxo = WS_XT + cp * XT_CP + 8 * t0, gso = WS_ST1 + 8 * t0;
  BAR();
  TA(9);
  v4u xa = ld16(rs, gxo, 0), xb = ld16(rs, gxo + 16, 0), s0 = ld16(rs, gso, 0), s1 = ld16(rs, gso + 16, 0);
  FENCE();
  TA(10);
  if (role < 3) {
    lptr ta = L + L_T + cw * T_CH + (16 * role + n16) * T_ROW + 16 * g4, tb = L + L_T + cw * T_CH + (48 + n16) * T_ROW + 16 * g4;
    f32x4 acc = {0.f, 0.f, 0.f, 0.f};
#pragma unroll
    for (int ks = 0; ks < 8; ++ks) acc = mfma16h(*(LAS v4u*)(ta + 64 * ks), *(LAS v4u*)(tb + 64 * ks), acc);
    if (STAMP == 2) { asm volatile("" : "+v"(acc)); }
    TA(11);
#pragma unroll
    for (int r = 0; r < 4; ++r) {
      const int i = 4 * g4 + r;
      int ci = 512 + 16 * i + n16;
      if (role == 0) ci = 128 * (i >> 3) + (i & 7) + 8 * n16;
      if (role == 1) ci = i < 8 ? 256 + 64 * (i >> 2) + (i & 3) + 4 * n16 : 384 + 32 * ((i - 8) >> 1) + (i & 1) + 2 * n16;
      if (ci <= 640) *(LAS _Float16*)(L + L_KC + cw * KC_CH + 16 + 2 * ci) = (_Float16)acc[r];
    }
  }
  TA(12);
  BAR();
  TS(4);
  TA(13);
  {
    unsigned e[12];
#pragma unroll
    for (int j = 0; j < 11; ++j) e[j] = *(LAS u16*)(uintptr_t)eo[j];
    e[11] = 0;
    unsigned P[6], Q[5];
#pragma unroll
    for (int j = 0; j < 6; ++j) P[j] = e[2 * j] | (e[2 * j + 1] << 16);
#pragma unroll
    for (int j = 0; j < 5; ++j) Q[j] = __builtin_amdgcn_alignbit(P[j + 1], P[j], 16);
    lptr kx = L + L_KX + (tid >> 8) * KX_CH + 16 * (tid & 255);
    *(LAS v4u*)(kx) = v4u{P[0], P[1], P[2], P[3]};
    *(LAS v4u*)(kx + KX_CP) = v4u{Q[0], Q[1], Q[2], Q[3]};
    *(LAS v4u*)(kx + 2 * KX_CP) = v4u{P[1], P[2], P[3], P[4]};
    *(LAS v4u*)(kx + 3 * KX_CP) = v4u{Q[1], Q[2], Q[3], Q[4]};
  }
  FENCE();
  TS(5);
  TA(14);
  VMWAIT(4);
  TA(15);
  st8(tag2, rs, (retile && l == 0) ? (rtW1 ? WS_F2 + un * 8 : WS_F3 + ((((un >> 1) & 7) * 56 + (un >> 4)) * 2 + (un & 1)) * 8) : 0x7ffffff0);
#pragma unroll 1
  for (int it = 0; it < SPIN_CAP; ++it) {
    const bool ok = xa.y == tag && xa.w == tag && xb.y == tag && xb.w == tag && s0.y == tag && s0.w == tag && s1.y == tag && s1.w == tag;
    if (__all(ok)) break;
    asm volatile("" ::: "memory");
    xa = ld16(rs, gxo, 0); xb = ld16(rs, gxo + 16, 0); s0 = ld16(rs, gso, 0); s1 = ld16(rs, gso + 16, 0);
  }
  FENCE();
  TS(6);
  TA(16);
  const int rb = (xcd & 1) * 8 + (idx >> 2), cb = (xcd >> 1) * 4 + (idx & 3);
  const int fgW = 7 * cb + (w > 0 ? w - 1 : 0);
  const int f2o = WS_F2 + ((fgW >> 2) * 32 + (l & 31)) * 8;
  const v2u f2s = ld8(rs, f2o, 0);
  FENCE();
  const unsigned xs[4] = {xa.x, xa.z, xb.x, xb.z};
  unsigned ud[4];
  {
    const unsigned st[4] = {s0.x, s0.z, s1.x, s1.z};
    const float d0 = bflo(pds), d1 = bfhi(pds);
    float u0[4], u1[4];
#pragma unroll
    for (int i = 0; i < 4; ++i) {
      const float m = bflo(st[i]), rsd = __builtin_amdgcn_rcpf(bfhi(st[i]));
      const unsigned c = pkbf(bflo(xs[i]) - m, bfhi(xs[i]) - m);
      const unsigned u = pkbf(bflo(c) * rsd, bfhi(c) * rsd);
      u0[i] = bflo(u); u1[i] = bfhi(u);
      ud[i] = pkbf(u0[i] * d0, u1[i] * d1);
    }
    *(LAS v2u*)(L + L_U + 480 + 2 * t0) = v2u{pkh(u0[0], u0[1]), pkh(u0[2], u0[3])};
    *(LAS v2u*)(L + L_U + U_CH + 480 + 2 * t0) = v2u{pkh(u1[0], u1[1]), pkh(u1[2], u1[3])};
  }
  TA(17);
  BAR();
  TS(7);
  TA(18);
  st4(tag, rs, (bid == 0 && tid == 0) ? WS_EPOCH : 0x7ffffff0);
  lptr kA = L + L_KX + cw * KX_CH + (3 - (n16 & 3)) * KX_CP + 2 * (12 - 4 * (n16 >> 2) + 8 * g4) + 64 * (6 - 2 * rg);
  lptr uB = L + L_U + cw * U_CH + 32 * n16 + 16 * g4 + 384 - 128 * rg;
#define AFRAG(a, rr) cat(*(LAS v2u*)(kA + 64 * (57 - 8 * (a) - (rr))), *(LAS v2u*)(kA + 64 * (57 - 8 * (a) - (rr)) + 8))
#define BFRAG(b, rr) (*(LAS v4u*)(uB + 512 * (b) + 64 * (1 - (rr))))
  const f32x2 invS = *(LAS f32x2*)(L + L_CNT + 32);
  lptr redw = L + L_RED + w * 1024 + 16 * l;
  const int wvo = w > 0 ? 16 * l : 0x7ffffff0;
  const int wso = WS_W1F + fgW * 32768;
  const int f4w = l < 4 ? WS_F4 + l * 16384 + (w * 256 + cp) * 8 : 0x7ffffff0;
  v4u wa[32];
  auto conv_out = [&]() {
    lptr rr = L + L_RED + (w < 6 ? w : w - 6) * 8192 + 16 * l;
    const f32x4 y0 = (*(LAS f32x4*)(rr) + *(LAS f32x4*)(rr + 1024)) + (*(LAS f32x4*)(rr + 2048) + *(LAS f32x4*)(rr + 3072));
    const f32x4 y1 = (*(LAS f32x4*)(rr + 4096) + *(LAS f32x4*)(rr + 5120)) + (*(LAS f32x4*)(rr + 6144) + *(LAS f32x4*)(rr + 7168));
    float h0[4], h1[4];
#pragma unroll
    for (int i = 0; i < 4; ++i) {
      const unsigned y = pkbf(y0[i] * invS.x, y1[i] * invS.y);
      const unsigned s = pkbf(bflo(y) + bflo(ud[i]), bfhi(y) + bfhi(ud[i]));
      h0[i] = bflo(xs[i]) + bflo(s); h1[i] = bfhi(xs[i]) + bfhi(s);
    }
    st16(v4u{pkbf(h0[0], h0[1]), pkbf(h0[2], h0[3]), pkbf(h1[0], h1[1]), pkbf(h1[2], h1[3])}, rs,
         WS_HT + (t0 >> 5) * 32768 + cp * 128 + (t0 & 31) * 4);
  };
  {
    f32x4 acc[2] = {{0.f, 0.f, 0.f, 0.f}, {0.f, 0.f, 0.f, 0.f}};
#pragma unroll
    for (int rr = 0; rr < 2; ++rr) {
      const v4u b0 = BFRAG(0, rr), b1 = BFRAG(1, rr), a0 = AFRAG(0, rr), a1 = AFRAG(1, rr);
      acc[0] = mfma16h(a0, b0, acc[0]); acc[1] = mfma16h(a0, b1, acc[1]); acc[1] = mfma16h(a1, b0, acc[1]);
    }
    *(LAS f32x4*)(redw) = acc[0]; *(LAS f32x4*)(redw + 8192) = acc[1];
  }
  TA(19);
  BAR();
  TA(20);
  if (w < 2) conv_out();
  FENCE();
  TA(21);
  wait8(rs, f2o, f2s, tag);
  TS(8);
  TA(22);
  {
    f32x4 acc[2] = {{0.f, 0.f, 0.f, 0.f}, {0.f, 0.f, 0.f, 0.f}};
#pragma unroll
    for (int rr = 0; rr < 2; ++rr) {
      v4u bf[4];
#pragma unroll
      for (int b = 0; b < 4; ++b) bf[b] = BFRAG(b, rr);
#pragma unroll
      for (int a = 0; a < 4; ++a) {
        const v4u af = AFRAG(a, rr);
        if (a < 3) { acc[0] = mfma16h(af, bf[2 - a], acc[0]); wa[3 * rr + a] = ld16(rs, wvo, wso + (3 * rr + a) * 1024); }
        acc[1] = mfma16h(af, bf[3 - a], acc[1]);
      }
    }
    *(LAS f32x4*)(redw + 16384) = acc[0]; *(LAS f32x4*)(redw + 24576) = acc[1];
  }
  TA(23);
  BAR();
  TA(24);
  if (w < 2) { VMWAIT(6); st8(tag2, rs, f4w); }
  else if (w < 4) conv_out();
  FENCE();
  TS(9);
  TA(25);
  const int qh = xcd & 1;
  const int f4r = WS_F4 + (xcd >> 1) * 16384 + qh * 2048;
  v2u f4s;
  {
    f32x4 acc[4];
#pragma unroll
    for (int t = 0; t < 4; ++t) acc[t] = f32x4{0.f, 0.f, 0.f, 0.f};
#pragma unroll
    for (int rr = 0; rr < 2; ++rr) {
      v4u bf[8];
#pragma unroll
      for (int b = 0; b < 8; ++b) bf[b] = BFRAG(b, rr);
#pragma unroll
      for (int a = 0; a < 8; ++a) {
        const v4u af = AFRAG(a, rr);
#pragma unroll
        for (int b = 0; b < 8; ++b)
          if (a + b >= 4 && a + b < 8) acc[a + b - 4] = mfma16h(af, bf[b], acc[a + b - 4]);
        if (a < 6) wa[6 + 6 * rr + a] = ld16(rs, wvo, wso + (6 + 6 * rr + a) * 1024);
      }
      if (rr == 0) {
        FENCE();
        if (w == 2 || w == 3) { VMWAIT(6); st8(tag2, rs, f4w); }
        FENCE();
      }
    }
    FENCE();
    f4s = ld8(rs, f4r + (64 * rg + l) * 8, 0);
    *(LAS f32x4*)(redw + 32768) = acc[0]; *(LAS f32x4*)(redw + 40960) = acc[1];
    *(LAS f32x4*)(redw) = acc[2]; *(LAS f32x4*)(redw + 8192) = acc[3];
  }
  TA(26);
  BAR();
  TS(10);
  TA(27);
  const unsigned char* htg = p.ws + WS_HT + (size_t)rb * 32768 + 16 * l;
  if (w < 4) {
    wait8(rs, f4r + (64 * rg + l) * 8, f4s, tag);
    glds4_sc1(htg + w * 8192, lds0 + w * 8192);
    glds4_sc1(htg + w * 8192 + 4096, lds0 + w * 8192 + 4096);
  } else {
    conv_out();
    asm volatile("s_waitcnt lgkmcnt(0)" ::: "memory");
    lds_arrive(L, 4);
  }
  FENCE();
#pragma unroll
  for (int ks = 18; ks < 22; ++ks) wa[ks] = ld16(rs, wvo, wso + ks * 1024);
  FENCE();
  TS(11);
  VMWAIT(4);
  if (w < 4) lds_arrive(L, 0);
  else st8(tag2, rs, f4w);
  FENCE();
  TS(12);
  const int trb = (4 * (g4 >> 1) + (q4 >> 1)) * 128 + (4 * (g4 & 1) + p4) * 16 + (q4 & 1) * 8;
  const int g2r = xcd * 4 + (idx >> 3), db = idx & 7, tbo = g2r >> 1, th = g2r & 1;
  const int f3o = WS_F3 + ((db * 56 + 7 * w) * 2 + (n16 < 14 ? n16 : 0)) * 8;
  const int wdo = WS_WDF + (db * 56 + 7 * w) * 4096;
  v4u wd[28];
#pragma unroll
  for (int i = 0; i < 16; ++i) wd[12 + i] = wa[i];
  if (w > 0) {
    v2u f3s;
#pragma unroll
    for (int s = 0; s < 4; ++s) {
      lds_wait(L, s, s == 0 ? 4 : 1);
      if (s == 0) TS(13);
      lptr bt = L + s * 32768 + trb;
      f32x16 acc;
#pragma unroll
      for (int r = 0; r < 16; ++r) acc[r] = 0.f;
      float cs0 = 0.f, cs1 = 0.f;
#pragma unroll
      for (int ks = 0; ks < 32; ++ks) {
        acc = mfma32b(wa[ks], cat(trd(bt + ks * 1024), trd(bt + ks * 1024 + 256)), acc);
        if (s == 0) {
          if (ks < 10) wa[22 + ks] = ld16(rs, wvo, wso + (22 + ks) * 1024);
          if (ks == 10) f3s = ld8(rs, f3o, 0);
          cs0 = dot2acc(wa[ks][0], 0x3f803f80u, cs0); cs1 = dot2acc(wa[ks][1], 0x3f803f80u, cs1);
          cs0 = dot2acc(wa[ks][2], 0x3f803f80u, cs0); cs1 = dot2acc(wa[ks][3], 0x3f803f80u, cs1);
        }
        if ((s == 1 || s == 2) && (ks & 3) == 0 && ks >= 8) wd[6 * (s - 1) + (ks >> 2) - 2] = ld16(rs, 16 * l, wdo + (6 * (s - 1) + (ks >> 2) - 2) * 1024);
        if (s == 3 && ks >= 8 && ks < 24) wd[4 + ks] = ld16(rs, 16 * l, wdo + (4 + ks) * 1024);
        if (s > 0 && ks == 7) {
          FENCE();
          VMWAIT(0);
          st8(tag2, rs, l == 0 ? WS_F6 + ((16 * (s - 1) + rb) * 128 + fgW) * 8 : 0x7ffffff0);
          FENCE();
        }
        if ((ks & 3) == 3) SBAR();
      }
      FENCE();
      if (s == 0) {
        wait8(rs, f3o, f3s, tag);
        const float c = x32_add(cs0 + cs1);
        if (l < 32) *(LAS float*)(L + L_CSW + w * 128 + 4 * l) = c;
      }
      FENCE();
      float csr[16];
#pragma unroll
      for (int i = 0; i < 4; ++i) {
        const f32x4 v = *(volatile LAS f32x4*)(L + L_CSW + w * 128 + 32 * i + 16 * (l >> 5));
        csr[4 * i] = v.x; csr[4 * i + 1] = v.y; csr[4 * i + 2] = v.z; csr[4 * i + 3] = v.w;
      }
      TS(14 + 2 * s);
      f32x2 st;
#pragma unroll 1
      for (int it = 0; it < (1 << 22); ++it) {
        st = *(volatile LAS f32x2*)(L + L_ST2 + s * 256 + 8 * (l & 31));
        if (__all(st.y != 0.f)) break;
        __builtin_amdgcn_s_sleep(1);
      }
      const float nm = -st.x, rstd = st.y;
      v4u hv;
#pragma unroll
      for (int j = 0; j < 4; ++j) {
        const unsigned gp = pkbf(__builtin_fmaf(nm, csr[2 * j], acc[2 * j]) * rstd, __builtin_fmaf(nm, csr[2 * j + 1], acc[2 * j + 1]) * rstd);
        const unsigned up = pkbf(__builtin_fmaf(nm, csr[8 + 2 * j], acc[8 + 2 * j]) * rstd, __builtin_fmaf(nm, csr[9 + 2 * j], acc[9 + 2 * j]) * rstd);
        const float g0 = bflo(gp), g1 = bfhi(gp);
        const float C1 = -2.f * 0.7978845608028654f * 1.4426950408889634f, C2c = C1 * 0.044715f;
        const float e0 = __builtin_amdgcn_exp2f(g0 * __builtin_fmaf(g0 * g0, C2c, C1));
        const float e1 = __builtin_amdgcn_exp2f(g1 * __builtin_fmaf(g1 * g1, C2c, C1));
        const unsigned ge = pkbf(g0 * __builtin_amdgcn_rcpf(1.f + e0), g1 * __builtin_amdgcn_rcpf(1.f + e1));
        hv[j] = pkbf(bflo(ge) * bflo(up), bfhi(ge) * bfhi(up));
      }
      st16(hv, rs, WS_HF + ((16 * s + rb) * 112 + fgW) * 1024 + 16 * l);
      FENCE();
      TS(15 + 2 * s);
    }
  } else {
    auto stats = [&](int s) {
      lptr bt = L + s * 32768 + trb;
      float s1 = 0.f, s2 = 0.f, s1b = 0.f, s2b = 0.f;
#pragma unroll 8
      for (int ks = 0; ks < 32; ++ks) {
        const v2u a = trd(bt + ks * 1024), b = trd(bt + ks * 1024 + 256);
        s1 = dot2acc(a.x, 0x3f803f80u, s1); s2 = dot2acc(a.x, a.x, s2);
        s1b = dot2acc(a.y, 0x3f803f80u, s1b); s2b = dot2acc(a.y, a.y, s2b);
        s1 = dot2acc(b.x, 0x3f803f80u, s1); s2 = dot2acc(b.x, b.x, s2);
        s1b = dot2acc(b.y, 0x3f803f80u, s1b); s2b = dot2acc(b.y, b.y, s2b);
      }
      const float mean = x32_add(s1 + s1b) * (1.f / DM);
      const float var = rbf(fmaxf(x32_add(s2 + s2b) * (1.f / DM) - mean * mean, 0.f));
      const float sd = rbf(__builtin_sqrtf(rbf(var + 1e-5f)));
      if (l < 32) *(LAS f32x2*)(L + L_ST2 + s * 256 + 8 * l) = f32x2{rbf(mean), 1.f / sd};
    };
#pragma unroll 1
    for (int s = 0; s < 4; ++s) {
      const int fo = f4r + (s < 3 ? s + 1 : 0) * 4096 + 16 * l;
      v4u fa = ld16(rs, fo, 0), fb = ld16(rs, fo + 1024, 0);
      FENCE();
      if (s == 0) lds_wait(L, 0, 4);
      else { VMWAIT(2); lds_arrive(L, s); }
      if (s == 0) stats(0);
      if (s < 3) {
#pragma unroll 1
        for (int it = 0; it < SPIN_CAP; ++it) {
          if (__all(ok16(fa, tag) && ok16(fb, tag))) break;
          asm volatile("" ::: "memory");
          fa = ld16(rs, fo, 0); fb = ld16(rs, fo + 1024, 0);
        }
        lds_wait(L, 4, 4);
#pragma unroll 1
        for (int j = 0; j < 8; ++j) glds4_sc1(htg + (size_t)(s + 1) * 16 * 32768 + j * 4096, lds0 + (s + 1) * 32768 + j * 4096);
      }
      TS(14 + 2 * s);
      if (s > 0) stats(s);
      TS(15 + 2 * s);
    }
    wait8(rs, f3o, ld8(rs, f3o, 0), tag);
  }
  FENCE();
  int wq = w;
  asm volatile("" : "+s"(wq));
  if (wq == 0) {
#pragma unroll
    for (int j = 0; j < 28; ++j) wd[j] = ld16(rs, 16 * l, wdo + j * 1024);
  }
  FENCE();
  {
    const int f6o = WS_F6 + ((16 * g4 + tbo) * 128 + 14 * w + (n16 < 14 ? n16 : 0)) * 8;
    v2u f6s = ld8(rs, f6o, 0);
    FENCE();
    VMWAIT(1);
    st8(tag2, rs, (l == 0 && w > 0) ? WS_F6 + ((48 + rb) * 128 + fgW) * 8 : 0x7ffffff0);
    FENCE();
    TS(22);
    const int hvo = (g4 >> 1) * 1024 + ((g4 & 1) * 32 + th * 16 + n16) * 16;
    const int fw = w & 3, rch = 64 * db + 16 * fw + (l >> 2);
    const int hro = WS_HT + tbo * 32768 + (rch >> 1) * 128 + (4 * th + p4) * 16 + (rch & 1) * 8;
#pragma unroll 1
    for (int it = 0; it < SPIN_CAP; ++it) {
      if (__all(ok8(f6s, tag) || g4 == 3)) break;
      asm volatile("" ::: "memory");
      f6s = ld8(rs, f6o, 0);
    }
    FENCE();
    v4u hb[4][7];
    v2u hres[2];
#pragma unroll
    for (int s = 0; s < 3; ++s)
#pragma unroll
      for (int ks = 0; ks < 7; ++ks) hb[s][ks] = ld16(rs, hvo, WS_HF + ((16 * s + tbo) * 112 + 14 * w + 2 * ks) * 1024);
    hres[0] = ld8(rs, hro, (w >> 2) * 16 * 32768);
    hres[1] = ld8(rs, hro, ((w >> 2) + 2) * 16 * 32768);
    FENCE();
    TS(23);
#pragma unroll
    for (int s = 0; s < 4; ++s) {
      f32x4 acc[4];
#pragma unroll
      for (int dt = 0; dt < 4; ++dt) acc[dt] = f32x4{0.f, 0.f, 0.f, 0.f};
#pragma unroll
      for (int ks = 0; ks < 7; ++ks)
#pragma unroll
        for (int dt = 0; dt < 4; ++dt) acc[dt] = mfma16b(wd[4 * ks + dt], hb[s][ks], acc[dt]);
      FENCE();
      if (s == 0) {
        f6s = ld8(rs, f6o, 0);
        BAR();
      }
      if (s == 1) {
        wait8(rs, f6o, f6s, tag);
#pragma unroll
        for (int ks = 0; ks < 7; ++ks) hb[3][ks] = ld16(rs, hvo, WS_HF + ((48 + tbo) * 112 + 14 * w + 2 * ks) * 1024);
        FENCE();
      }
      const bool fin = (w >> 2) == (s & 1);
      lptr red = L + L_RED2 + (s & 1) * 32768 + 16 * l;
#pragma unroll
      for (int dt = 0; dt < 4; ++dt) *(LAS f32x4*)(red + w * 4096 + dt * 1024) = acc[dt];
      if (fin) *(LAS v2u*)(L + L_HX + w * 512 + 8 * l) = hres[s >> 1];
      BAR();
      if (fin) {
        const v2u ht = trd(L + L_HX + w * 512 + 8 * l);
        f32x4 sum = *(LAS f32x4*)(red + fw * 1024);
#pragma unroll
        for (int v = 1; v < 8; ++v) sum += *(LAS f32x4*)(red + v * 4096 + fw * 1024);
        const unsigned a0 = pkbf(sum.x, sum.y), a1 = pkbf(sum.z, sum.w);
        const v2u o = {pkbf(bflo(ht.x) + bflo(a0), bfhi(ht.x) + bfhi(a0)), pkbf(bflo(ht.y) + bflo(a1), bfhi(ht.y) + bfhi(a1))};
        *(GAS v2u*)(p.out + (size_t)(512 * s + 16 * g2r + n16) * DM + 64 * db + 16 * fw + 4 * g4) = o;
      }
      TS(24 + s);
    }
  }
  asm volatile(".Lcode_end:");
}

extern "C" void kernel_launch(void* const* d_in, const int* in_sizes, int n_in, void* d_out, int out_size, void* d_ws,
                              size_t ws_size, hipStream_t stream) {
  Params p;
  p.x = (const u16*)d_in[0]; p.logA = (const u16*)d_in[1]; p.aim = (const u16*)d_in[2]; p.cre = (const u16*)d_in[3];
  p.cim = (const u16*)d_in[4]; p.logdt = (const u16*)d_in[5]; p.dskip = (const u16*)d_in[6];
  p.wg = (const u16*)d_in[8]; p.wu = (const u16*)d_in[9]; p.wd = (const u16*)d_in[10];
  p.out = (u16*)d_out; p.ws = (unsigned char*)d_ws;
  (void)hipFuncSetAttribute((const void*)fused_kernel, hipFuncAttributeMaxDynamicSharedMemorySize, L_BYTES);
  fused_kernel<<<dim3(256), dim3(512), L_BYTES, stream>>>(p);
}
```

```cpp
#include <hip/hip_runtime.h>
#include <stdint.h>

typedef unsigned short u16;
typedef unsigned long long u64;
typedef unsigned v4u __attribute__((ext_vector_type(4)));
typedef unsigned v2u __attribute__((ext_vector_type(2)));
typedef float f32x2 __attribute__((ext_vector_type(2)));
typedef float f32x4 __attribute__((ext_vector_type(4)));
typedef float f32x16 __attribute__((ext_vector_type(16)));
typedef short s16x4 __attribute__((ext_vector_type(4)));
typedef _Float16 f16x8 __attribute__((ext_vector_type(8)));
typedef _Float16 f16x2 __attribute__((ext_vector_type(2)));
typedef __bf16 b16x8 __attribute__((ext_vector_type(8)));
typedef __bf16 b16x2 __attribute__((ext_vector_type(2)));
#define LAS __attribute__((address_space(3)))
#define GAS __attribute__((address_space(1)))
typedef LAS unsigned char* lptr;

#define CAS __attribute__((address_space(4)))

#define STAMP 0

constexpr int SEQ = 2048, DM = 512, NMODE = 128, FF = 1792;
constexpr int SC1 = 16;
constexpr int SPIN_CAP = 20000;
constexpr int OOB = 0x40000000;
constexpr unsigned SENT = 0xffffffffu;

constexpr int WS_EPOCH = 0;
constexpr int WS_ST1 = 4096;
constexpr int WS_F2 = 65536;
constexpr int WS_HT = 1 << 20;
constexpr int WS_W1F = 3 << 20;
constexpr int WS_WDF = 7 << 20;
constexpr int WS_HF = 10 << 20;
constexpr int WS_XT = 17 << 20;
constexpr int XT_CP = 16384 + 256;
constexpr int WS_DBG = 22 << 20;
constexpr int WS_BYTES = 24 << 20;
static_assert(WS_XT + 256 * XT_CP <= WS_DBG, "ws map");

constexpr int L_RT = 0, L_CSP = 24576, L_DUMP = 28672;
constexpr int KX_CP = 4160, KX_CH = 4 * KX_CP;
constexpr int L_KX0 = 32768, L_U = L_KX0 + KX_CH, U_CH = 4576, L_KC = L_U + 2 * U_CH, KC_CH = 1312;
constexpr int L_T = 65536, T_ROW = 528, T_B = 41, T_CH = 57 * T_ROW;
constexpr int L_RED = 65536;
constexpr int L_KX1 = 131072;
constexpr int L_CNT = L_KX1 + KX_CH;
constexpr int L_ST2 = L_CNT + 64;
constexpr int L_CSW = L_ST2 + 2048;
constexpr int L_HX = L_CSW + 1024;
constexpr int L_STAMP = L_HX + 4096;
constexpr int L_BYTES = L_STAMP + 1024;
static_assert(L_BYTES <= 163840, "lds");
static_assert(L_KC + 2 * KC_CH <= 65536 && 2 * T_CH <= 65536, "lds");
static_assert(L_U % 16 == 0 && L_KC % 16 == 0 && L_CNT % 16 == 0 && T_CH % 16 == 0, "align");
enum { C_S1 = 0, C_S2, C_S3, C_W1, C_T0, C_T1, C_T2, C_T3, C_M3, C_P0, C_P1, C_P2, C_P3 };

struct Params {
  const u16 *x, *logA, *aim, *cre, *cim, *logdt, *dskip, *wg, *wu, *wd;
  u16* out;
  unsigned char* ws;
};

__device__ __forceinline__ float asf(unsigned v) { return __builtin_bit_cast(float, v); }
__device__ __forceinline__ unsigned asu(float v) { return __builtin_bit_cast(unsigned, v); }
__device__ __forceinline__ float bflo(unsigned v) { return asf(v << 16); }
__device__ __forceinline__ float bfhi(unsigned v) { return asf(v & 0xffff0000u); }
__device__ __forceinline__ unsigned pkbf(float lo, float hi) {
  b16x2 r = {(__bf16)lo, (__bf16)hi};
  return __builtin_bit_cast(unsigned, r);
}
__device__ __forceinline__ unsigned pkbf_s(float lo, float hi) {
  asm("" : "+v"(lo));
  asm("" : "+v"(hi));
  return pkbf(lo, hi);
}
__device__ __forceinline__ float rbf(float f) { return bflo(pkbf(f, f)); }
__device__ __forceinline__ unsigned pkh(float lo, float hi) {
  f16x2 r = {(_Float16)lo, (_Float16)hi};
  return __builtin_bit_cast(unsigned, r);
}
template <int C> __device__ __forceinline__ unsigned dppu(unsigned v) {
  return (unsigned)__builtin_amdgcn_update_dpp(0, (int)v, C, 0xf, 0xf, true);
}
template <int C> __device__ __forceinline__ float dppf(float v) { return asf(dppu<C>(asu(v))); }
__device__ __forceinline__ float x16_add(float v) {
  auto s = __builtin_amdgcn_permlane16_swap(asu(v), asu(v), false, false);
  return asf(s[0]) + asf(s[1]);
}
__device__ __forceinline__ float fold32(float a, float b) {
  auto s = __builtin_amdgcn_permlane32_swap(asu(a), asu(b), false, false);
  return asf(s[0]) + asf(s[1]);
}
__device__ __forceinline__ float wave_max(float v) {
  v = fmaxf(v, dppf<0xB1>(v)); v = fmaxf(v, dppf<0x4E>(v)); v = fmaxf(v, dppf<0x141>(v)); v = fmaxf(v, dppf<0x140>(v));
  auto s = __builtin_amdgcn_permlane16_swap(asu(v), asu(v), false, false);
  v = fmaxf(asf(s[0]), asf(s[1]));
  auto t = __builtin_amdgcn_permlane32_swap(asu(v), asu(v), false, false);
  return fmaxf(asf(t[0]), asf(t[1]));
}
__device__ __forceinline__ float dot2acc(unsigned a, unsigned b, float c) {
  return __builtin_amdgcn_fdot2_f32_bf16(__builtin_bit_cast(b16x2, a), __builtin_bit_cast(b16x2, b), c, false);
}
__device__ __forceinline__ unsigned umax(unsigned a, unsigned b) { return a > b ? a : b; }
__device__ __forceinline__ unsigned umin(unsigned a, unsigned b) { return a < b ? a : b; }
constexpr unsigned ONES = 0x3f803f80u;
#define BAR() asm volatile("s_waitcnt lgkmcnt(0)\n\ts_barrier" ::: "memory")
#define VMWAIT(N) asm volatile("s_waitcnt vmcnt(" #N ")" ::: "memory")
#define SBAR() __builtin_amdgcn_sched_barrier(0)
#define FENCE() do { asm volatile("" ::: "memory"); __builtin_amdgcn_sched_barrier(0); } while (0)
#define PRIO(n) __builtin_amdgcn_s_setprio(n)

__device__ __forceinline__ void glds1(const void* g, unsigned dst) {
  unsigned sv;
  asm volatile("s_mov_b32 %0, m0\n\ts_mov_b32 m0, %2\n\ts_nop 0\n\tglobal_load_lds_dwordx4 %1, off\n\ts_mov_b32 m0, %0"
               : "=&s"(sv) : "v"(g), "s"(dst) : "memory");
}
__device__ __forceinline__ v2u trd(lptr p) {
  return __builtin_bit_cast(v2u, __builtin_amdgcn_ds_read_tr16_b64_v4i16((LAS s16x4*)p));
}
__device__ __forceinline__ v4u cat(v2u a, v2u b) { return v4u{a.x, a.y, b.x, b.y}; }
__device__ __forceinline__ f32x4 mfma16h(v4u a, v4u b, f32x4 c) {
  return __builtin_amdgcn_mfma_f32_16x16x32_f16(__builtin_bit_cast(f16x8, a), __builtin_bit_cast(f16x8, b), c, 0, 0, 0);
}
__device__ __forceinline__ f32x4 mfma16b(v4u a, v4u b, f32x4 c) {
  return __builtin_amdgcn_mfma_f32_16x16x32_bf16(__builtin_bit_cast(b16x8, a), __builtin_bit_cast(b16x8, b), c, 0, 0, 0);
}
__device__ __forceinline__ f32x16 mfma32b(v4u a, v4u b, f32x16 c) {
  return __builtin_amdgcn_mfma_f32_32x32x16_bf16(__builtin_bit_cast(b16x8, a), __builtin_bit_cast(b16x8, b), c, 0, 0, 0);
}
typedef __amdgpu_buffer_rsrc_t rsrc_t;
__device__ __forceinline__ v4u ld16(rsrc_t rs, int voff, int soff) { return __builtin_amdgcn_raw_buffer_load_b128(rs, voff, soff, SC1); }
__device__ __forceinline__ v2u ld8(rsrc_t rs, int voff, int soff) { return __builtin_amdgcn_raw_buffer_load_b64(rs, voff, soff, SC1); }
__device__ __forceinline__ void st16(v4u v, rsrc_t rs, int voff, int soff) { __builtin_amdgcn_raw_buffer_store_b128(v, rs, voff + soff, 0, SC1); }
__device__ __forceinline__ void st8(v2u v, rsrc_t rs, int voff, int soff) { __builtin_amdgcn_raw_buffer_store_b64(v, rs, voff, soff, SC1); }
__device__ __forceinline__ void st4(unsigned v, rsrc_t rs, int voff, int soff) { __builtin_amdgcn_raw_buffer_store_b32(v, rs, voff, soff, SC1); }
template <class T> __device__ __forceinline__ T gld(const void* base, unsigned off) { return *(const GAS T*)((const GAS unsigned char*)base + off); }

template <int C> __device__ __forceinline__ void arrive(unsigned cnt) {
  asm volatile("s_waitcnt lgkmcnt(0)\n\ts_mov_b64 exec, 1\n\tds_add_u32 %0, %1 offset:%2\n\ts_mov_b64 exec, -1"
               :: "v"(cnt), "v"(1u), "n"(4 * C) : "memory");
  __builtin_amdgcn_sched_barrier(0);
}
template <int C> __device__ __forceinline__ void await(lptr L, unsigned n) {
#pragma unroll 1
  for (int it = 0; it < (1 << 22); ++it) {
    unsigned v = *(volatile LAS unsigned*)(L + L_CNT + 4 * C);
    if (__builtin_amdgcn_readfirstlane(v) >= n) break;
    __builtin_amdgcn_s_sleep(1);
  }
  asm volatile("" ::: "memory");
}

struct C2 { f32x2 r, i; };
__device__ __forceinline__ C2 cmul(C2 a, C2 b) { return C2{a.r * b.r - a.i * b.i, a.r * b.i + a.i * b.r}; }
__device__ __forceinline__ C2 cexp2(f32x2 a2, f32x2 bt, float p) {
  const float e0 = __builtin_amdgcn_exp2f(a2.x * p), e1 = __builtin_amdgcn_exp2f(a2.y * p);
  const float f0 = __builtin_amdgcn_fractf(bt.x * p), f1 = __builtin_amdgcn_fractf(bt.y * p);
  return C2{f32x2{e0 * __builtin_amdgcn_cosf(f0), e1 * __builtin_amdgcn_cosf(f1)},
            f32x2{e0 * __builtin_amdgcn_sinf(f0), e1 * __builtin_amdgcn_sinf(f1)}};
}
__device__ __forceinline__ void put(lptr t, int row, C2 v) {
  *(LAS v2u*)(t + row * T_ROW) = v2u{pkh(v.r.x, v.i.x), pkh(v.r.y, v.i.y)};
}

#define TS(i) do {} while (0)

__global__ void __launch_bounds__(512, 2) fused_kernel(Params p) {
  extern __shared__ __attribute__((aligned(16))) unsigned char smem[];
  lptr L = (lptr)smem;
  const unsigned lds0 = (unsigned)(uintptr_t)L;
  const int tid = threadIdx.x, l = tid & 63, w = __builtin_amdgcn_readfirstlane(tid >> 6);
  const int bid = blockIdx.x, xcd = bid & 7, idx = bid >> 3;
  const int cp = bid, cw = w >> 2, rg = w & 3, ch = 2 * cp + cw;
  const int g4 = l >> 4, q4 = (l >> 2) & 3, p4 = l & 3, n16 = l & 15;
  const int l16 = 16 * l;
  asm volatile("" :: "s"(p.x), "s"(p.ws));
  rsrc_t rs = __builtin_amdgcn_make_buffer_rsrc((void*)p.ws, (short)0, WS_BYTES, 0x00020000);
  TS(0);

  const unsigned xo = (unsigned)(bid * 8192 + p4 * 2048 + w * 128 + (l >> 2) * 8);
  const v2u xa2 = gld<v2u>(p.x, xo), xb2 = gld<v2u>(p.x, xo + 1024);
  const v4u xrow = gld<v4u>(p.x, (unsigned)(bid * 8192 + w * 1024 + l16));
  FENCE();
  const v4u sent4 = {SENT, SENT, SENT, SENT};
  st16(sent4, rs, l16, WS_HT + bid * 8192 + w * 1024);
  const unsigned po = (unsigned)(ch * 256 + 4 * l);
  const unsigned pA = gld<unsigned>(p.logA, po), pB = gld<unsigned>(p.aim, po);
  const unsigned pCr = gld<unsigned>(p.cre, po), pCi = gld<unsigned>(p.cim, po);
  const unsigned ep = *(const CAS unsigned*)(p.ws + WS_EPOCH);
  const unsigned pdt = *(const CAS unsigned*)(p.logdt + 2 * cp);
  const unsigned pds = *(const CAS unsigned*)(p.dskip + 2 * cp);
  FENCE();
  {
    u64 pc; unsigned len;
    asm volatile("s_getpc_b64 %0\n.Lcode_here:\n\ts_mov_b32 %1, .Lcode_end-.Lcode_here" : "=s"(pc), "=s"(len));
    const unsigned o = (unsigned)(((idx & 3) * 8 + w) * 1024 + l16);
    glds1((const void*)((pc & ~15ull) + (o < len ? o : 0u)), lds0 + L_DUMP);
  }
  FENCE();
  const int role = cw ? 3 - rg : rg;
  const bool retile = bid < 224;
  const int rbid = retile ? bid : 0;
  if (tid < 14) *(LAS unsigned*)(L + L_CNT + 4 * tid) = 0u;
  if (tid >= 256 && tid < 384) *(LAS v4u*)(L + L_ST2 + 16 * (tid - 256)) = v4u{0u, 0u, 0u, 0u};
  if (tid >= 64 && tid < 124) { const int i = tid - 64; *(LAS v4u*)(L + L_U + (i / 30) * U_CH + (i % 30) * 16) = v4u{0u, 0u, 0u, 0u}; }
  if (tid >= 128 && tid < 144) { const int i = tid - 128; *(LAS v4u*)(L + (i < 8 ? L_KX0 : L_KX1 - KX_CH) + (i >> 1) * KX_CP + 4096 + (i & 1) * 16) = v4u{0u, 0u, 0u, 0u}; }
  if (tid >= 192 && tid < 194) *(LAS v4u*)(L + L_KC + (tid - 192) * KC_CH) = v4u{0u, 0u, 0u, 0u};
  unsigned eo[11], kco[4];
#pragma unroll
  for (int j = 0; j < 11; ++j) {
    const int li = 2047 - 8 * (tid & 255) - j;
    const int cf = (int)(pkbf((float)li, 0.f) & 0xffffu) - 17024;
    eo[j] = lds0 + L_KC + cw * KC_CH + 16 + 2 * (li < 128 ? li : cf);
    asm volatile("" : "+v"(eo[j]));
  }
#pragma unroll
  for (int r = 0; r < 4; ++r) {
    const int i = 4 * g4 + r;
    int ci = 512 + 16 * i + n16;
    if (role == 0) ci = 128 * (i >> 3) + (i & 7) + 8 * n16;
    if (role == 1) ci = i < 8 ? 256 + 64 * (i >> 2) + (i & 3) + 4 * n16 : 384 + 32 * ((i - 8) >> 1) + (i & 1) + 2 * n16;
    kco[r] = lds0 + L_KC + cw * KC_CH + 16 + 2 * (ci < 641 ? ci : 641);
    asm volatile("" : "+v"(kco[r]));
  }
  FENCE();
  const int ks1 = 4 * (rbid & 7) + (w >> 1), cg = rbid >> 3;
  const unsigned w1o = (unsigned)((16 * ks1 + (l >> 3)) * (FF * 2) + 128 * cg + 16 * (l & 7));
  const u16* w1p = (w & 1) ? p.wu : p.wg;
  const v4u r1a = gld<v4u>(w1p, w1o), r1b = gld<v4u>(w1p, w1o + 8 * FF * 2);
  FENCE();
  TS(1);
  const unsigned tag = ep + 1u;
  {
    const int o = (l >> 2) * (2 * XT_CP) + p4 * 16, so = WS_XT + 32 * w * XT_CP + bid * 64;
    st16(v4u{xa2.x, tag, xb2.x, tag}, rs, o, so);
    st16(v4u{xa2.y, tag, xb2.y, tag}, rs, o, so + XT_CP);
  }
  FENCE();
  {
    float s = dot2acc(xrow.x, ONES, 0.f), q = dot2acc(xrow.x, xrow.x, 0.f);
    s = dot2acc(xrow.y, ONES, s); q = dot2acc(xrow.y, xrow.y, q);
    s = dot2acc(xrow.z, ONES, s); q = dot2acc(xrow.z, xrow.z, q);
    s = dot2acc(xrow.w, ONES, s); q = dot2acc(xrow.w, xrow.w, q);
    float v = fold32(s, q);
    v += dppf<0xB1>(v); v += dppf<0x4E>(v); v += dppf<0x141>(v); v += dppf<0x140>(v);
    v = x16_add(v) * (1.f / DM);
    const float mean = asf(__builtin_amdgcn_readlane(asu(v), 0));
    const float var = rbf(fmaxf(v - mean * mean, 0.f));
    const float sd = __builtin_amdgcn_sqrtf(rbf(var + 1e-5f));
    const unsigned pk = pkbf(mean, sd);
    FENCE();
    VMWAIT(9);
    st8(v2u{pk, tag}, rs, l == 32 ? 0 : OOB, WS_ST1 + (8 * bid + w) * 8);
  }
  FENCE();
  TS(2);
  const v4u r2 = gld<v4u>(p.wd, (unsigned)((16 * (rbid >> 1) + 8 * (l >> 5) + 4 * (rbid & 1) + ((l >> 3) & 3)) * (DM * 2) + 128 * w + 16 * (l & 7)));
  {
    const int fo = w < 7 ? l16 : OOB;
#pragma unroll
    for (int j = 0; j < 4; ++j) st16(sent4, rs, fo, WS_HF + bid * 28672 + w * 4096 + j * 1024);
  }
  FENCE();
  {
    const float LOG2E = 1.4426950408889634f, INV2PI = 0.15915494309189535f;
    const float dt = rbf(__builtin_amdgcn_exp2f((cw ? bfhi(pdt) : bflo(pdt)) * LOG2E));
    const unsigned ea = pkbf(__builtin_amdgcn_exp2f(bflo(pA) * LOG2E), __builtin_amdgcn_exp2f(bfhi(pA) * LOG2E));
    const f32x2 a2 = {-bflo(ea) * dt * LOG2E, -bfhi(ea) * dt * LOG2E};
    const f32x2 bt = {bflo(pB) * dt * INV2PI, bfhi(pB) * dt * INV2PI};
    lptr t = L + L_T + cw * T_CH + 8 * l;
    const C2 one = {f32x2{1.f, 1.f}, f32x2{0.f, 0.f}};
    if (role == 0) {
      const C2 e1 = cexp2(a2, bt, 1.f), e128 = cexp2(a2, bt, 128.f);
      C2 v = e1;
      put(t, 0, one); put(t, 8, e128);
#pragma unroll
      for (int i = 1; i < 8; ++i) {
        put(t, i, v); put(t, 8 + i, cmul(v, e128));
        if (i < 7) v = cmul(v, e1);
      }
    } else if (role == 1) {
      const C2 e2 = cexp2(a2, bt, 2.f), e128 = cexp2(a2, bt, 128.f), e256 = cexp2(a2, bt, 256.f);
      const C2 e4 = cmul(e2, e2), e6 = cmul(e4, e2), e384 = cmul(e256, e128);
      put(t, 16, e256); put(t, 17, cmul(e256, e2)); put(t, 18, cmul(e256, e4)); put(t, 19, cmul(e256, e6));
      put(t, 20, e384); put(t, 21, cmul(e384, e2)); put(t, 22, cmul(e384, e4)); put(t, 23, cmul(e384, e6));
      C2 v = cmul(e256, e256);
#pragma unroll
      for (int j = 0; j < 4; ++j) {
        put(t, 24 + 2 * j, v); put(t, 25 + 2 * j, cmul(v, e4));
        if (j < 3) v = cmul(v, e128);
      }
    } else if (role == 2) {
      const C2 e128 = cexp2(a2, bt, 128.f);
      C2 v = cexp2(a2, bt, 1024.f);
#pragma unroll
      for (int i = 0; i < 9; ++i) {
        put(t, 32 + i, v);
        if (i < 8) v = cmul(v, e128);
      }
    } else {
      const f32x2 cr = {bflo(pCr) * dt, bfhi(pCr) * dt}, ci = {bflo(pCi) * dt, bfhi(pCi) * dt};
      const float mx = wave_max(fmaxf(fabsf(cr.x) + fabsf(ci.x), fabsf(cr.y) + fabsf(ci.y)));
      int es = 258 - (int)(asu(mx) >> 23);
      es = es > 220 ? 220 : (es < 34 ? 34 : es);
      const float S2 = asf((unsigned)(es + 1) << 23);
      if (l == 0) *(LAS unsigned*)(L + L_CNT + 56 + 4 * cw) = (unsigned)(254 - es) << 23;
      const C2 e8 = cexp2(a2, -bt, 8.f), e64 = cexp2(a2, -bt, 64.f);
      C2 v = {cr * S2, ci * -S2};
      C2 v2 = cmul(v, e64);
#pragma unroll
      for (int j = 0; j < 8; ++j) {
        put(t, T_B + j, v); put(t, T_B + 8 + j, v2);
        if (j < 7) { v = cmul(v, e8); v2 = cmul(v2, e8); }
      }
    }
  }
  FENCE();
  TS(3);
  lptr rt = L + L_RT + w * 3072;
  {
    *(LAS v4u*)(rt + l16) = r1a;
    *(LAS v4u*)(rt + 1024 + l16) = r1b;
    lptr a0 = rt + (8 * (g4 >> 1) + q4) * 128 + 32 * (g4 & 1) + 8 * p4;
    const int o = retile ? (g4 & 1) * 32768 + ((g4 >> 1) * 32 + n16) * 16 : OOB;
    const int so = WS_W1F + (4 * cg * 32 + ks1) * 1024 + (w & 1) * 256;
    lptr cs = L + L_CSP + ((g4 & 1) * 32 + (w & 1) * 16 + n16) * 32 + ((w >> 1) * 2 + (g4 >> 1)) * 4;
#pragma unroll
    for (int f = 0; f < 2; ++f) {
      const v4u fr = cat(trd(a0 + 64 * f), trd(a0 + 64 * f + 512));
      st16(fr, rs, o, so + f * 65536);
      *(LAS float*)(cs + f * 2048) = dot2acc(fr.w, ONES, dot2acc(fr.z, ONES, dot2acc(fr.y, ONES, dot2acc(fr.x, ONES, 0.f))));
    }
  }
  FENCE();
  TS(4);
  BAR();
  if (role < 3) {
    lptr ta = L + L_T + cw * T_CH + (16 * role + n16) * T_ROW + 16 * g4, tb = L + L_T + cw * T_CH + (T_B + n16) * T_ROW + 16 * g4;
    f32x4 acc = {0.f, 0.f, 0.f, 0.f};
#pragma unroll
    for (int ks = 0; ks < 8; ++ks) acc = mfma16h(*(LAS v4u*)(ta + 64 * ks), *(LAS v4u*)(tb + 64 * ks), acc);
#pragma unroll
    for (int r = 0; r < 4; ++r) *(LAS _Float16*)(uintptr_t)kco[r] = (_Float16)acc[r];
  }
  FENCE();
  {
    *(LAS v4u*)(rt + 2048 + l16) = r2;
    lptr a1 = rt + 2048 + q4 * 128 + 32 * g4 + 8 * p4;
    st16(cat(trd(a1), trd(a1 + 512)), rs, retile ? g4 * 1024 + n16 * 16 : OOB,
         WS_WDF + (w * 56 + (rbid >> 2)) * 4096 + (rbid & 3) * 256);
  }
  BAR();
  TS(5);
  {
    unsigned e[12];
#pragma unroll
    for (int j = 0; j < 11; ++j) e[j] = *(LAS u16*)(uintptr_t)eo[j];
    e[11] = 0;
    unsigned P[6], Q[5];
#pragma unroll
    for (int j = 0; j < 6; ++j) P[j] = e[2 * j] | (e[2 * j + 1] << 16);
#pragma unroll
    for (int j = 0; j < 5; ++j) Q[j] = __builtin_amdgcn_alignbit(P[j + 1], P[j], 16);
    lptr kx = L + (cw ? L_KX1 : L_KX0) + 16 * (tid & 255);
    *(LAS v4u*)(kx) = v4u{P[0], P[1], P[2], P[3]};
    *(LAS v4u*)(kx + KX_CP) = v4u{Q[0], Q[1], Q[2], Q[3]};
    *(LAS v4u*)(kx + 2 * KX_CP) = v4u{P[1], P[2], P[3], P[4]};
    *(LAS v4u*)(kx + 3 * KX_CP) = v4u{Q[1], Q[2], Q[3], Q[4]};
  }
  FENCE();
  TS(6);
  const int gxs = WS_XT + cp * XT_CP + 2048 * w, gss = WS_ST1 + 2048 * w;
  v4u xa = ld16(rs, 2 * l16, gxs), xb = ld16(rs, 2 * l16 + 16, gxs), s0 = ld16(rs, 2 * l16, gss), s1 = ld16(rs, 2 * l16 + 16, gss);
  FENCE();
  VMWAIT(5);
  {
    unsigned old = 0;
    if (l == 0) old = __hip_atomic_fetch_add((LAS unsigned*)(L + L_CNT) + C_W1, 1u, __ATOMIC_RELAXED, __HIP_MEMORY_SCOPE_WORKGROUP);
    if (__builtin_amdgcn_readfirstlane(old) == 7 && retile) {
      lptr c = L + L_CSP + 64 * l;
      const f32x4 a = *(LAS f32x4*)(c) + *(LAS f32x4*)(c + 16), b = *(LAS f32x4*)(c + 32) + *(LAS f32x4*)(c + 48);
      st16(v4u{asu((a.x + a.y) + (a.z + a.w)), tag, asu((b.x + b.y) + (b.z + b.w)), tag}, rs, g4 * 2048 + n16 * 16,
           WS_F2 + cg * 8192 + (bid & 7) * 256);
    }
  }
  FENCE();
  TS(7);
  {
    int it = 0;
    while (((xa.y ^ tag) | (xa.w ^ tag) | (xb.y ^ tag) | (xb.w ^ tag) | (s0.y ^ tag) | (s0.w ^ tag) | (s1.y ^ tag) | (s1.w ^ tag)) != 0u && ++it < SPIN_CAP) {
      asm volatile("" ::: "memory");
      xa = ld16(rs, 2 * l16, gxs); xb = ld16(rs, 2 * l16 + 16, gxs); s0 = ld16(rs, 2 * l16, gss); s1 = ld16(rs, 2 * l16 + 16, gss);
    }
  }
  FENCE();
  TS(8);
  const int rb = (xcd & 1) * 8 + (idx >> 2), cb = (xcd >> 1) * 4 + (idx & 3);
  const int fgW = 7 * cb + (w > 0 ? w - 1 : 0);
  v4u f2a = ld16(rs, l16, WS_F2 + fgW * 2048), f2b = ld16(rs, l16, WS_F2 + fgW * 2048 + 1024);
  FENCE();
  float hb0[4], hb1[4];
  {
    const unsigned xs[4] = {xa.x, xa.z, xb.x, xb.z}, st[4] = {s0.x, s0.z, s1.x, s1.z};
    const float d0 = bflo(pds), d1 = bfhi(pds);
    float u0[4], u1[4];
#pragma unroll
    for (int i = 0; i < 4; ++i) {
      const float m = bflo(st[i]), rsd = __builtin_amdgcn_rcpf(bfhi(st[i]));
      const float x0 = bflo(xs[i]), x1 = bfhi(xs[i]);
      u0[i] = (x0 - m) * rsd; u1[i] = (x1 - m) * rsd;
      hb0[i] = __builtin_fmaf(u0[i], d0, x0); hb1[i] = __builtin_fmaf(u1[i], d1, x1);
    }
    lptr uw = L + L_U + 480 + 512 * w + 8 * l;
    *(LAS v2u*)(uw) = v2u{pkh(u0[0], u0[1]), pkh(u0[2], u0[3])};
    *(LAS v2u*)(uw + U_CH) = v2u{pkh(u1[0], u1[1]), pkh(u1[2], u1[3])};
  }
  BAR();
  TS(9);
  st4(tag, rs, (bid == 0 && tid == 0) ? WS_EPOCH : OOB, 0);
  const unsigned cnt = lds0 + L_CNT;
  lptr kA = L + (cw ? L_KX1 : L_KX0) + (3 - (n16 & 3)) * KX_CP + 2 * (12 - 4 * (n16 >> 2) + 8 * g4) + 64 * (6 - 2 * rg);
  lptr uB = L + L_U + cw * U_CH + 32 * n16 + 16 * g4 + 384 - 128 * rg;
#define AFRAG(a, rr) cat(*(LAS v2u*)(kA + 64 * (57 - 8 * (a) - (rr))), *(LAS v2u*)(kA + 64 * (57 - 8 * (a) - (rr)) + 8))
#define BFRAG(b, rr) (*(LAS v4u*)(uB + 512 * (b) + 64 * (1 - (rr))))
  lptr redw = L + L_RED + w * 1024 + 16 * (4 * n16 + (g4 ^ ((n16 >> 1) & 3)));
  const int wvo = w > 0 ? l16 : OOB;
  const int wso = WS_W1F + fgW * 32768;
  v4u wa[32];
  auto conv_out = [&]() {
    const f32x2 invS = *(LAS f32x2*)(L + L_CNT + 56);
    lptr rr = L + L_RED + w * 8192 + 16 * ((l & ~3) + (p4 ^ ((l >> 3) & 3)));
    f32x4 y[8];
#pragma unroll
    for (int v = 0; v < 8; ++v) y[v] = *(LAS f32x4*)(rr + 1024 * v);
    float h0[4], h1[4];
#pragma unroll
    for (int i = 0; i < 4; ++i) {
      h0[i] = __builtin_fmaf(((y[0][i] + y[1][i]) + y[2][i]) + y[3][i], invS.x, hb0[i]);
      h1[i] = __builtin_fmaf(((y[4][i] + y[5][i]) + y[6][i]) + y[7][i], invS.y, hb1[i]);
    }
    st16(v4u{umin(pkbf_s(h0[0], h0[1]), SENT - 1), pkbf_s(h0[2], h0[3]), umin(pkbf_s(h1[0], h1[1]), SENT - 1), pkbf_s(h1[2], h1[3])}, rs,
         (l >> 3) * 32768 + (l & 7) * 16, WS_HT + w * 8 * 32768 + cp * 128);
  };
  v4u ka[2][2], kb[2][2];
  {
    f32x4 acc[2] = {{0.f, 0.f, 0.f, 0.f}, {0.f, 0.f, 0.f, 0.f}};
#pragma unroll
    for (int rr = 0; rr < 2; ++rr) {
      kb[rr][0] = BFRAG(0, rr); ka[rr][0] = AFRAG(0, rr); kb[rr][1] = BFRAG(1, rr); ka[rr][1] = AFRAG(1, rr);
    }
#pragma unroll
    for (int rr = 0; rr < 2; ++rr) {
      acc[0] = mfma16h(ka[rr][0], kb[rr][0], acc[0]); acc[1] = mfma16h(ka[rr][0], kb[rr][1], acc[1]); acc[1] = mfma16h(ka[rr][1], kb[rr][0], acc[1]);
    }
    *(LAS f32x4*)(redw) = acc[0]; *(LAS f32x4*)(redw + 8192) = acc[1];
  }
  FENCE();
  VMWAIT(3);
  arrive<C_S1>(cnt);
  {
    int it = 0;
    while (((f2a.y ^ tag) | (f2a.w ^ tag) | (f2b.y ^ tag) | (f2b.w ^ tag)) != 0u && ++it < SPIN_CAP) {
      asm volatile("" ::: "memory");
      f2a = ld16(rs, l16, WS_F2 + fgW * 2048); f2b = ld16(rs, l16, WS_F2 + fgW * 2048 + 1024);
    }
    FENCE();
#pragma unroll
    for (int ks = 0; ks < 4; ++ks) wa[ks] = ld16(rs, wvo, wso + ks * 1024);
    const float c = x16_add(fold32(asf(f2a.x) + asf(f2b.x), asf(f2a.z) + asf(f2b.z)));
    *(LAS float*)(L + L_CSW + w * 128 + 8 * n16 + 4 * (l >> 5)) = c;
  }
  FENCE();
  TS(10);
  if (w < 2) { await<C_S1>(L, 8); conv_out(); }
  FENCE();
  {
    f32x4 acc[2] = {{0.f, 0.f, 0.f, 0.f}, {0.f, 0.f, 0.f, 0.f}};
#pragma unroll
    for (int rr = 0; rr < 2; ++rr) {
      const v4u b2 = BFRAG(2, rr), b3 = BFRAG(3, rr), a2 = AFRAG(2, rr), a3 = AFRAG(3, rr);
      acc[0] = mfma16h(ka[rr][1], kb[rr][1], acc[0]);
      acc[1] = mfma16h(ka[rr][1], b2, acc[1]);
      acc[0] = mfma16h(ka[rr][0], b2, acc[0]);
      wa[4 + 2 * rr] = ld16(rs, wvo, wso + (4 + 2 * rr) * 1024);
      acc[1] = mfma16h(ka[rr][0], b3, acc[1]);
      acc[0] = mfma16h(a2, kb[rr][0], acc[0]);
      acc[1] = mfma16h(a2, kb[rr][1], acc[1]);
      wa[5 + 2 * rr] = ld16(rs, wvo, wso + (5 + 2 * rr) * 1024);
      acc[1] = mfma16h(a3, kb[rr][0], acc[1]);
    }
    *(LAS f32x4*)(redw + 16384) = acc[0]; *(LAS f32x4*)(redw + 24576) = acc[1];
  }
  FENCE();
  arrive<C_S2>(cnt);
  TS(11);
  const int tso = WS_HT + (16 * cw + rb) * 32768 + rg * 8192;
  v4u tl[8];
  {
    f32x4 acc[4];
#pragma unroll
    for (int t = 0; t < 4; ++t) acc[t] = f32x4{0.f, 0.f, 0.f, 0.f};
#pragma unroll
    for (int rr = 0; rr < 2; ++rr) {
      v4u bf[8];
#pragma unroll
      for (int b = 7; b >= 0; --b) bf[b] = BFRAG(b, rr);
#pragma unroll
      for (int a = 0; a < 8; ++a) {
        const v4u af = AFRAG(a, rr);
#pragma unroll
        for (int b = 7; b >= 0; --b)
          if (a + b >= 4 && a + b < 8) acc[a + b - 4] = mfma16h(af, bf[b], acc[a + b - 4]);
        if (a < 6) wa[8 + 6 * rr + a] = ld16(rs, wvo, wso + (8 + 6 * rr + a) * 1024);
        if (rr == 0 && a == 1) {
          FENCE();
          if (w == 2 || w == 3) { await<C_S2>(L, 8); conv_out(); }
          FENCE();
        }
      }
    }
    FENCE();
    *(LAS f32x4*)(redw + 32768) = acc[0]; *(LAS f32x4*)(redw + 40960) = acc[1];
    *(LAS f32x4*)(redw + 49152) = acc[2]; *(LAS f32x4*)(redw + 57344) = acc[3];
  }
  FENCE();
  arrive<C_S3>(cnt);
  TS(12);
  if (w >= 4) { await<C_S3>(L, 8); conv_out(); }
  FENCE();
#pragma unroll
  for (int j = 0; j < 8; ++j) tl[j] = ld16(rs, l16, tso + j * 1024);
  FENCE();
#pragma unroll
  for (int ks = 20; ks < 32; ++ks) wa[ks] = ld16(rs, wvo, wso + ks * 1024);
  FENCE();
  TS(13);
  {
    int it = 0;
    while (umax(umax(umax(tl[0].x, tl[1].x), umax(tl[2].x, tl[3].x)), umax(umax(tl[4].x, tl[5].x), umax(tl[6].x, tl[7].x))) == SENT && ++it < SPIN_CAP) {
      asm volatile("" ::: "memory");
#pragma unroll
      for (int j = 0; j < 8; ++j) tl[j] = ld16(rs, l16, tso + j * 1024);
    }
    lptr td = L + w * 8192 + l16;
#pragma unroll
    for (int j = 0; j < 8; ++j) *(LAS v4u*)(td + j * 1024) = tl[j];
    arrive<C_T0>(cnt + 4 * cw);
  }
  FENCE();
  TS(14);
  const int g2r = xcd * 4 + (idx >> 3), db = idx & 7, tbo = g2r >> 1, th = g2r & 1;
  const int wdo = WS_WDF + (db * 56 + 7 * w) * 4096;
  const int hvo = (g4 >> 1) * 1024 + ((g4 & 1) * 32 + th * 16 + n16) * 16;
  const int hso = WS_HF + (tbo * 112 + 14 * w) * 1024;
  const int rch = 64 * db + 16 * rg + (l >> 2);
  const int hro = WS_HT + (16 * cw + tbo) * 32768 + (rch >> 1) * 128 + (4 * th + p4) * 16 + (rch & 1) * 8;
#define WDLD(j) ld16(rs, l16, wdo + ((j) >> 2) * 4096 + ((((j) & 3) + rg) & 3) * 1024)
#define HLD(s, ks) ld16(rs, hvo, hso + (s) * (16 * 112 * 1024) + (ks) * 2048)
  v4u wd[28], hb[4][7];
  auto gstats = [&](int s) {
    lptr bt = L + s * 32768 + g4 * 512 + (q4 >> 1) * 128 + p4 * 16 + (q4 & 1) * 8;
    const v4u ones = {ONES, ONES, ONES, ONES};
    f32x4 gq[2] = {{0.f, 0.f, 0.f, 0.f}, {0.f, 0.f, 0.f, 0.f}}, gs[2] = {{0.f, 0.f, 0.f, 0.f}, {0.f, 0.f, 0.f, 0.f}};
    v4u f[8];
#pragma unroll
    for (int i = 0; i < 8; ++i) f[i] = cat(trd(bt + (i >> 1) * 2048 + (i & 1) * 64), trd(bt + (i >> 1) * 2048 + (i & 1) * 64 + 256));
#pragma unroll 1
    for (int k4 = 0; k4 < 4; ++k4) {
      bt += 8192;
      SBAR();
#pragma unroll
      for (int i = 0; i < 8; ++i) {
        gq[i & 1] = mfma16b(f[i], f[i], gq[i & 1]);
        gs[i & 1] = mfma16b(ones, f[i], gs[i & 1]);
        SBAR();
        f[i] = cat(trd(bt + (i >> 1) * 2048 + (i & 1) * 64), trd(bt + (i >> 1) * 2048 + (i & 1) * 64 + 256));
        SBAR();
      }
    }
    const float C1 = -2.f * 0.7978845608028654f * 1.4426950408889634f, C2c = C1 * 0.044715f;
#pragma unroll
    for (int hh = 0; hh < 2; ++hh) {
      const float q = (p4 & 2) ? ((p4 & 1) ? gq[hh][3] : gq[hh][2]) : ((p4 & 1) ? gq[hh][1] : gq[hh][0]);
      const float mean = gs[hh][0] * (1.f / DM);
      const float var = rbf(fmaxf(q * (1.f / DM) - mean * mean, 0.f));
      const float sd = rbf(__builtin_amdgcn_sqrtf(rbf(var + 1e-5f)));
      const float r = __builtin_amdgcn_rcpf(sd);
      if (q4 == g4) *(LAS f32x4*)(L + L_ST2 + s * 512 + hh * 256 + 16 * n16) = f32x4{-rbf(mean), C1 * r, C2c * r * r * r, sd * sd};
    }
  };
  if (w > 0) {
    const int trb = (4 * (g4 >> 1) + (q4 >> 1)) * 128 + (4 * (g4 & 1) + p4) * 16 + (q4 & 1) * 8;
#pragma unroll
    for (int s = 0; s < 4; ++s) {
      if (s == 0) await<C_T0>(L, 4);
      if (s == 1) await<C_T1>(L, 4);
      if (s == 2) await<C_T2>(L, 1);
      if (s == 3) await<C_T3>(L, 1);
      TS(15 + 2 * s);
      PRIO(1);
      lptr bt = L + s * 32768 + trb;
      f32x16 acc;
#pragma unroll
      for (int r = 0; r < 16; ++r) acc[r] = 0.f;
      v4u bf[4];
#pragma unroll
      for (int ks = 0; ks < 3; ++ks) bf[ks] = cat(trd(bt + ks * 1024), trd(bt + ks * 1024 + 256));
      SBAR();
#pragma unroll
      for (int ks = 0; ks < 32; ++ks) {
        if (ks + 3 < 32) bf[(ks + 3) & 3] = cat(trd(bt + (ks + 3) * 1024), trd(bt + (ks + 3) * 1024 + 256));
        acc = mfma32b(wa[ks], bf[ks & 3], acc);
        if ((s == 1 || s == 2) && (ks & 3) == 0 && ks >= 8) wd[6 * (s - 1) + (ks >> 2) - 2] = WDLD(6 * (s - 1) + (ks >> 2) - 2);
        if (s == 3 && ks >= 1 && ks < 17) wd[11 + ks] = WDLD(11 + ks);
        if (s == 3 && ks >= 17 && ks < 31) hb[(ks - 17) / 7][(ks - 17) % 7] = HLD((ks - 17) / 7, (ks - 17) % 7);
        if (ks == 16) PRIO(2);
        SBAR();
      }
      PRIO(0);
      if (s == 3) arrive<C_M3>(cnt);
      FENCE();
      float csr[16];
#pragma unroll
      for (int i = 0; i < 4; ++i) {
        const f32x4 v = *(LAS f32x4*)(L + L_CSW + w * 128 + 32 * i + 16 * (l >> 5));
        csr[4 * i] = v.x; csr[4 * i + 1] = v.y; csr[4 * i + 2] = v.z; csr[4 * i + 3] = v.w;
      }
      f32x4 st;
#pragma unroll 1
      for (int it = 0; it < (1 << 22); ++it) {
        st = *(volatile LAS f32x4*)(L + L_ST2 + s * 512 + 16 * (l & 31));
        if (__all(st.w != 0.f)) break;
        __builtin_amdgcn_s_sleep(1);
      }
      TS(16 + 2 * s);
      float o[8];
#pragma unroll
      for (int j = 0; j < 8; ++j) {
        float g = __builtin_fmaf(st.x, csr[j], acc[j]);
        const float u = __builtin_fmaf(st.x, csr[8 + j], acc[8 + j]);
        asm("" : "+v"(g));
        const float e = __builtin_amdgcn_exp2f(g * __builtin_fmaf(g * g, st.z, st.y));
        o[j] = (g * u) * __builtin_amdgcn_rcpf(__builtin_fmaf(e, st.w, st.w));
      }
      st16(v4u{umin(pkbf_s(o[0], o[1]), SENT - 1), pkbf_s(o[2], o[3]), pkbf_s(o[4], o[5]), pkbf_s(o[6], o[7])}, rs, l16,
           WS_HF + ((16 * s + rb) * 112 + fgW) * 1024);
      FENCE();
    }
  } else {
    PRIO(3);
    await<C_T0>(L, 4);
    TS(15);
    gstats(0);
    TS(16);
    FENCE();
#pragma unroll 1
    for (int s = 2; s < 4; ++s) {
      const int so = WS_HT + (16 * s + rb) * 32768;
#pragma unroll
      for (int j = 0; j < 32; ++j) wa[j] = ld16(rs, l16 + (j & 3) * 1024, so + (j >> 2) * 4096);
      FENCE();
      if (s == 2) await<C_T1>(L, 4);
      gstats(s - 1);
      FENCE();
      auto miss = [&]() {
        unsigned m = wa[0].x;
#pragma unroll
        for (int j = 1; j < 32; ++j) m = umax(m, wa[j].x);
        return m == SENT;
      };
      if (miss()) {
        int it = 0;
        do {
          asm volatile("" ::: "memory");
#pragma unroll
          for (int j = 0; j < 32; ++j) wa[j] = ld16(rs, l16 + (j & 3) * 1024, so + (j >> 2) * 4096);
        } while (miss() && ++it < SPIN_CAP);
      }
      lptr td = L + s * 32768 + l16;
#pragma unroll
      for (int j = 0; j < 32; ++j) *(LAS v4u*)(td + j * 1024) = wa[j];
      arrive<C_T0>(cnt + 4 * s);
      FENCE();
      if (s == 2) TS(18); else TS(20);
    }
#pragma unroll
    for (int i = 0; i < 16; ++i) wd[12 + i] = wa[i];
#pragma unroll
    for (int i = 0; i < 14; ++i) hb[i / 7][i % 7] = wa[16 + i];
  }
  FENCE();
  int wq = w;
  asm volatile("" : "+s"(wq));
  if (wq == 0) {
#pragma unroll
    for (int j = 0; j < 28; ++j) wd[j] = WDLD(j);
    FENCE();
    TS(21);
    gstats(3);
    TS(22);
    arrive<C_M3>(cnt);
    PRIO(0);
#pragma unroll
    for (int j = 0; j < 14; ++j) hb[j / 7][j % 7] = HLD(j / 7, j % 7);
  }
  FENCE();
  {
    v2u hres[2];
    hres[0] = ld8(rs, hro, 0);
    hres[1] = ld8(rs, hro, 32 * 32768);
    await<C_M3>(L, 8);
    TS(23);
#pragma unroll
    for (int ks = 0; ks < 7; ++ks) hb[2][ks] = HLD(2, ks);
    FENCE();
    unsigned pw[4];
#pragma unroll
    for (int j = 0; j < 4; ++j) pw[j] = lds0 + w * 4096 + n16 * 256 + (((((j + rg) & 3) * 4 + g4) ^ n16) * 16);
    const int tk = 2 * w + (l >> 5), c2 = l & 31;
    lptr prd = L + tk * 256 + (((c2 >> 1) ^ tk) * 16) + (c2 & 1) * 8;
    GAS unsigned* op = (GAS unsigned*)((GAS unsigned char*)p.out + (unsigned)((16 * g2r + tk) * 1024 + 128 * db + 4 * c2));
    auto reduce = [&](int s) {
      f32x2 v[8];
#pragma unroll
      for (int j = 0; j < 8; ++j) v[j] = *(LAS f32x2*)(prd + s * 32768 + j * 4096);
      float a = v[0].x, b = v[0].y;
#pragma unroll
      for (int j = 1; j < 8; ++j) { a += v[j].x; b += v[j].y; }
      op[s * (512 * 256)] = pkbf_s(a, b);
    };
#pragma unroll
    for (int s = 0; s < 4; ++s) {
      {
        int it = 0;
        while (umax(umax(umax(hb[s][0].x, hb[s][1].x), umax(hb[s][2].x, hb[s][3].x)), umax(umax(hb[s][4].x, hb[s][5].x), umax(hb[s][6].x, hres[s >> 1].x))) == SENT && ++it < SPIN_CAP) {
          asm volatile("" ::: "memory");
#pragma unroll
          for (int ks = 0; ks < 7; ++ks) hb[s][ks] = HLD(s, ks);
          hres[s >> 1] = ld8(rs, hro, (s >> 1) * 32 * 32768);
        }
      }
      FENCE();
      if (s == 2) {
#pragma unroll
        for (int ks = 0; ks < 7; ++ks) hb[3][ks] = HLD(3, ks);
        FENCE();
      }
      f32x4 acc[4];
#pragma unroll
      for (int dt = 0; dt < 4; ++dt) acc[dt] = f32x4{0.f, 0.f, 0.f, 0.f};
      if (cw == (s & 1)) {
        *(LAS v2u*)(L + L_HX + w * 512 + 8 * l) = hres[s >> 1];
        const v2u ht = trd(L + L_HX + w * 512 + 8 * l);
        acc[0] = f32x4{bflo(ht.x), bfhi(ht.x), bflo(ht.y), bfhi(ht.y)};
      }
#pragma unroll
      for (int ks = 0; ks < 7; ++ks)
#pragma unroll
        for (int dt = 3; dt >= 0; --dt) acc[dt] = mfma16b(wd[4 * ks + dt], hb[s][ks], acc[dt]);
#pragma unroll
      for (int dt = 3; dt >= 0; --dt) *(LAS f32x4*)(uintptr_t)(pw[dt] + s * 32768) = acc[dt];
      FENCE();
      if (s == 0) arrive<C_P0>(cnt);
      if (s == 1) { arrive<C_P1>(cnt); await<C_P0>(L, 8); reduce(0); await<C_P1>(L, 8); reduce(1); }
      if (s == 2) { arrive<C_P2>(cnt); await<C_P2>(L, 8); reduce(2); }
      if (s == 3) { arrive<C_P3>(cnt); await<C_P3>(L, 8); reduce(3); }
      FENCE();
      TS(24 + s);
    }
  }
  asm volatile(".Lcode_end:");
}

extern "C" void kernel_launch(void* const* d_in, const int* in_sizes, int n_in, void* d_out, int out_size, void* d_ws,
                              size_t ws_size, hipStream_t stream) {
  Params p;
  p.x = (const u16*)d_in[0]; p.logA = (const u16*)d_in[1]; p.aim = (const u16*)d_in[2]; p.cre = (const u16*)d_in[3];
  p.cim = (const u16*)d_in[4]; p.logdt = (const u16*)d_in[5]; p.dskip = (const u16*)d_in[6];
  p.wg = (const u16*)d_in[8]; p.wu = (const u16*)d_in[9]; p.wd = (const u16*)d_in[10];
  p.out = (u16*)d_out; p.ws = (unsigned char*)d_ws;
  (void)hipFuncSetAttribute((const void*)fused_kernel, hipFuncAttributeMaxDynamicSharedMemorySize, L_BYTES);
  fused_kernel<<<dim3(256), dim3(512), L_BYTES, stream>>>(p);
}
```

```cpp
#include <hip/hip_runtime.h>
#include <stdint.h>

typedef unsigned short u16;
typedef unsigned long long u64;
typedef unsigned v4u __attribute__((ext_vector_type(4)));
typedef unsigned v2u __attribute__((ext_vector_type(2)));
typedef float f32x2 __attribute__((ext_vector_type(2)));
typedef float f32x4 __attribute__((ext_vector_type(4)));
typedef float f32x16 __attribute__((ext_vector_type(16)));
typedef short s16x4 __attribute__((ext_vector_type(4)));
typedef _Float16 f16x8 __attribute__((ext_vector_type(8)));
typedef _Float16 f16x2 __attribute__((ext_vector_type(2)));
typedef __bf16 b16x8 __attribute__((ext_vector_type(8)));
typedef __bf16 b16x2 __attribute__((ext_vector_type(2)));
#define LAS __attribute__((address_space(3)))
#define GAS __attribute__((address_space(1)))
typedef LAS unsigned char* lptr;

#define CAS __attribute__((address_space(4)))

#define STAMP 0

constexpr int SEQ = 2048, DM = 512, NMODE = 128, FF = 1792;
constexpr int SC1 = 16;
constexpr int SPIN_CAP = 20000;
constexpr int OOB = 0x40000000;
constexpr unsigned SENT = 0xffffffffu;

constexpr int WS_EPOCH = 0;
constexpr int WS_ST1 = 4096;
constexpr int WS_F2 = 65536;
constexpr int WS_HT = 1 << 20;
constexpr int WS_W1F = 3 << 20;
constexpr int WS_WDF = 7 << 20;
constexpr int WS_HF = 10 << 20;
constexpr int WS_XT = 17 << 20;
constexpr int XT_CP = 16384 + 256;
constexpr int WS_DBG = 22 << 20;
constexpr int WS_BYTES = 24 << 20;
static_assert(WS_XT + 256 * XT_CP <= WS_DBG, "ws map");

constexpr int L_RT = 0, L_CSP = 24576, L_DUMP = 28672;
constexpr int KX_CP = 4160, KX_CH = 4 * KX_CP;
constexpr int L_KX0 = 32768, L_U = L_KX0 + KX_CH, U_CH = 4576, L_KC = L_U + 2 * U_CH, KC_CH = 1312;
constexpr int L_T = 65536, T_ROW = 528, T_B = 41, T_CH = 57 * T_ROW;
constexpr int L_RED = 65536;
constexpr int L_KX1 = 131072;
constexpr int L_CNT = L_KX1 + KX_CH;
constexpr int L_ST2 = L_CNT + 128;
constexpr int L_CSW = L_ST2 + 2048;
constexpr int L_INV = L_CSW + 1024;
constexpr int L_HX = L_INV + 16;
constexpr int L_BYTES = L_HX + 4096;
static_assert(L_BYTES <= 163840, "lds");
static_assert(L_KC + 2 * KC_CH <= 65536 && 2 * T_CH <= 65536, "lds");
static_assert(L_U % 16 == 0 && L_KC % 16 == 0 && L_CNT % 16 == 0 && T_CH % 16 == 0, "align");
enum { C_S1 = 0, C_S2, C_S3, C_W1, C_T0, C_T1, C_T2, C_T3, C_M0, C_M1, C_M2, C_M3, C_P0, C_P1, C_P2, C_P3 };

struct Params {
  const u16 *x, *logA, *aim, *cre, *cim, *logdt, *dskip, *wg, *wu, *wd;
  u16* out;
  unsigned char* ws;
};

__device__ __forceinline__ float asf(unsigned v) { return __builtin_bit_cast(float, v); }
__device__ __forceinline__ unsigned asu(float v) { return __builtin_bit_cast(unsigned, v); }
__device__ __forceinline__ float bflo(unsigned v) { return asf(v << 16); }
__device__ __forceinline__ float bfhi(unsigned v) { return asf(v & 0xffff0000u); }
__device__ __forceinline__ unsigned pkbf(float lo, float hi) {
  b16x2 r = {(__bf16)lo, (__bf16)hi};
  return __builtin_bit_cast(unsigned, r);
}
__device__ __forceinline__ unsigned pkbf_s(float lo, float hi) {
  asm("" : "+v"(lo));
  asm("" : "+v"(hi));
  return pkbf(lo, hi);
}
__device__ __forceinline__ float rbf(float f) { return bflo(pkbf(f, f)); }
__device__ __forceinline__ unsigned pkh(float lo, float hi) {
  f16x2 r = {(_Float16)lo, (_Float16)hi};
  return __builtin_bit_cast(unsigned, r);
}
template <int C> __device__ __forceinline__ unsigned dppu(unsigned v) {
  return (unsigned)__builtin_amdgcn_update_dpp(0, (int)v, C, 0xf, 0xf, true);
}
template <int C> __device__ __forceinline__ float dppf(float v) { return asf(dppu<C>(asu(v))); }
__device__ __forceinline__ float x16_add(float v) {
  auto s = __builtin_amdgcn_permlane16_swap(asu(v), asu(v), false, false);
  return asf(s[0]) + asf(s[1]);
}
__device__ __forceinline__ float fold32(float a, float b) {
  auto s = __builtin_amdgcn_permlane32_swap(asu(a), asu(b), false, false);
  return asf(s[0]) + asf(s[1]);
}
__device__ __forceinline__ float wave_max(float v) {
  v = fmaxf(v, dppf<0xB1>(v)); v = fmaxf(v, dppf<0x4E>(v)); v = fmaxf(v, dppf<0x141>(v)); v = fmaxf(v, dppf<0x140>(v));
  auto s = __builtin_amdgcn_permlane16_swap(asu(v), asu(v), false, false);
  v = fmaxf(asf(s[0]), asf(s[1]));
  auto t = __builtin_amdgcn_permlane32_swap(asu(v), asu(v), false, false);
  return fmaxf(asf(t[0]), asf(t[1]));
}
__device__ __forceinline__ float dot2acc(unsigned a, unsigned b, float c) {
  return __builtin_amdgcn_fdot2_f32_bf16(__builtin_bit_cast(b16x2, a), __builtin_bit_cast(b16x2, b), c, false);
}
__device__ __forceinline__ unsigned umax(unsigned a, unsigned b) { return a > b ? a : b; }
__device__ __forceinline__ unsigned umin(unsigned a, unsigned b) { return a < b ? a : b; }
constexpr unsigned ONES = 0x3f803f80u;
#define BAR() asm volatile("s_waitcnt lgkmcnt(0)\n\ts_barrier" ::: "memory")
#define VMWAIT(N) asm volatile("s_waitcnt vmcnt(" #N ")" ::: "memory")
#define SBAR() __builtin_amdgcn_sched_barrier(0)
#define FENCE() do { asm volatile("" ::: "memory"); __builtin_amdgcn_sched_barrier(0); } while (0)
#define PRIO(n) __builtin_amdgcn_s_setprio(n)

__device__ __forceinline__ void glds1(const void* g, unsigned dst) {
  unsigned sv;
  asm volatile("s_mov_b32 %0, m0\n\ts_mov_b32 m0, %2\n\ts_nop 0\n\tglobal_load_lds_dwordx4 %1, off\n\ts_mov_b32 m0, %0"
               : "=&s"(sv) : "v"(g), "s"(dst) : "memory");
}
__device__ __forceinline__ v2u trd(lptr p) {
  return __builtin_bit_cast(v2u, __builtin_amdgcn_ds_read_tr16_b64_v4i16((LAS s16x4*)p));
}
__device__ __forceinline__ v4u cat(v2u a, v2u b) { return v4u{a.x, a.y, b.x, b.y}; }
__device__ __forceinline__ f32x4 mfma16h(v4u a, v4u b, f32x4 c) {
  return __builtin_amdgcn_mfma_f32_16x16x32_f16(__builtin_bit_cast(f16x8, a), __builtin_bit_cast(f16x8, b), c, 0, 0, 0);
}
__device__ __forceinline__ f32x4 mfma16b(v4u a, v4u b, f32x4 c) {
  return __builtin_amdgcn_mfma_f32_16x16x32_bf16(__builtin_bit_cast(b16x8, a), __builtin_bit_cast(b16x8, b), c, 0, 0, 0);
}
__device__ __forceinline__ f32x16 mfma32b(v4u a, v4u b, f32x16 c) {
  return __builtin_amdgcn_mfma_f32_32x32x16_bf16(__builtin_bit_cast(b16x8, a), __builtin_bit_cast(b16x8, b), c, 0, 0, 0);
}
typedef __amdgpu_buffer_rsrc_t rsrc_t;
__device__ __forceinline__ v4u ld16(rsrc_t rs, int voff, int soff) { return __builtin_amdgcn_raw_buffer_load_b128(rs, voff, soff, SC1); }
__device__ __forceinline__ v2u ld8(rsrc_t rs, int voff, int soff) { return __builtin_amdgcn_raw_buffer_load_b64(rs, voff, soff, SC1); }
__device__ __forceinline__ void st16(v4u v, rsrc_t rs, int voff, int soff) { __builtin_amdgcn_raw_buffer_store_b128(v, rs, voff + soff, 0, SC1); }
__device__ __forceinline__ void st8(v2u v, rsrc_t rs, int voff, int soff) { __builtin_amdgcn_raw_buffer_store_b64(v, rs, voff, soff, SC1); }
__device__ __forceinline__ void st4(unsigned v, rsrc_t rs, int voff, int soff) { __builtin_amdgcn_raw_buffer_store_b32(v, rs, voff, soff, SC1); }
template <class T> __device__ __forceinline__ T gld(const void* base, unsigned off) { return *(const GAS T*)((const GAS unsigned char*)base + off); }

template <int C> __device__ __forceinline__ void arrive(unsigned cnt) {
  asm volatile("s_mov_b64 exec, 1\n\tds_add_u32 %0, %1 offset:%2\n\ts_mov_b64 exec, -1"
               :: "v"(cnt), "v"(1u), "n"(4 * C) : "memory");
  __builtin_amdgcn_sched_barrier(0);
}
template <int C> __device__ __forceinline__ unsigned peek(lptr L) { return *(volatile LAS unsigned*)(L + L_CNT + 4 * C); }
__device__ __forceinline__ bool below(unsigned v, unsigned n) { return (unsigned)__builtin_amdgcn_readfirstlane(v) < n; }
template <int C> __device__ __forceinline__ void await(lptr L, unsigned n) {
  if (below(peek<C>(L), n)) {
    int it = 0;
    do __builtin_amdgcn_s_sleep(1); while (below(peek<C>(L), n) && ++it < (1 << 22));
  }
  asm volatile("" ::: "memory");
}
__device__ __forceinline__ unsigned neq3(unsigned a, unsigned b, unsigned t) { return __builtin_amdgcn_bitop3_b32(a, b, t, 0x7e); }
__device__ __forceinline__ bool anylane(bool c) { return __builtin_amdgcn_ballot_w64(c) != 0; }

struct C2 { f32x2 r, i; };
__device__ __forceinline__ C2 cmul(C2 a, C2 b) { return C2{a.r * b.r - a.i * b.i, a.r * b.i + a.i * b.r}; }
__device__ __forceinline__ C2 cexp2(f32x2 a2, f32x2 bt, float p) {
  const float e0 = __builtin_amdgcn_exp2f(a2.x * p), e1 = __builtin_amdgcn_exp2f(a2.y * p);
  const float f0 = __builtin_amdgcn_fractf(bt.x * p), f1 = __builtin_amdgcn_fractf(bt.y * p);
  return C2{f32x2{e0 * __builtin_amdgcn_cosf(f0), e1 * __builtin_amdgcn_cosf(f1)},
            f32x2{e0 * __builtin_amdgcn_sinf(f0), e1 * __builtin_amdgcn_sinf(f1)}};
}
__device__ __forceinline__ void put(lptr t, int row, C2 v) {
  *(LAS v2u*)(t + row * T_ROW) = v2u{pkh(v.r.x, v.i.x), pkh(v.r.y, v.i.y)};
}

#define TS(i) do {} while (0)

__global__ void __launch_bounds__(512, 2) fused_kernel(Params p) {
  extern __shared__ __attribute__((aligned(16))) unsigned char smem[];
  lptr L = (lptr)smem;
  const unsigned lds0 = (unsigned)(uintptr_t)L;
  const int tid = threadIdx.x, l = tid & 63, w = __builtin_amdgcn_readfirstlane(tid >> 6);
  const int bid = blockIdx.x, xcd = bid & 7, idx = bid >> 3;
  const int cp = bid, cw = w >> 2, rg = w & 3, ch = 2 * cp + cw;
  const int g4 = l >> 4, q4 = (l >> 2) & 3, p4 = l & 3, n16 = l & 15;
  const int l16 = 16 * l;
  TS(0);
  if (w < 2) {
    u64 pc; unsigned len;
    asm volatile("s_getpc_b64 %0\n.Lcode_here:\n\ts_mov_b32 %1, .Lcode_end-.Lcode_here" : "=s"(pc), "=s"(len));
    const unsigned o = (unsigned)(((idx + 16 * w) & 31) * 1024 + l16);
    glds1((const void*)((pc & ~15ull) + (o < len ? o : 0u)), lds0 + L_DUMP);
  }
  FENCE();
  asm volatile("" :: "s"(p.x), "s"(p.ws));
  rsrc_t rs = __builtin_amdgcn_make_buffer_rsrc((void*)p.ws, (short)0, WS_BYTES, 0x00020000);

  TS(32);
  const unsigned xo = (unsigned)(bid * 8192 + p4 * 2048 + w * 128 + (l >> 2) * 8);
  const v4u xrow = gld<v4u>(p.x, (unsigned)(bid * 8192 + w * 1024 + l16));
  const v2u xa2 = gld<v2u>(p.x, xo), xb2 = gld<v2u>(p.x, xo + 1024);
  FENCE();
  const unsigned po = (unsigned)(ch * 256 + 4 * l);
  const unsigned pA = gld<unsigned>(p.logA, po), pB = gld<unsigned>(p.aim, po);
  const unsigned pCr = gld<unsigned>(p.cre, po), pCi = gld<unsigned>(p.cim, po);
  const unsigned ep = *(const CAS unsigned*)(p.ws + WS_EPOCH);
  const unsigned pdt = *(const CAS unsigned*)(p.logdt + 2 * cp);
  const unsigned pds = *(const CAS unsigned*)(p.dskip + 2 * cp);
  FENCE();
  const v4u sent4 = {SENT, SENT, SENT, SENT};
  const int hto = (l >> 3) * 32768 + (l & 7) * 16 + WS_HT + w * 8 * 32768 + cp * 128;
  st16(sent4, rs, w < 2 ? hto : OOB, 0);
  FENCE();
  const int role = cw ? 3 - rg : rg;
  const bool retile = bid < 224;
  const int rbid = retile ? bid : 0;
  {
    int za = L_CNT + 16 * tid;
    if (w == 3) za = L_U + (l >> 5) * U_CH + (l & 31) * 16;
    if (w > 3) za = l < 32 ? (l < 16 ? L_KX0 : L_KX1 - KX_CH) + (l >> 2) * KX_CP + 4096 + p4 * 16 : L_KC + (g4 & 1) * KC_CH + n16 * 16;
    *(LAS v4u*)(L + za) = v4u{0u, 0u, 0u, 0u};
  }
  unsigned eo[11], kco[4];
#pragma unroll
  for (int j = 0; j < 11; ++j) {
    const int li = 2047 - 8 * (tid & 255) - j;
    const int cf = (int)(pkbf((float)li, 0.f) & 0xffffu) - 17024;
    eo[j] = lds0 + L_KC + cw * KC_CH + 16 + 2 * (li < 128 ? li : cf);
    asm volatile("" : "+v"(eo[j]));
  }
#pragma unroll
  for (int r = 0; r < 4; ++r) {
    const int i = 4 * g4 + r;
    int ci = 512 + 16 * i + n16;
    if (role == 0) ci = 128 * (i >> 3) + (i & 7) + 8 * n16;
    if (role == 1) ci = i < 8 ? 256 + 64 * (i >> 2) + (i & 3) + 4 * n16 : 384 + 32 * ((i - 8) >> 1) + (i & 1) + 2 * n16;
    kco[r] = lds0 + L_KC + cw * KC_CH + 16 + 2 * (ci < 641 ? ci : 641);
    asm volatile("" : "+v"(kco[r]));
  }
  FENCE();
  const int ks1 = 4 * (rbid & 7) + (w >> 1), cg = rbid >> 3;
  const unsigned w1o = (unsigned)((16 * ks1 + (l >> 3)) * (FF * 2) + 128 * cg + 16 * (l & 7));
  const u16* w1p = (w & 1) ? p.wu : p.wg;
  const v4u r1a = gld<v4u>(w1p, w1o), r1b = gld<v4u>(w1p, w1o + 8 * FF * 2);
  FENCE();
  TS(1);
  const unsigned tag = ep + 1u;
  {
    const int o = (l >> 2) * (2 * XT_CP) + p4 * 16, so = WS_XT + 32 * w * XT_CP + bid * 64;
    st16(v4u{xa2.x, tag, xb2.x, tag}, rs, o, so);
    st16(v4u{xa2.y, tag, xb2.y, tag}, rs, o, so + XT_CP);
  }
  FENCE();
  TS(36);
  {
    float s = dot2acc(xrow.x, ONES, 0.f), q = dot2acc(xrow.x, xrow.x, 0.f);
    s = dot2acc(xrow.y, ONES, s); q = dot2acc(xrow.y, xrow.y, q);
    s = dot2acc(xrow.z, ONES, s); q = dot2acc(xrow.z, xrow.z, q);
    s = dot2acc(xrow.w, ONES, s); q = dot2acc(xrow.w, xrow.w, q);
    float v = fold32(s, q);
    v += dppf<0xB1>(v); v += dppf<0x4E>(v); v += dppf<0x141>(v); v += dppf<0x140>(v);
    v = x16_add(v) * (1.f / DM);
    const float mean = asf(__builtin_amdgcn_readlane(asu(v), 0));
    const float var = rbf(fmaxf(v - mean * mean, 0.f));
    const float sd = __builtin_amdgcn_sqrtf(rbf(var + 1e-5f));
    const unsigned pk = pkbf(mean, sd);
    FENCE();
    VMWAIT(4);
    st8(v2u{pk, tag}, rs, l == 32 ? 0 : OOB, WS_ST1 + (8 * bid + w) * 8);
  }
  FENCE();
  TS(2);
  const v4u r2 = gld<v4u>(p.wd, (unsigned)((16 * (rbid >> 1) + 8 * (l >> 5) + 4 * (rbid & 1) + ((l >> 3) & 3)) * (DM * 2) + 128 * w + 16 * (l & 7)));
  const int hfo = (w < 7 ? l16 : OOB) + WS_HF + bid * 7168 + w * 1024;
  st16(sent4, rs, w < 2 ? OOB : hto, 0);
  st16(sent4, rs, hfo, 0);
  FENCE();
  {
    const float LOG2E = 1.4426950408889634f, INV2PI = 0.15915494309189535f;
    const float dt = rbf(__builtin_amdgcn_exp2f((cw ? bfhi(pdt) : bflo(pdt)) * LOG2E));
    const unsigned ea = pkbf(__builtin_amdgcn_exp2f(bflo(pA) * LOG2E), __builtin_amdgcn_exp2f(bfhi(pA) * LOG2E));
    const f32x2 a2 = {-bflo(ea) * dt * LOG2E, -bfhi(ea) * dt * LOG2E};
    const f32x2 bt = {bflo(pB) * dt * INV2PI, bfhi(pB) * dt * INV2PI};
    lptr t = L + L_T + cw * T_CH + 8 * l;
    const C2 one = {f32x2{1.f, 1.f}, f32x2{0.f, 0.f}};
    if (role == 0) {
      const C2 e1 = cexp2(a2, bt, 1.f), e128 = cexp2(a2, bt, 128.f);
      C2 v = e1;
      put(t, 0, one); put(t, 8, e128);
#pragma unroll
      for (int i = 1; i < 8; ++i) {
        put(t, i, v); put(t, 8 + i, cmul(v, e128));
        if (i < 7) v = cmul(v, e1);
      }
    } else if (role == 1) {
      const C2 e2 = cexp2(a2, bt, 2.f), e128 = cexp2(a2, bt, 128.f), e256 = cexp2(a2, bt, 256.f);
      const C2 e4 = cmul(e2, e2), e6 = cmul(e4, e2), e384 = cmul(e256, e128);
      put(t, 16, e256); put(t, 17, cmul(e256, e2)); put(t, 18, cmul(e256, e4)); put(t, 19, cmul(e256, e6));
      put(t, 20, e384); put(t, 21, cmul(e384, e2)); put(t, 22, cmul(e384, e4)); put(t, 23, cmul(e384, e6));
      C2 v = cmul(e256, e256);
#pragma unroll
      for (int j = 0; j < 4; ++j) {
        put(t, 24 + 2 * j, v); put(t, 25 + 2 * j, cmul(v, e4));
        if (j < 3) v = cmul(v, e128);
      }
    } else if (role == 2) {
      const C2 e128 = cexp2(a2, bt, 128.f);
      C2 v = cexp2(a2, bt, 1024.f);
#pragma unroll
      for (int i = 0; i < 9; ++i) {
        put(t, 32 + i, v);
        if (i < 8) v = cmul(v, e128);
      }
    } else {
      const f32x2 cr = {bflo(pCr) * dt, bfhi(pCr) * dt}, ci = {bflo(pCi) * dt, bfhi(pCi) * dt};
      const float mx = wave_max(fmaxf(fabsf(cr.x) + fabsf(ci.x), fabsf(cr.y) + fabsf(ci.y)));
      int es = 258 - (int)(asu(mx) >> 23);
      es = es > 220 ? 220 : (es < 34 ? 34 : es);
      const float S2 = asf((unsigned)(es + 1) << 23);
      if (l == 0) *(LAS unsigned*)(L + L_INV + 4 * cw) = (unsigned)(254 - es) << 23;
      const C2 e8 = cexp2(a2, -bt, 8.f), e64 = cexp2(a2, -bt, 64.f);
      C2 v = {cr * S2, ci * -S2};
      C2 v2 = cmul(v, e64);
#pragma unroll
      for (int j = 0; j < 8; ++j) {
        put(t, T_B + j, v); put(t, T_B + 8 + j, v2);
        if (j < 7) { v = cmul(v, e8); v2 = cmul(v2, e8); }
      }
    }
  }
  FENCE();
  TS(3);
  lptr rt = L + L_RT + w * 3072;
  {
    *(LAS v4u*)(rt + l16) = r1a;
    *(LAS v4u*)(rt + 1024 + l16) = r1b;
    lptr a0 = rt + (8 * (g4 >> 1) + q4) * 128 + 32 * (g4 & 1) + 8 * p4;
    const int o = retile ? (g4 & 1) * 32768 + ((g4 >> 1) * 32 + n16) * 16 : OOB;
    const int so = WS_W1F + (4 * cg * 32 + ks1) * 1024 + (w & 1) * 256;
    lptr cs = L + L_CSP + ((g4 & 1) * 32 + (w & 1) * 16 + n16) * 32 + ((w >> 1) * 2 + (g4 >> 1)) * 4;
#pragma unroll
    for (int f = 0; f < 2; ++f) {
      const v4u fr = cat(trd(a0 + 64 * f), trd(a0 + 64 * f + 512));
      st16(fr, rs, o, so + f * 65536);
      *(LAS float*)(cs + f * 2048) = dot2acc(fr.w, ONES, dot2acc(fr.z, ONES, dot2acc(fr.y, ONES, dot2acc(fr.x, ONES, 0.f))));
    }
  }
  FENCE();
  lptr ta = L + L_T + cw * T_CH + (16 * role + n16) * T_ROW + 16 * g4, tb = L + L_T + cw * T_CH + (T_B + n16) * T_ROW + 16 * g4;
  asm volatile("" : "+v"(ta), "+v"(tb));
  TS(4);
  BAR();
  TS(38);
  if (role < 3) {
    v4u fa[8], fb[8];
#pragma unroll
    for (int ks = 0; ks < 8; ++ks) { fa[ks] = *(LAS v4u*)(ta + 64 * ks); fb[ks] = *(LAS v4u*)(tb + 64 * ks); }
    SBAR();
    f32x4 acc = {0.f, 0.f, 0.f, 0.f};
#pragma unroll
    for (int ks = 0; ks < 8; ++ks) acc = mfma16h(fa[ks], fb[ks], acc);
#pragma unroll
    for (int r = 0; r < 4; ++r) *(LAS _Float16*)(uintptr_t)kco[r] = (_Float16)acc[r];
  }
  FENCE();
  TS(39);
  BAR();
  TS(5);
  const int gxs = WS_XT + cp * XT_CP + 2048 * w, gss = WS_ST1 + 2048 * w;
  v4u xa = ld16(rs, 2 * l16, gxs), xb = ld16(rs, 2 * l16 + 16, gxs), s0 = ld16(rs, 2 * l16, gss), s1 = ld16(rs, 2 * l16 + 16, gss);
  FENCE();
  TS(40);
  {
    unsigned e[12];
#pragma unroll
    for (int j = 0; j < 11; ++j) e[j] = *(LAS u16*)(uintptr_t)eo[j];
    e[11] = 0;
    unsigned P[6], Q[5];
#pragma unroll
    for (int j = 0; j < 6; ++j) P[j] = e[2 * j] | (e[2 * j + 1] << 16);
#pragma unroll
    for (int j = 0; j < 5; ++j) Q[j] = __builtin_amdgcn_alignbit(P[j + 1], P[j], 16);
    lptr kx = L + (cw ? L_KX1 : L_KX0) + 16 * (tid & 255);
    *(LAS v4u*)(kx) = v4u{P[0], P[1], P[2], P[3]};
    *(LAS v4u*)(kx + KX_CP) = v4u{Q[0], Q[1], Q[2], Q[3]};
    *(LAS v4u*)(kx + 2 * KX_CP) = v4u{P[1], P[2], P[3], P[4]};
    *(LAS v4u*)(kx + 3 * KX_CP) = v4u{Q[1], Q[2], Q[3], Q[4]};
  }
  FENCE();
  TS(6);
  VMWAIT(4);
  TS(41);
  {
    unsigned old = 0;
    if (l == 0) old = __hip_atomic_fetch_add((LAS unsigned*)(L + L_CNT) + C_W1, 1u, __ATOMIC_RELAXED, __HIP_MEMORY_SCOPE_WORKGROUP);
    if (__builtin_amdgcn_readfirstlane(old) == 7 && retile) {
      lptr c = L + L_CSP + 64 * l;
      const f32x4 a = *(LAS f32x4*)(c) + *(LAS f32x4*)(c + 16), b = *(LAS f32x4*)(c + 32) + *(LAS f32x4*)(c + 48);
      st16(v4u{asu((a.x + a.y) + (a.z + a.w)), tag, asu((b.x + b.y) + (b.z + b.w)), tag}, rs, g4 * 2048 + n16 * 16,
           WS_F2 + cg * 8192 + (bid & 7) * 256);
    }
  }
  FENCE();
  {
    *(LAS v4u*)(rt + 2048 + l16) = r2;
    lptr a1 = rt + 2048 + q4 * 128 + 32 * g4 + 8 * p4;
    st16(cat(trd(a1), trd(a1 + 512)), rs, retile ? g4 * 1024 + n16 * 16 : OOB,
         WS_WDF + (w * 56 + (rbid >> 2)) * 4096 + (rbid & 3) * 256);
#pragma unroll
    for (int j = 1; j < 4; ++j) st16(sent4, rs, hfo, j * (16 * 112 * 1024));
  }
  FENCE();
  TS(7);
#define GMISS() anylane((neq3(xa.y, xb.y, tag) | neq3(s0.y, s0.w, tag) | neq3(s1.y, s1.w, tag)) != 0u)
  if (GMISS()) {
    int it = 0;
    do {
      asm volatile("" ::: "memory");
      xa = ld16(rs, 2 * l16, gxs); xb = ld16(rs, 2 * l16 + 16, gxs); s0 = ld16(rs, 2 * l16, gss); s1 = ld16(rs, 2 * l16 + 16, gss);
    } while (GMISS() && ++it < SPIN_CAP);
  }
  FENCE();
  TS(8);
  const int rb = (xcd & 1) * 8 + (idx >> 2), cb = (xcd >> 1) * 4 + (idx & 3);
  const int fgW = 7 * cb + (w > 0 ? w - 1 : 0);
  v4u f2a = ld16(rs, l16, WS_F2 + fgW * 2048), f2b = ld16(rs, l16, WS_F2 + fgW * 2048 + 1024);
  FENCE();
  float hb0[4], hb1[4];
  {
    const unsigned xs[4] = {xa.x, xa.z, xb.x, xb.z}, st[4] = {s0.x, s0.z, s1.x, s1.z};
    const float d0 = bflo(pds), d1 = bfhi(pds);
    float u0[4], u1[4];
#pragma unroll
    for (int i = 0; i < 4; ++i) {
      const float m = bflo(st[i]), rsd = __builtin_amdgcn_rcpf(bfhi(st[i]));
      const float x0 = bflo(xs[i]), x1 = bfhi(xs[i]);
      u0[i] = (x0 - m) * rsd; u1[i] = (x1 - m) * rsd;
      hb0[i] = __builtin_fmaf(u0[i], d0, x0); hb1[i] = __builtin_fmaf(u1[i], d1, x1);
    }
    lptr uw = L + L_U + 480 + 512 * w + 8 * l;
    *(LAS v2u*)(uw) = v2u{pkh(u0[0], u0[1]), pkh(u0[2], u0[3])};
    *(LAS v2u*)(uw + U_CH) = v2u{pkh(u1[0], u1[1]), pkh(u1[2], u1[3])};
  }
  const unsigned cnt = lds0 + L_CNT;
  lptr kA = L + (cw ? L_KX1 : L_KX0) + (3 - (n16 & 3)) * KX_CP + 2 * (12 - 4 * (n16 >> 2) + 8 * g4) + 64 * (6 - 2 * rg);
  lptr uB = L + L_U + cw * U_CH + 32 * n16 + 16 * g4 + 384 - 128 * rg;
  lptr redw = L + L_RED + w * 1024 + 16 * (4 * n16 + (g4 ^ ((n16 >> 1) & 3)));
  lptr redr = L + L_RED + w * 8192 + 16 * ((l & ~3) + (p4 ^ ((l >> 3) & 3)));
  int wvo = (w > 0 ? l16 : OOB) + WS_W1F + fgW * 32768;
  asm volatile("" : "+v"(kA), "+v"(uB), "+v"(redw), "+v"(redr), "+v"(wvo));
  TS(42);
  BAR();
  TS(9);
#define AFRAG(a, rr) cat(*(volatile LAS v2u*)(kA + 64 * (57 - 8 * (a) - (rr))), *(volatile LAS v2u*)(kA + 64 * (57 - 8 * (a) - (rr)) + 8))
#define BFRAG(b, rr) (*(LAS v4u*)(uB + 512 * (b) + 64 * (1 - (rr))))
#define WALD(j) ld16(rs, wvo + ((j) & 3) * 1024, ((j) >> 2) * 4096)
  v4u wa[32];
  auto conv_out = [&](int c) {
    const f32x2 invS = *(LAS f32x2*)(L + L_INV);
    f32x4 y[8];
    int it = 0;
    unsigned done;
    do {
      done = *(volatile LAS unsigned*)(L + L_CNT + 4 * c);
      FENCE();
#pragma unroll
      for (int v = 0; v < 8; ++v) y[v] = *(LAS f32x4*)(redr + 1024 * v);
      FENCE();
    } while (below(done, 8) && ++it < (1 << 22));
    float h0[4], h1[4];
#pragma unroll
    for (int i = 0; i < 4; ++i) {
      h0[i] = __builtin_fmaf(((y[0][i] + y[1][i]) + y[2][i]) + y[3][i], invS.x, hb0[i]);
      h1[i] = __builtin_fmaf(((y[4][i] + y[5][i]) + y[6][i]) + y[7][i], invS.y, hb1[i]);
    }
    st16(v4u{umin(pkbf_s(h0[0], h0[1]), SENT - 1), pkbf_s(h0[2], h0[3]), umin(pkbf_s(h1[0], h1[1]), SENT - 1), pkbf_s(h1[2], h1[3])}, rs, hto, 0);
  };
  {
    v4u A[2][8], B[2][8];
    f32x4 t[8];
#pragma unroll
    for (int i = 0; i < 8; ++i) t[i] = f32x4{0.f, 0.f, 0.f, 0.f};
#define MM(a, b, rr) t[(a) + (b)] = mfma16h(A[rr][a], B[rr][b], t[(a) + (b)])
#define MM2(a, b) do { MM(a, b, 0); MM(a, b, 1); } while (0)
#define LDA(a) do { A[0][a] = AFRAG(a, 0); A[1][a] = AFRAG(a, 1); } while (0)
#define LDB(b) do { B[0][b] = BFRAG(b, 0); B[1][b] = BFRAG(b, 1); } while (0)
    LDB(0); LDA(0); SBAR(); LDB(1); LDA(1); SBAR(); LDB(2); LDA(2); SBAR();
    MM2(0, 0); SBAR();
    LDB(3); SBAR();
    MM2(0, 1); SBAR();
    LDA(3); SBAR();
    MM2(1, 0); SBAR();
    st4(tag, rs, (bid == 0 && tid == 0) ? WS_EPOCH : OOB, 0);
    SBAR();
    MM2(1, 1); SBAR();
    *(LAS f32x4*)(redw) = t[0]; *(LAS f32x4*)(redw + 8192) = t[1];
    FENCE();
    VMWAIT(13);
    arrive<C_S1>(cnt);
    MM2(0, 2); MM2(2, 0); SBAR();
    TS(10);
    if (w < 2) conv_out(C_S1);
    FENCE();
    MM2(1, 2); MM2(2, 1); SBAR();
    LDB(4); SBAR();
    MM2(0, 3); MM2(3, 0); SBAR();
    LDA(4); SBAR();
    MM2(1, 3); SBAR();
    *(LAS f32x4*)(redw + 16384) = t[2]; *(LAS f32x4*)(redw + 24576) = t[3];
    FENCE();
#define FMISS() anylane((neq3(f2a.y, f2a.w, tag) | neq3(f2b.y, f2b.w, tag)) != 0u)
    if (FMISS()) {
      int it = 0;
      do {
        asm volatile("" ::: "memory");
        f2a = ld16(rs, l16, WS_F2 + fgW * 2048); f2b = ld16(rs, l16, WS_F2 + fgW * 2048 + 1024);
      } while (FMISS() && ++it < SPIN_CAP);
    }
    FENCE();
    VMWAIT(3);
    arrive<C_S2>(cnt);
    TS(11);
    wa[0] = WALD(0); wa[1] = WALD(1); SBAR();
    MM2(2, 2); SBAR();
    wa[2] = WALD(2); SBAR();
    MM2(3, 1); SBAR();
    wa[3] = WALD(3);
    *(LAS float*)(L + L_CSW + w * 128 + 8 * n16 + 4 * (l >> 5)) = x16_add(fold32(asf(f2a.x) + asf(f2b.x), asf(f2a.z) + asf(f2b.z)));
    SBAR();
    MM2(2, 3); SBAR();
    wa[4] = WALD(4); SBAR();
    MM2(3, 2); SBAR();
    wa[5] = WALD(5);
    FENCE();
    if (w == 2 || w == 3) conv_out(C_S2);
    FENCE();
    MM2(3, 3); SBAR();
    wa[6] = WALD(6); LDB(5); SBAR();
    MM2(3, 4); SBAR();
    wa[7] = WALD(7); SBAR();
    MM2(2, 4); SBAR();
    LDA(5); SBAR();
    MM2(1, 4); SBAR();
    wa[8] = WALD(8); SBAR();
    MM2(0, 4); SBAR();
    wa[9] = WALD(9); SBAR();
    MM2(4, 3); SBAR();
    LDB(6); SBAR();
    MM2(4, 2); SBAR();
    wa[10] = WALD(10); SBAR();
    MM2(4, 1); SBAR();
    LDA(6); SBAR();
    MM2(4, 0); SBAR();
    wa[11] = WALD(11); SBAR();
    MM2(2, 5); SBAR();
    *(LAS f32x4*)(redw + 32768) = t[4]; LDB(7); SBAR();
    MM2(1, 5); SBAR();
    wa[12] = WALD(12); SBAR();
    MM2(0, 5); SBAR();
    LDA(7); SBAR();
    MM2(5, 0); SBAR();
    wa[13] = WALD(13); SBAR();
    MM2(5, 1); SBAR();
    *(LAS f32x4*)(redw + 40960) = t[5]; SBAR();
    MM2(5, 2); SBAR();
    wa[14] = WALD(14); SBAR();
    MM2(0, 6); MM2(6, 0); SBAR();
    wa[15] = WALD(15); SBAR();
    MM2(1, 6); SBAR();
    *(LAS f32x4*)(redw + 49152) = t[6]; SBAR();
    MM2(6, 1); MM2(0, 7); MM2(7, 0); SBAR();
    *(LAS f32x4*)(redw + 57344) = t[7];
  }
  FENCE();
  arrive<C_S3>(cnt);
  TS(12);
  const int tso = WS_HT + (16 * cw + rb) * 32768 + rg * 8192;
  v4u tl[8];
#define TLD(o) do { _Pragma("unroll") for (int j = 0; j < 8; ++j) tl[j] = ld16(rs, l16 + (j & 3) * 1024, (o) + (j >> 2) * 4096); } while (0)
#define TMISS() anylane(umax(umax(umax(tl[0].x, tl[1].x), umax(tl[2].x, tl[3].x)), umax(umax(tl[4].x, tl[5].x), umax(tl[6].x, tl[7].x))) == SENT)
  auto tile_put = [&](int go, int lo) {
    if (TMISS()) {
      int it = 0;
      do {
        asm volatile("" ::: "memory");
        TLD(go);
      } while (TMISS() && ++it < SPIN_CAP);
    }
    lptr td = L + lo + w * 8192 + l16;
#pragma unroll
    for (int j = 0; j < 8; ++j) *(LAS v4u*)(td + j * 1024) = tl[j];
  };
  if (w >= 4) {
    conv_out(C_S3);
    FENCE();
#pragma unroll
    for (int ks = 16; ks < 32; ++ks) wa[ks] = WALD(ks);
    FENCE();
    await<C_T0>(L, 4);
    TLD(tso);
    FENCE();
    TS(13);
    tile_put(tso, 0);
    arrive<C_T1>(cnt);
  } else {
    TLD(tso);
    FENCE();
#pragma unroll
    for (int ks = 16; ks < 32; ++ks) wa[ks] = WALD(ks);
    FENCE();
    TS(13);
    tile_put(tso, 0);
    arrive<C_T0>(cnt);
  }
  FENCE();
  TS(14);
  const int g2r = xcd * 4 + (idx >> 3), db = idx & 7, tbo = g2r >> 1, th = g2r & 1;
  const int wdo = WS_WDF + (db * 56 + 7 * w) * 4096;
  const int hvo = (g4 >> 1) * 1024 + ((g4 & 1) * 32 + th * 16 + n16) * 16;
  const int hso = WS_HF + (tbo * 112 + 14 * w) * 1024;
  const int rch = 64 * db + 16 * rg + (l >> 2);
  const int hro = WS_HT + (16 * cw + tbo) * 32768 + (rch >> 1) * 128 + (4 * th + p4) * 16 + (rch & 1) * 8;
#define WDLD(j) ld16(rs, l16, wdo + ((j) >> 2) * 4096 + ((((j) & 3) + rg) & 3) * 1024)
#define HLD(s, ks) ld16(rs, hvo, hso + (s) * (16 * 112 * 1024) + (ks) * 2048)
  v4u wd[28], hb[4][7];
  auto gstats = [&](int s) {
    lptr bt = L + s * 32768 + g4 * 512 + (q4 >> 1) * 128 + p4 * 16 + (q4 & 1) * 8;
    const v4u ones = {ONES, ONES, ONES, ONES};
    f32x4 gq[2] = {{0.f, 0.f, 0.f, 0.f}, {0.f, 0.f, 0.f, 0.f}}, gs[2] = {{0.f, 0.f, 0.f, 0.f}, {0.f, 0.f, 0.f, 0.f}};
    v2u fa[32], fb[32];
    constexpr int D = 6;
#pragma unroll
    for (int i = 0; i < D; ++i) { fa[i] = trd(bt + (i >> 1) * 2048 + (i & 1) * 64); fb[i] = trd(bt + (i >> 1) * 2048 + (i & 1) * 64 + 256); }
    SBAR();
#pragma unroll
    for (int i = 0; i < 32; ++i) {
      const v4u f = cat(fa[i], fb[i]);
      gq[i & 1] = mfma16b(f, f, gq[i & 1]);
      SBAR();
      if (i + D < 32) fa[i + D] = trd(bt + ((i + D) >> 1) * 2048 + (i & 1) * 64);
      SBAR();
      gs[i & 1] = mfma16b(ones, f, gs[i & 1]);
      SBAR();
      if (i + D < 32) fb[i + D] = trd(bt + ((i + D) >> 1) * 2048 + (i & 1) * 64 + 256);
      SBAR();
    }
    const float C1 = -2.f * 0.7978845608028654f * 1.4426950408889634f, C2c = C1 * 0.044715f;
#pragma unroll
    for (int hh = 0; hh < 2; ++hh) {
      const float q = (p4 & 2) ? ((p4 & 1) ? gq[hh][3] : gq[hh][2]) : ((p4 & 1) ? gq[hh][1] : gq[hh][0]);
      const float mean = gs[hh][0] * (1.f / DM);
      const float var = rbf(fmaxf(q * (1.f / DM) - mean * mean, 0.f));
      const float sd = rbf(__builtin_amdgcn_sqrtf(rbf(var + 1e-5f)));
      const float r = __builtin_amdgcn_rcpf(sd);
      if (q4 == g4) *(LAS f32x4*)(L + L_ST2 + s * 512 + hh * 256 + 16 * n16) = f32x4{-rbf(mean), C1 * r, C2c * r * r * r, sd * sd};
    }
  };
  if (w > 0) {
    const int trb = (4 * (g4 >> 1) + (q4 >> 1)) * 128 + (4 * (g4 & 1) + p4) * 16 + (q4 & 1) * 8;
#pragma unroll
    for (int s = 0; s < 4; ++s) {
      if (s == 0) await<C_T0>(L, 4);
      if (s == 1) await<C_T1>(L, 4);
      if (s == 2) await<C_T2>(L, 1);
      if (s == 3) await<C_T3>(L, 1);
      TS(15 + 2 * s);
      PRIO(1);
      lptr bt = L + s * 32768 + trb;
      f32x16 acc;
#pragma unroll
      for (int r = 0; r < 16; ++r) acc[r] = 0.f;
      v4u bf[4];
#pragma unroll
      for (int ks = 0; ks < 3; ++ks) bf[ks] = cat(trd(bt + ks * 1024), trd(bt + ks * 1024 + 256));
      SBAR();
#pragma unroll
      for (int ks = 0; ks < 32; ++ks) {
        if (ks + 3 < 32) bf[(ks + 3) & 3] = cat(trd(bt + (ks + 3) * 1024), trd(bt + (ks + 3) * 1024 + 256));
        acc = mfma32b(wa[ks], bf[ks & 3], acc);
        if ((s == 1 || s == 2) && (ks & 3) == 0 && ks >= 8) wd[6 * (s - 1) + (ks >> 2) - 2] = WDLD(6 * (s - 1) + (ks >> 2) - 2);
        if (s == 3 && ks >= 1 && ks < 17) wd[11 + ks] = WDLD(11 + ks);
        if (s == 3 && ks >= 17 && ks < 31) hb[(ks - 17) / 7][(ks - 17) % 7] = HLD((ks - 17) / 7, (ks - 17) % 7);
        if (ks == 16) PRIO(2);
        SBAR();
      }
      PRIO(0);
      if (s == 3) arrive<C_M3>(cnt);
      FENCE();
      float csr[16];
#pragma unroll
      for (int i = 0; i < 4; ++i) {
        const f32x4 v = *(LAS f32x4*)(L + L_CSW + w * 128 + 32 * i + 16 * (l >> 5));
        csr[4 * i] = v.x; csr[4 * i + 1] = v.y; csr[4 * i + 2] = v.z; csr[4 * i + 3] = v.w;
      }
      f32x4 st;
#pragma unroll 1
      for (int it = 0; it < (1 << 22); ++it) {
        st = *(volatile LAS f32x4*)(L + L_ST2 + s * 512 + 16 * (l & 31));
        if (__all(st.w != 0.f)) break;
        __builtin_amdgcn_s_sleep(1);
      }
      TS(16 + 2 * s);
      float o[8];
#pragma unroll
      for (int j = 0; j < 8; ++j) {
        float g = __builtin_fmaf(st.x, csr[j], acc[j]);
        const float u = __builtin_fmaf(st.x, csr[8 + j], acc[8 + j]);
        asm("" : "+v"(g));
        const float e = __builtin_amdgcn_exp2f(g * __builtin_fmaf(g * g, st.z, st.y));
        o[j] = (g * u) * __builtin_amdgcn_rcpf(__builtin_fmaf(e, st.w, st.w));
      }
      st16(v4u{umin(pkbf_s(o[0], o[1]), SENT - 1), pkbf_s(o[2], o[3]), pkbf_s(o[4], o[5]), pkbf_s(o[6], o[7])}, rs, l16,
           WS_HF + ((16 * s + rb) * 112 + fgW) * 1024);
      FENCE();
    }
  } else {
    PRIO(3);
    await<C_T0>(L, 4);
    TS(15);
    gstats(0);
    TS(16);
    FENCE();
#pragma unroll 1
    for (int s = 2; s < 4; ++s) {
      const int so = WS_HT + (16 * s + rb) * 32768;
#pragma unroll
      for (int j = 0; j < 32; ++j) wa[j] = ld16(rs, l16 + (j & 3) * 1024, so + (j >> 2) * 4096);
      FENCE();
      if (s == 2) await<C_T1>(L, 4);
      gstats(s - 1);
      FENCE();
      auto miss = [&]() {
        unsigned m = wa[0].x;
#pragma unroll
        for (int j = 1; j < 32; ++j) m = umax(m, wa[j].x);
        return m == SENT;
      };
      if (miss()) {
        int it = 0;
        do {
          asm volatile("" ::: "memory");
#pragma unroll
          for (int j = 0; j < 32; ++j) wa[j] = ld16(rs, l16 + (j & 3) * 1024, so + (j >> 2) * 4096);
        } while (miss() && ++it < SPIN_CAP);
      }
      lptr td = L + s * 32768 + l16;
#pragma unroll
      for (int j = 0; j < 32; ++j) *(LAS v4u*)(td + j * 1024) = wa[j];
      arrive<C_T0>(cnt + 4 * s);
      FENCE();
      if (s == 2) TS(18); else TS(20);
    }
#pragma unroll
    for (int i = 0; i < 16; ++i) wd[12 + i] = wa[i];
#pragma unroll
    for (int i = 0; i < 14; ++i) hb[i / 7][i % 7] = wa[16 + i];
  }
  FENCE();
  int wq = w;
  asm volatile("" : "+s"(wq));
  if (wq == 0) {
#pragma unroll
    for (int j = 0; j < 28; ++j) wd[j] = WDLD(j);
    FENCE();
    TS(21);
    gstats(3);
    TS(22);
    arrive<C_M3>(cnt);
    PRIO(0);
#pragma unroll
    for (int j = 0; j < 14; ++j) hb[j / 7][j % 7] = HLD(j / 7, j % 7);
  }
  FENCE();
  {
    v2u hres[2];
    hres[0] = ld8(rs, hro, 0);
    hres[1] = ld8(rs, hro, 32 * 32768);
    await<C_M3>(L, 8);
    TS(23);
#pragma unroll
    for (int ks = 0; ks < 7; ++ks) hb[2][ks] = HLD(2, ks);
    FENCE();
    unsigned pw[4];
#pragma unroll
    for (int j = 0; j < 4; ++j) pw[j] = lds0 + w * 4096 + n16 * 256 + (((((j + rg) & 3) * 4 + g4) ^ n16) * 16);
    const int tk = 2 * w + (l >> 5), c2 = l & 31;
    lptr prd = L + tk * 256 + (((c2 >> 1) ^ tk) * 16) + (c2 & 1) * 8;
    GAS unsigned* op = (GAS unsigned*)((GAS unsigned char*)p.out + (unsigned)((16 * g2r + tk) * 1024 + 128 * db + 4 * c2));
    auto reduce = [&](int s) {
      f32x2 v[8];
#pragma unroll
      for (int j = 0; j < 8; ++j) v[j] = *(LAS f32x2*)(prd + s * 32768 + j * 4096);
      float a = v[0].x, b = v[0].y;
#pragma unroll
      for (int j = 1; j < 8; ++j) { a += v[j].x; b += v[j].y; }
      op[s * (512 * 256)] = pkbf_s(a, b);
    };
#pragma unroll
    for (int s = 0; s < 4; ++s) {
      {
        int it = 0;
        while (umax(umax(umax(hb[s][0].x, hb[s][1].x), umax(hb[s][2].x, hb[s][3].x)), umax(umax(hb[s][4].x, hb[s][5].x), umax(hb[s][6].x, hres[s >> 1].x))) == SENT && ++it < SPIN_CAP) {
          asm volatile("" ::: "memory");
#pragma unroll
          for (int ks = 0; ks < 7; ++ks) hb[s][ks] = HLD(s, ks);
          hres[s >> 1] = ld8(rs, hro, (s >> 1) * 32 * 32768);
        }
      }
      FENCE();
      if (s == 2) {
#pragma unroll
        for (int ks = 0; ks < 7; ++ks) hb[3][ks] = HLD(3, ks);
        FENCE();
      }
      f32x4 acc[4];
#pragma unroll
      for (int dt = 0; dt < 4; ++dt) acc[dt] = f32x4{0.f, 0.f, 0.f, 0.f};
      if (cw == (s & 1)) {
        *(LAS v2u*)(L + L_HX + w * 512 + 8 * l) = hres[s >> 1];
        const v2u ht = trd(L + L_HX + w * 512 + 8 * l);
        acc[0] = f32x4{bflo(ht.x), bfhi(ht.x), bflo(ht.y), bfhi(ht.y)};
      }
#pragma unroll
      for (int ks = 0; ks < 7; ++ks)
#pragma unroll
        for (int dt = 3; dt >= 0; --dt) acc[dt] = mfma16b(wd[4 * ks + dt], hb[s][ks], acc[dt]);
#pragma unroll
      for (int dt = 3; dt >= 0; --dt) *(LAS f32x4*)(uintptr_t)(pw[dt] + s * 32768) = acc[dt];
      FENCE();
      if (s == 0) arrive<C_P0>(cnt);
      if (s == 1) { arrive<C_P1>(cnt); await<C_P0>(L, 8); reduce(0); await<C_P1>(L, 8); reduce(1); }
      if (s == 2) { arrive<C_P2>(cnt); await<C_P2>(L, 8); reduce(2); }
      if (s == 3) { arrive<C_P3>(cnt); await<C_P3>(L, 8); reduce(3); }
      FENCE();
      TS(24 + s);
    }
  }
  asm volatile(".Lcode_end:");
}

extern "C" void kernel_launch(void* const* d_in, const int* in_sizes, int n_in, void* d_out, int out_size, void* d_ws,
                              size_t ws_size, hipStream_t stream) {
  Params p;
  p.x = (const u16*)d_in[0]; p.logA = (const u16*)d_in[1]; p.aim = (const u16*)d_in[2]; p.cre = (const u16*)d_in[3];
  p.cim = (const u16*)d_in[4]; p.logdt = (const u16*)d_in[5]; p.dskip = (const u16*)d_in[6];
  p.wg = (const u16*)d_in[8]; p.wu = (const u16*)d_in[9]; p.wd = (const u16*)d_in[10];
  p.out = (u16*)d_out; p.ws = (unsigned char*)d_ws;
  (void)hipFuncSetAttribute((const void*)fused_kernel, hipFuncAttributeMaxDynamicSharedMemorySize, L_BYTES);
  fused_kernel<<<dim3(256), dim3(512), L_BYTES, stream>>>(p);
}
```

```cpp
#include <hip/hip_runtime.h>
#include <stdint.h>

typedef unsigned short u16;
typedef unsigned long long u64;
typedef unsigned v4u __attribute__((ext_vector_type(4)));
typedef unsigned v2u __attribute__((ext_vector_type(2)));
typedef float f32x2 __attribute__((ext_vector_type(2)));
typedef float f32x4 __attribute__((ext_vector_type(4)));
typedef float f32x16 __attribute__((ext_vector_type(16)));
typedef short s16x4 __attribute__((ext_vector_type(4)));
typedef _Float16 f16x8 __attribute__((ext_vector_type(8)));
typedef _Float16 f16x2 __attribute__((ext_vector_type(2)));
typedef __bf16 b16x8 __attribute__((ext_vector_type(8)));
typedef __bf16 b16x2 __attribute__((ext_vector_type(2)));
typedef __bf16 b16x16 __attribute__((ext_vector_type(16)));
typedef unsigned v8u __attribute__((ext_vector_type(8)));
#define LAS __attribute__((address_space(3)))
#define GAS __attribute__((address_space(1)))
typedef LAS unsigned char* lptr;

#define CAS __attribute__((address_space(4)))

#define STAMP 0
#define LS 2
#define RP 1
#define F2P 0
#define PUB 1
#define EW 0
#define PR4 0
#define BUB 4
#define EO 0
#define HWS 2
#define ESA 1
#define ESB 2
#define WST 2

constexpr int SEQ = 2048, DM = 512, NMODE = 128, FF = 1792;
constexpr int SC1 = 16;
constexpr int SPIN_CAP = 20000;
constexpr int OOB = 0x40000000;
constexpr unsigned SENT = 0xffffffffu;

constexpr int WS_EPOCH = 0;
constexpr int WS_FH = 4096;
constexpr int WS_F2 = 65536;
constexpr int WS_HT = 1 << 20;
constexpr int WS_W1F = 3 << 20;
constexpr int WS_WDF = 7 << 20;
constexpr int WS_HF = 10 << 20;
constexpr int HF_S = 16 * 112 * 1024;
constexpr int WS_XT = 17 << 20;
constexpr int XT_Q = 32768 + 256;
constexpr int WS_DBG = 22 << 20;
constexpr int WS_BYTES = 24 << 20;
static_assert(WS_XT + 128 * XT_Q <= WS_DBG, "ws map");

constexpr int L_RT = 0, L_RD = 16384, L_DUMP = 28672;
constexpr int KX_CP = 4160, KX_CH = 4 * KX_CP;
constexpr int L_KX0 = 32768, L_U = L_KX0 + KX_CH, U_CH = 4576, L_KC = L_U + 2 * U_CH;
constexpr int KC0 = 0x3f78, KC_CH = 2 * (0x4548 - KC0);
constexpr int L_T = 65536, T_ROW = 544, T_B = 41, T_CH = 57 * T_ROW;
constexpr int L_RED = 65536;
constexpr int L_KX1 = 131072;
constexpr int L_CNT = L_KX1 + KX_CH;
constexpr int L_ST2 = L_CNT + 128;
constexpr int L_CSW = L_ST2 + 2048;
constexpr int L_INV = L_CSW + 1024;
constexpr int L_HX = L_INV + 16;
constexpr int L_XC = L_HX + 4096, XC_ROW = 1056;
constexpr int L_BYTES = L_XC + 8 * XC_ROW;
static_assert(L_BYTES <= 163840, "lds");
static_assert(L_KC + 2 * KC_CH <= 65536 && 2 * T_CH <= 65536, "lds");
static_assert(L_U % 16 == 0 && L_KC % 16 == 0 && L_CNT % 16 == 0 && T_CH % 16 == 0, "align");
enum { C_S1 = 0, C_S2, C_S3, C_T0, C_T1, C_T2, C_T3, C_M2, C_M3, C_P0, C_P1, C_P2, C_P3 };

struct Params {
  const u16 *x, *logA, *aim, *cre, *cim, *logdt, *dskip, *wg, *wu, *wd;
  u16* out;
  unsigned char* ws;
};

__device__ __forceinline__ float asf(unsigned v) { return __builtin_bit_cast(float, v); }
__device__ __forceinline__ unsigned asu(float v) { return __builtin_bit_cast(unsigned, v); }
__device__ __forceinline__ float bflo(unsigned v) { return asf(v << 16); }
__device__ __forceinline__ float bfhi(unsigned v) { return asf(v & 0xffff0000u); }
__device__ __forceinline__ unsigned pkbf(float lo, float hi) {
  b16x2 r = {(__bf16)lo, (__bf16)hi};
  return __builtin_bit_cast(unsigned, r);
}
__device__ __forceinline__ unsigned pkbf_s(float lo, float hi) {
  asm("" : "+v"(lo));
  asm("" : "+v"(hi));
  return pkbf(lo, hi);
}
__device__ __forceinline__ float rbf(float f) { return bflo(pkbf(f, f)); }
__device__ __forceinline__ unsigned pkh(float lo, float hi) {
  f16x2 r = {(_Float16)lo, (_Float16)hi};
  return __builtin_bit_cast(unsigned, r);
}
template <int C> __device__ __forceinline__ unsigned dppu(unsigned v) {
  return (unsigned)__builtin_amdgcn_update_dpp(0, (int)v, C, 0xf, 0xf, true);
}
template <int C> __device__ __forceinline__ float dppf(float v) { return asf(dppu<C>(asu(v))); }
__device__ __forceinline__ float x16_add(float v) {
  auto s = __builtin_amdgcn_permlane16_swap(asu(v), asu(v), false, false);
  return asf(s[0]) + asf(s[1]);
}
__device__ __forceinline__ float fold32(float a, float b) {
  auto s = __builtin_amdgcn_permlane32_swap(asu(a), asu(b), false, false);
  return asf(s[0]) + asf(s[1]);
}
__device__ __forceinline__ float dot2acc(unsigned a, unsigned b, float c) {
  return __builtin_amdgcn_fdot2_f32_bf16(__builtin_bit_cast(b16x2, a), __builtin_bit_cast(b16x2, b), c, false);
}
__device__ __forceinline__ unsigned umax(unsigned a, unsigned b) { return a > b ? a : b; }
__device__ __forceinline__ unsigned umin(unsigned a, unsigned b) { return a < b ? a : b; }
constexpr unsigned ONES = 0x3f803f80u;
#define BAR() asm volatile("s_waitcnt lgkmcnt(0)\n\ts_barrier" ::: "memory")
#define VMWAIT(N) asm volatile("s_waitcnt vmcnt(" #N ")" ::: "memory")
#define SBAR() __builtin_amdgcn_sched_barrier(0)
#define FENCE() do { asm volatile("" ::: "memory"); __builtin_amdgcn_sched_barrier(0); } while (0)
#define PRIO(n) __builtin_amdgcn_s_setprio(n)

__device__ __forceinline__ void glds1(const void* g, unsigned dst) {
  unsigned sv;
  asm volatile("s_mov_b32 %0, m0\n\ts_mov_b32 m0, %2\n\ts_nop 0\n\tglobal_load_lds_dwordx4 %1, off\n\ts_mov_b32 m0, %0"
               : "=&s"(sv) : "v"(g), "s"(dst) : "memory");
}
__device__ __forceinline__ v2u trd(lptr p) {
  return __builtin_bit_cast(v2u, __builtin_amdgcn_ds_read_tr16_b64_v4i16((LAS s16x4*)p));
}
__device__ __forceinline__ v4u cat(v2u a, v2u b) { return v4u{a.x, a.y, b.x, b.y}; }
__device__ __forceinline__ f32x4 mfma16h(v4u a, v4u b, f32x4 c) {
  return __builtin_amdgcn_mfma_f32_16x16x32_f16(__builtin_bit_cast(f16x8, a), __builtin_bit_cast(f16x8, b), c, 0, 0, 0);
}
__device__ __forceinline__ f32x4 mfma16b(v4u a, v4u b, f32x4 c) {
  return __builtin_amdgcn_mfma_f32_16x16x32_bf16(__builtin_bit_cast(b16x8, a), __builtin_bit_cast(b16x8, b), c, 0, 0, 0);
}
__device__ __forceinline__ f32x16 mfma32b(v4u a, v4u b, f32x16 c) {
  return __builtin_amdgcn_mfma_f32_32x32x16_bf16(__builtin_bit_cast(b16x8, a), __builtin_bit_cast(b16x8, b), c, 0, 0, 0);
}
typedef __amdgpu_buffer_rsrc_t rsrc_t;
__device__ __forceinline__ v4u ld16(rsrc_t rs, int voff, int soff) { return __builtin_amdgcn_raw_buffer_load_b128(rs, voff, soff, SC1); }
__device__ __forceinline__ unsigned ld4(rsrc_t rs, int voff, int soff) { return __builtin_amdgcn_raw_buffer_load_b32(rs, voff, soff, SC1); }
__device__ __forceinline__ v2u ld8(rsrc_t rs, int voff, int soff) { return __builtin_amdgcn_raw_buffer_load_b64(rs, voff, soff, SC1); }
__device__ __forceinline__ void st16(v4u v, rsrc_t rs, int voff, int soff) { __builtin_amdgcn_raw_buffer_store_b128(v, rs, voff + soff, 0, SC1); }
__device__ __forceinline__ void st8(v2u v, rsrc_t rs, int voff, int soff) { __builtin_amdgcn_raw_buffer_store_b64(v, rs, voff, soff, SC1); }
__device__ __forceinline__ void st4(unsigned v, rsrc_t rs, int voff, int soff) { __builtin_amdgcn_raw_buffer_store_b32(v, rs, voff, soff, SC1); }
template <int OFF> __device__ __forceinline__ void dma(rsrc_t rs, int voff, int soff, unsigned dst) {
  asm volatile("s_mov_b32 m0, %3\n\ts_nop 0\n\tbuffer_load_dwordx4 %0, %1, %2 offen offset:%4 sc1 lds"
               :: "v"(voff), "s"(rs), "s"(soff), "s"(dst), "n"(OFF) : "memory");
}
__device__ __forceinline__ f32x16 mfma32k8(v2u a, v2u b, f32x16 c) {
  return __builtin_amdgcn_mfma_f32_32x32x8bf16_1k(__builtin_bit_cast(s16x4, a), __builtin_bit_cast(s16x4, b), c, 0, 0, 0);
}
template <int V> struct ic { static constexpr int v = V; };
template <class T, T... I> struct iseq {
  template <class F> static __device__ __forceinline__ void run(F&& f) { (f(ic<I>{}), ...); }
};
template <int N, class F> __device__ __forceinline__ void sfor(F&& f) { __make_integer_seq<iseq, int, N>::run(f); }
constexpr int WC = 28 / WST + 1;
constexpr int every(int g, int a, int d, int n) { return g >= a && (g - a) % d == 0 && (g - a) / d < n ? (g - a) / d : -1; }
constexpr int G_T[4] = {0, 0, 29, 35}, D_T[4] = {1, 1, 1, 2};
constexpr int G_V[4] = {0, 16, 46, 66}, G_W[4] = {0, 28, 60, 84};
constexpr int op_tile(int g) { for (int s = 1; s < 4; ++s) if (every(g, G_T[s], D_T[s], 5) >= 0) return 8 * s + every(g, G_T[s], D_T[s], 5); return -1; }
constexpr int op_mark(int g) { return every(g, 6, 2, 4); }
constexpr int G_FLAG = 27, G_FH = 93;
constexpr int op_wd(int g) {
  if (every(g, 55, 2, 3) >= 0) return every(g, 55, 2, 3);
  if (every(g, 69, 2, 7) >= 0) return 3 + every(g, 69, 2, 7);
  if (every(g, 97, 1, 7) >= 0) return 21 + every(g, 97, 1, 7);
  return -1;
}
constexpr int op_stage(int g) {
  if (every(g, 49, 2, 3) >= 0) return 10 + every(g, 49, 2, 3);
  if (every(g, 61, 2, 4) >= 0) return 13 + every(g, 61, 2, 4);
  if (every(g, 85, 2, 4) >= 0) return 17 + every(g, 85, 2, 4);
  return -1;
}
constexpr int op_h(int g) {
  if (every(g, 104, 2, 4) >= 0) return every(g, 104, 2, 4);
  if (every(g, 111, 2, 9) >= 0) return 4 + every(g, 111, 2, 9);
  return -1;
}
constexpr int op_back(int g) {
  if (every(g, 105, 2, 3) >= 0) return 10 + every(g, 105, 2, 3);
  if (every(g, 112, 2, 8) >= 0) return 13 + every(g, 112, 2, 8);
  return -1;
}
constexpr int NH = 13;
constexpr int ES[4] = {1, ESA, ESA, ESB};
constexpr int hstep(int s) { return (EO + 3 + (26 + ES[s] - 1) / ES[s] + 1) & ~1; }
constexpr int nv(int g) {
  return (op_tile(g) >= 0) + (op_mark(g) >= 0) + (g == G_FLAG) + (g == G_FH) + (g > 32 && (g & 31) == hstep(g >> 5)) + (op_wd(g) >= 0) + (op_stage(g) >= 0) + (op_h(g) >= 0);
}
constexpr bool single() { for (int g = 0; g < 128; ++g) if (nv(g) > 1) return false; return true; }
static_assert(single(), "schedule");
constexpr int younger(int a, int b) { int n = 0; for (int g = a + 1; g < b; ++g) n += nv(g); return n; }
#define VMW(N) asm volatile("s_waitcnt vmcnt(%0)" :: "n"(N) : "memory")
#define VMWC(N) __builtin_amdgcn_s_waitcnt(0x0f70 | (N))
#define PIN(x) asm volatile("" : "+v"(x))
template <class T> __device__ __forceinline__ T gld(const void* base, unsigned off) { return *(const GAS T*)((const GAS unsigned char*)base + off); }

template <int C> __device__ __forceinline__ void arrive(unsigned cnt) {
  asm volatile("s_mov_b64 exec, 1\n\tds_add_u32 %0, %1 offset:%2\n\ts_mov_b64 exec, -1"
               :: "v"(cnt), "v"(1u), "n"(4 * C) : "memory");
  __builtin_amdgcn_sched_barrier(0);
}
template <int C> __device__ __forceinline__ unsigned peek(lptr L) { return *(volatile LAS unsigned*)(L + L_CNT + 4 * C); }
__device__ __forceinline__ bool below(unsigned v, unsigned n) { return (unsigned)__builtin_amdgcn_readfirstlane(v) < n; }
template <int C> __device__ __forceinline__ void await(lptr L, unsigned n) {
  if (below(peek<C>(L), n)) {
    int it = 0;
    do __builtin_amdgcn_s_sleep(1); while (below(peek<C>(L), n) && ++it < (1 << 22));
  }
  asm volatile("" ::: "memory");
}
__device__ __forceinline__ unsigned neq3(unsigned a, unsigned b, unsigned t) { return __builtin_amdgcn_bitop3_b32(a, b, t, 0x7e); }
__device__ __forceinline__ bool anylane(bool c) { return __builtin_amdgcn_ballot_w64(c) != 0; }

__device__ __forceinline__ void tabaddr(unsigned c, unsigned base, unsigned& lo, unsigned& hi) {
  asm volatile("v_mad_u32_u16 %0, %1, 2, %2" : "=v"(lo) : "v"(c), "s"(base));
  asm volatile("v_mad_u32_u16 %0, %1, 2, %2 op_sel:[1,0,0,0]" : "=v"(hi) : "v"(c), "s"(base));
}
__device__ __forceinline__ f32x2 cmul(f32x2 a, f32x2 b) {
  return __builtin_elementwise_fma(f32x2{a.y, a.y}, f32x2{-b.y, b.x}, f32x2{a.x, a.x} * b);
}
__device__ __forceinline__ f32x2 cexp2(f32x2 z, float p) {
  const f32x2 v = z * p;
  const float f = __builtin_amdgcn_fractf(v.y);
  return f32x2{__builtin_amdgcn_cosf(f), __builtin_amdgcn_sinf(f)} * __builtin_amdgcn_exp2f(v.x);
}
__device__ __forceinline__ void put(lptr t, int row, f32x2 v) { *(volatile LAS unsigned*)(t + row * T_ROW) = pkh(v.x, v.y); }
__device__ __forceinline__ void putc(lptr t, int row, f32x2 v) { *(volatile LAS unsigned*)(t + row * T_ROW) = pkh(v.x, -v.y); }
template <int C> __device__ __forceinline__ unsigned dppmax(unsigned v) {
  return umax(v, (unsigned)__builtin_amdgcn_update_dpp(0, (int)v, C, 0xf, 0xf, true));
}

#define TS(i) do {} while (0)
#define TQ(v) do {} while (0)
#define TW(v, i) do {} while (0)

__global__ void __launch_bounds__(512, 2) fused_kernel(Params p) {
  extern __shared__ __attribute__((aligned(16))) unsigned char smem[];
  lptr L = (lptr)smem;
  const unsigned lds0 = (unsigned)(uintptr_t)L;
  const int tid = threadIdx.x, l = tid & 63, w = __builtin_amdgcn_readfirstlane(tid >> 6);
  const int bid = blockIdx.x, xcd = bid & 7, idx = bid >> 3;
  const int cq = (idx >> 1) * 8 + xcd, cp = 2 * cq + (idx & 1), cw = w >> 2, rg = w & 3, ch = 2 * cp + cw;
  const int g4 = l >> 4, q4 = (l >> 2) & 3, p4 = l & 3, n16 = l & 15;
  const int l16 = 16 * l;
  TS(0);
  if (w < 2) {
    u64 pc; unsigned len;
    asm volatile("s_getpc_b64 %0\n.Lcode_here:\n\ts_mov_b32 %1, .Lcode_end-.Lcode_here" : "=s"(pc), "=s"(len));
    const unsigned o = (unsigned)((idx + 32 * w) * 1024 + l16);
    glds1((const void*)((pc & ~15ull) + (o < len ? o : 0u)), lds0 + L_DUMP);
  }
  FENCE();
  asm volatile("" :: "s"(p.x), "s"(p.ws));
  rsrc_t rs = __builtin_amdgcn_make_buffer_rsrc((void*)p.ws, (short)0, WS_BYTES, 0x00020000);

  TS(32);
  v4u xrow;
  asm volatile("global_load_dwordx4 %0, %1, %2" : "=v"(xrow) : "v"((32 * (bid >> 2) + (bid & 3) + 4 * w) * 1024 + l16), "s"(p.x));
  unsigned ep = *(const CAS unsigned*)(p.ws + WS_EPOCH);
  FENCE();
  const v4u sent4 = {SENT, SENT, SENT, SENT};
  const int hto = (l >> 3) * 32768 + ((l & 7) ^ (cp & 4)) * 16 + WS_HT + w * 8 * 32768 + cp * 128;
  st16(sent4, rs, w < 2 ? hto : OOB, 0);
  FENCE();
  unsigned pA, pB, pCr, pCi;
  asm volatile("global_load_dword %0, %4, %5\n\tglobal_load_dword %1, %4, %6\n\tglobal_load_dword %2, %4, %7\n\tglobal_load_dword %3, %4, %8"
               : "=&v"(pA), "=&v"(pB), "=&v"(pCr), "=&v"(pCi) : "v"(ch * 256 + 4 * l), "s"(p.logA), "s"(p.aim), "s"(p.cre), "s"(p.cim));
  unsigned pdt = *(const CAS unsigned*)(p.logdt + 2 * cp);
  unsigned pds = *(const CAS unsigned*)(p.dskip + 2 * cp);
  FENCE();
  const int role = cw ? 3 - rg : rg;
  const bool retile = bid < 224;
  const int rbid = retile ? bid : 0, cg = rbid >> 3;
  auto rawdma = [&]() {
    const int w1o = (64 * (rbid & 7) + 16 * (w >> 1) + (l >> 3)) * (FF * 2) + 128 * cg + 16 * (l & 7);
    const int wdo = (16 * (rbid >> 1) + 8 * (l >> 5) + 4 * (rbid & 1) + ((l >> 3) & 3)) * (DM * 2) + 128 * w + 16 * (l & 7);
    unsigned sv;
    asm volatile("s_mov_b32 %0, m0\n\ts_mov_b32 m0, %6\n\ts_nop 0\n\tglobal_load_lds_dwordx4 %1, %4\n\ts_add_u32 m0, m0, 1024\n\ts_nop 0\n\t"
                 "global_load_lds_dwordx4 %2, %4\n\ts_mov_b32 m0, %7\n\ts_nop 0\n\tglobal_load_lds_dwordx4 %3, %5\n\ts_mov_b32 m0, %0"
                 : "=&s"(sv) : "v"(w1o), "v"(w1o + 8 * FF * 2), "v"(wdo), "s"((w & 1) ? p.wu : p.wg), "s"(p.wd),
                    "s"(lds0 + L_RT + (w & 1) * 8192 + (w >> 1) * 2048), "s"(lds0 + L_RD + w * 1024) : "memory", "scc");
  };
  if (EW) { rawdma(); FENCE(); }
  {
    const v4u z = {0u, 0u, 0u, 0u};
    *(LAS v4u*)(L + L_CNT + 16 * (tid & 255)) = z;
    const int za = (cw ? L_KX1 : L_KX0) + 4096 + rg * KX_CP + p4 * 16;
    const int zb = ((rg & 1) ? L_U - 512 + cw * U_CH : L_KC + cw * KC_CH) + (l16 & ((rg & 1) ? 0x3f0 : 0));
    *(LAS v4u*)(L + (l < 32 ? za : zb)) = z;
  }
  unsigned eo[12], kco[4];
  {
    const unsigned kcb = lds0 + L_KC + cw * KC_CH - 2 * KC0;
    float lf = (float)(2047 - 8 * (tid & 255));
    asm("" : "+v"(lf));
    const float Z0 = asf(0x3f7f0000u), ZN = asf(0x3f7e0000u);
#pragma unroll
    for (int j = 0; j < 12; j += 2) {
      float a = lf - (float)j, b = lf - (float)(j + 1);
      if (j == 6) b = fmaxf(b, Z0);
      if (j >= 8) { a = fmaxf(a, ZN); b = j < 10 ? fmaxf(b, ZN) : a; }
      tabaddr(pkbf_s(a, b), kcb, eo[j], eo[j + 1]);
    }
    const u64 pb = role == 0 ? 0x0084008000040000ull : (role == 1 ? 0x0300020001020100ull : 0x0a01080106010401ull);
    const float d1 = role == 0 ? 1.f : (role == 1 ? 4.f : 128.f), d2 = role == 0 ? 2.f : (role == 1 ? 128.f : 256.f);
    const float p0 = (float)(((unsigned)(pb >> (l & 48)) & 0xffffu) + 8 * n16), p2 = p0 + d2;
    tabaddr(pkbf_s(fmaxf(p0, Z0), p0 + d1), kcb, kco[0], kco[1]);
    tabaddr(pkbf_s(p2, p2 + d1), kcb, kco[2], kco[3]);
  }
  lptr xcw = L + L_XC + w * XC_ROW + l16, xcr = L + L_XC + (l & 7) * XC_ROW + 128 * w + 16 * (l >> 3);
  int xto = (l >> 3) * (2 * XT_Q) + (l & 7) * 16 + WS_XT + 16 * w * XT_Q + ((4 * (bid >> 5) + (bid & 3)) * 8 + ((bid >> 2) & 7)) * 128;
  lptr xcs = L + L_XC + w * XC_ROW + 1024, xct = L + L_XC + (l & 7) * XC_ROW + 1024;
  int xto2 = xto + XT_Q;
  lptr rta = L + L_RT + (w & 1) * 8192 + (w >> 1) * 2048 + (8 * (g4 >> 1) + q4) * 128 + 32 * (g4 & 1) + 8 * p4;
  int rto = (retile ? (g4 & 1) * 32768 + ((g4 >> 1) * 32 + n16) * 16 : OOB) + WS_W1F + (4 * cg * 32 + 4 * (rbid & 7) + (w >> 1)) * 1024 + (w & 1) * 256;
  asm volatile("" : "+v"(xcw), "+v"(xcr), "+v"(xto), "+v"(xcs), "+v"(xct), "+v"(xto2), "+v"(rta), "+v"(rto));
  FENCE();
  if (!EW) rawdma();
  FENCE();
  TS(1);
  asm volatile("s_waitcnt vmcnt(8)" : "+v"(xrow));
  PRIO(3);
  TS(33);
  {
    float s = dot2acc(xrow.x, ONES, 0.f), q = dot2acc(xrow.x, xrow.x, 0.f);
    s = dot2acc(xrow.y, ONES, s); q = dot2acc(xrow.y, xrow.y, q);
    s = dot2acc(xrow.z, ONES, s); q = dot2acc(xrow.z, xrow.z, q);
    s = dot2acc(xrow.w, ONES, s); q = dot2acc(xrow.w, xrow.w, q);
    f32x2 xf[4] = {{bflo(xrow.x), bfhi(xrow.x)}, {bflo(xrow.y), bfhi(xrow.y)}, {bflo(xrow.z), bfhi(xrow.z)}, {bflo(xrow.w), bfhi(xrow.w)}};
    float v = fold32(s, q);
    v += dppf<0xB1>(v); v += dppf<0x4E>(v); v += dppf<0x141>(v); v += dppf<0x140>(v);
    v = x16_add(v) * (1.f / DM);
    const auto sw = __builtin_amdgcn_permlane32_swap(asu(v), asu(v), false, false);
    const float mean = asf(sw[0]);
    const float var = rbf(fmaxf(asf(sw[1]) - mean * mean, 0.f));
    unsigned pk = pkbf(mean, __builtin_amdgcn_sqrtf(rbf(var + 1e-5f)));
    asm("" : "+v"(pk));
    const float rsd = __builtin_amdgcn_rcpf(bfhi(pk));
    f32x2 rn = {rsd, -bflo(pk) * rsd};
    asm("" : "+v"(rn));
    unsigned us[4];
#pragma unroll
    for (int i = 0; i < 4; ++i) {
      const f32x2 u = __builtin_elementwise_fma(xf[i], f32x2{rn.x, rn.x}, f32x2{rn.y, rn.y});
      us[i] = pkh(u.x, u.y);
    }
    *(LAS v4u*)(xcw) = v4u{us[0], us[1], us[2], us[3]};
    *(LAS unsigned*)(xcs) = pk;
  }
  FENCE();
  TS(34);
  VMWAIT(7);
  TS(35);
  BAR();
  asm volatile("" : "+s"(ep));
  const unsigned tag = ep + 1u;
  TS(36);
  {
    const v2u ua = *(volatile LAS v2u*)(xcr), ub = *(volatile LAS v2u*)(xcr + 8);
    const unsigned sa = *(volatile LAS unsigned*)(xct), sb = *(volatile LAS unsigned*)(xct);
    st16(v4u{ua.x, ua.y, sa, tag}, rs, xto, 0);
    st16(v4u{ub.x, ub.y, sb, tag}, rs, xto2, 0);
  }
  FENCE();
  TS(2);
  PRIO(0);
  asm volatile("s_waitcnt vmcnt(5)" : "+v"(pA), "+v"(pB), "+v"(pCr), "+v"(pCi), "+s"(pdt), "+s"(pds));
  TS(44);
  v4u rf[2];
  auto retile_a = [&]() {
    FENCE();
    VMWAIT(3);
    TS(58);
    rf[0] = cat(trd(rta), trd(rta + 512)); rf[1] = cat(trd(rta + 64), trd(rta + 576));
    FENCE();
  };
  auto retile_b = [&]() {
    FENCE();
    st16(rf[0], rs, rto, 0); st16(rf[1], rs, rto, 65536);
    st16(sent4, rs, w < 2 ? OOB : hto, 0);
    float c[2];
#pragma unroll
    for (int i = 0; i < 2; ++i) c[i] = dot2acc(rf[i].w, ONES, dot2acc(rf[i].z, ONES, dot2acc(rf[i].y, ONES, dot2acc(rf[i].x, ONES, 0.f))));
    *(LAS float*)(L + L_HX + 2048 + 4 * tid) = fold32(c[0], c[1]);
    FENCE();
  };
  if (RP == 0) { retile_a(); retile_b(); }
  {
    const float LOG2E = 1.4426950408889634f, INV2PI = 0.15915494309189535f;
    const unsigned sel = (rg & 1) ? 0x03020c0cu : 0x01000c0cu;
#define HALF(v) asf(__builtin_amdgcn_perm(0u, v, sel))
    const float dt = rbf(__builtin_amdgcn_exp2f((cw ? bfhi(pdt) : bflo(pdt)) * LOG2E));
    const f32x2 z = f32x2{rbf(__builtin_amdgcn_exp2f(HALF(pA) * LOG2E)), HALF(pB)} * dt * f32x2{-LOG2E, INV2PI};
    lptr t = L + L_T + cw * T_CH + 256 * (rg & 1) + 4 * l;
    if (((rg >> 1) ^ cw) == 0) {
      f32x2 e[8];
      e[1] = cexp2(z, 1.f);
      const f32x2 e128 = cexp2(z, 128.f);
      put(t, 0, f32x2{1.f, 0.f}); put(t, 8, e128);
      const f32x2 e256 = cmul(e128, e128);
      e[2] = cmul(e[1], e[1]);
      const f32x2 e384 = cmul(e256, e128), e512 = cmul(e256, e256);
      e[3] = cmul(e[2], e[1]); e[4] = cmul(e[2], e[2]);
      const f32x2 e640 = cmul(e512, e128), e768 = cmul(e512, e256), e896 = cmul(e512, e384);
      e[5] = cmul(e[4], e[1]); e[6] = cmul(e[4], e[2]); e[7] = cmul(e[4], e[3]);
#pragma unroll
      for (int i = 1; i < 8; ++i) {
        put(t, i, e[i]); put(t, 8 + i, cmul(e[i], e128));
        if (RP == 1 && i == 2) retile_a();
        if (RP == 1 && i == 5) retile_b();
      }
      put(t, 16, e256); put(t, 17, cmul(e256, e[4])); put(t, 18, e384); put(t, 19, cmul(e384, e[4]));
      put(t, 20, cmul(e256, e[2])); put(t, 21, cmul(e256, e[6])); put(t, 22, cmul(e384, e[2])); put(t, 23, cmul(e384, e[6]));
      put(t, 24, e512); put(t, 25, cmul(e512, e[4])); put(t, 26, e640); put(t, 27, cmul(e640, e[4]));
      put(t, 28, e768); put(t, 29, cmul(e768, e[4])); put(t, 30, e896); put(t, 31, cmul(e896, e[4]));
    } else {
      unsigned m = umax(umax(pCr << 1, pCr << 17), umax(pCi << 1, pCi << 17));
      m = dppmax<0xB1>(m); m = dppmax<0x4E>(m); m = dppmax<0x141>(m); m = dppmax<0x140>(m);
      m = dppmax<0x142>(m); m = dppmax<0x143>(m);
      const f32x2 e8 = cexp2(z, 8.f), e64 = cexp2(z, 64.f);
      f32x2 a = cexp2(z, 1024.f);
      const f32x2 e128 = cmul(e64, e64), e16 = cmul(e8, e8), e256 = cmul(e128, e128);
      int ec = (int)((unsigned)__builtin_amdgcn_readlane((int)m, 63) >> 24);
      ec = ec < 5 ? 5 : ec;
      const unsigned db = (unsigned)__builtin_amdgcn_readfirstlane((int)asu(dt));
      int ei = ec + (int)(db >> 23) - 131;
      ei = ei < 0 ? 0 : (ei > 254 ? 254 : ei);
      *(LAS unsigned*)(L + L_INV + 4 * cw) = (unsigned)ei << 23;
      f32x2 v = f32x2{HALF(pCr), HALF(pCi)} * asf((db & 0x007fffffu) | ((unsigned)(259 - ec) << 23));
      f32x2 v1 = cmul(v, e8), a1 = cmul(a, e128);
      if (RP == 1) retile_a();
#pragma unroll
      for (int j = 0; j < 4; ++j) {
        putc(t, T_B + 2 * j, v); putc(t, T_B + 2 * j + 1, v1);
        putc(t, T_B + 8 + 2 * j, cmul(v, e64)); putc(t, T_B + 9 + 2 * j, cmul(v1, e64));
        put(t, 32 + 2 * j, a); put(t, 33 + 2 * j, a1);
        a = cmul(a, e256);
        if (j < 3) { v = cmul(v, e16); v1 = cmul(v1, e16); a1 = cmul(a1, e256); }
        if (RP == 1 && j == 0) retile_b();
      }
      put(t, 40, a);
    }
  }
  FENCE();
  TS(41);
  if (RP == 2) { retile_a(); retile_b(); }
  TS(3);
  lptr ta = L + L_T + cw * T_CH + (16 * role + n16) * T_ROW + 16 * g4, tb = L + L_T + cw * T_CH + (T_B + n16) * T_ROW + 16 * g4;
  if (role == 3) tb = L + L_HX + 2048 + (w & 1) * 256 + 4 * l;
  asm volatile("" : "+v"(ta), "+v"(tb));
  if (!PUB) VMWAIT(1);
  TS(4);
  BAR();
  TS(38);
  float csum;
  int f2o = OOB;
  if (role < 3) {
    v4u fr[8], fb[8];
#pragma unroll
    for (int ks = 0; ks < 8; ++ks) { fr[ks] = *(LAS v4u*)(ta + 64 * ks); fb[ks] = *(LAS v4u*)(tb + 64 * ks); }
    SBAR();
    f32x4 acc = {0.f, 0.f, 0.f, 0.f};
#pragma unroll
    for (int ks = 0; ks < 8; ++ks) acc = mfma16h(fr[ks], fb[ks], acc);
#pragma unroll
    for (int r = 0; r < 4; ++r) *(LAS _Float16*)(uintptr_t)kco[r] = (_Float16)acc[r];
    csum = 0.f;
  } else {
    csum = (*(LAS float*)(tb) + *(LAS float*)(tb + 512)) + (*(LAS float*)(tb + 1024) + *(LAS float*)(tb + 1536));
    if (retile) f2o = g4 * 2048 + n16 * 8;
  }
  FENCE();
#define F2ST() st8(v2u{asu(csum), tag}, rs, f2o, WS_F2 + cg * 8192 + (bid & 7) * 256 + (w & 1) * 128)
  if (!PUB) F2ST();
  FENCE();
  TS(39);
  const int gxs = WS_XT + cq * XT_Q + 4096 * w;
  v4u gx[4];
  asm volatile("buffer_load_dwordx4 %0, %4, %5, %6 offen sc1\n\tbuffer_load_dwordx4 %1, %4, %5, %6 offen offset:1024 sc1\n\t"
               "buffer_load_dwordx4 %2, %4, %5, %6 offen offset:2048 sc1\n\tbuffer_load_dwordx4 %3, %4, %5, %6 offen offset:3072 sc1"
               : "=&v"(gx[0]), "=&v"(gx[1]), "=&v"(gx[2]), "=&v"(gx[3]) : "v"(l16), "s"(rs), "s"(gxs) : "memory");
  FENCE();
  TS(40);
  if (PUB) VMWAIT(5);
  TS(37);
  BAR();
  if (PUB) F2ST();
  FENCE();
  TS(5);
  {
    unsigned P[6], Q[5];
#pragma unroll
    for (int j = 0; j < 6; ++j) {
      const f16x2 e = {*(LAS _Float16*)(uintptr_t)eo[2 * j], j < 5 ? *(LAS _Float16*)(uintptr_t)eo[2 * j + 1] : (_Float16)0.f};
      P[j] = __builtin_bit_cast(unsigned, e);
    }
#pragma unroll
    for (int j = 0; j < 5; ++j) Q[j] = __builtin_amdgcn_alignbit(P[j + 1], P[j], 16);
    lptr kx = L + (cw ? L_KX1 : L_KX0) + 16 * (tid & 255);
    *(LAS v4u*)(kx) = v4u{P[0], P[1], P[2], P[3]};
    *(LAS v4u*)(kx + KX_CP) = v4u{Q[0], Q[1], Q[2], Q[3]};
    *(LAS v4u*)(kx + 2 * KX_CP) = v4u{P[1], P[2], P[3], P[4]};
    *(LAS v4u*)(kx + 3 * KX_CP) = v4u{Q[1], Q[2], Q[3], Q[4]};
  }
  const v4u wdf = cat(trd(L + L_RD + w * 1024 + q4 * 128 + 32 * g4 + 8 * p4), trd(L + L_RD + w * 1024 + q4 * 128 + 32 * g4 + 8 * p4 + 512));
  FENCE();
  TS(6);
  asm volatile("s_waitcnt vmcnt(%4)" : "+v"(gx[0]), "+v"(gx[1]), "+v"(gx[2]), "+v"(gx[3]) : "n"(PUB));
  TS(7);
#define GMISS() ((neq3(gx[0].w, gx[1].w, tag) | neq3(gx[2].w, gx[3].w, tag)) != 0u)
  if (anylane(GMISS())) {
    int it = 0;
    do {
      asm volatile("" ::: "memory");
      if (GMISS()) {
#pragma unroll
        for (int i = 0; i < 4; ++i) gx[i] = ld16(rs, l16 + 1024 * i, gxs);
      }
    } while (anylane(GMISS()) && ++it < SPIN_CAP);
  }
  FENCE();
  TS(8);
  unsigned up[4];
  {
#pragma unroll
    for (int i = 0; i < 4; ++i) up[i] = (cp & 1) ? gx[i].y : gx[i].x;
    lptr uw = L + L_U + 480 + 512 * w + 8 * l;
    *(LAS v2u*)(uw) = v2u{__builtin_amdgcn_perm(up[1], up[0], 0x05040100u), __builtin_amdgcn_perm(up[3], up[2], 0x05040100u)};
    *(LAS v2u*)(uw + U_CH) = v2u{__builtin_amdgcn_perm(up[1], up[0], 0x07060302u), __builtin_amdgcn_perm(up[3], up[2], 0x07060302u)};
  }
  FENCE();
  st16(wdf, rs, (retile ? g4 * 1024 + n16 * 16 : OOB) + WS_WDF + (w * 56 + (rbid >> 2)) * 4096 + (rbid & 3) * 256, 0);
  FENCE();
  const int rb = (xcd & 1) * 8 + (idx >> 2), cb = (xcd >> 1) * 4 + (idx & 3);
  const int fgW = 7 * cb + (w > 0 ? w - 1 : 0);
  const unsigned cnt = lds0 + L_CNT;
  lptr kA = L + (cw ? L_KX1 : L_KX0) + (3 - (n16 & 3)) * KX_CP + 2 * (12 - 4 * (n16 >> 2) + 8 * g4) + 64 * (6 - 2 * rg);
  lptr uB = L + L_U + cw * U_CH + 32 * n16 + 16 * g4 + 384 - 128 * rg;
  lptr redw = L + L_RED + w * 1024 + 16 * (4 * n16 + (g4 ^ ((n16 >> 1) & 3)));
  lptr redr = L + L_RED + w * 8192 + 16 * ((l & ~3) + (p4 ^ ((l >> 3) & 3)));
  int wvo = (w > 0 ? l16 : OOB) + WS_W1F + fgW * 32768;
  int hfo = (w > 0 ? l16 : OOB) + WS_HF + (rb * 112 + fgW) * 1024;
  asm volatile("" : "+v"(kA), "+v"(uB), "+v"(redw), "+v"(redr), "+v"(wvo), "+v"(hfo));
  TS(42);
  BAR();
  TS(9);
#define AFRAG(a, rr) cat(*(volatile LAS v2u*)(kA + 64 * (57 - 8 * (a) - (rr))), *(volatile LAS v2u*)(kA + 64 * (57 - 8 * (a) - (rr)) + 8))
#define BFRAG(b, rr) (*(LAS v4u*)(uB + 512 * (b) + 64 * (1 - (rr))))
  const int rot = (idx >> 2) * 4096;
#define WALD(j) ld16(rs, wvo + ((j) & 3) * 1024, (((j) >> 2) * 4096) ^ rot)
  v4u wa[32];
  float hb0[4], hb1[4];
  auto conv_out = [&](int c) {
    const f32x2 invS = *(LAS f32x2*)(L + L_INV);
    f32x4 y[8];
    int it = 0;
    unsigned done;
    do {
      done = *(volatile LAS unsigned*)(L + L_CNT + 4 * c);
      FENCE();
#pragma unroll
      for (int v = 0; v < 8; ++v) y[v] = *(LAS f32x4*)(redr + 1024 * v);
      FENCE();
    } while (below(done, 8) && ++it < (1 << 22));
    float h0[4], h1[4];
#pragma unroll
    for (int i = 0; i < 4; ++i) {
      h0[i] = __builtin_fmaf(((y[0][i] + y[1][i]) + y[2][i]) + y[3][i], invS.x, hb0[i]);
      h1[i] = __builtin_fmaf(((y[4][i] + y[5][i]) + y[6][i]) + y[7][i], invS.y, hb1[i]);
    }
    st16(v4u{umin(pkbf_s(h0[0], h0[1]), SENT - 1), pkbf_s(h0[2], h0[3]), umin(pkbf_s(h1[0], h1[1]), SENT - 1), pkbf_s(h1[2], h1[3])}, rs, hto, 0);
  };
  {
    v4u A[2][8], B[2][8];
    f32x4 t[8];
#pragma unroll
    for (int i = 0; i < 8; ++i) t[i] = f32x4{0.f, 0.f, 0.f, 0.f};
#define MM(a, b, rr) t[(a) + (b)] = mfma16h(A[rr][a], B[rr][b], t[(a) + (b)])
#define MM2(a, b) do { MM(a, b, 0); MM(a, b, 1); } while (0)
#define LDA(a) do { A[0][a] = AFRAG(a, 0); A[1][a] = AFRAG(a, 1); } while (0)
#define LDB(b) do { B[0][b] = BFRAG(b, 0); B[1][b] = BFRAG(b, 1); } while (0)
    LDB(0); LDA(0); SBAR(); LDB(1); LDA(1); SBAR(); LDB(2); LDA(2); SBAR();
#pragma unroll
    for (int i = 0; i < 4; ++i) {
      const f16x2 u = __builtin_bit_cast(f16x2, up[i]);
      const float m = bflo(gx[i].z), sd = bfhi(gx[i].z);
      hb0[i] = __builtin_fmaf((float)u.x, sd + bflo(pds), m); hb1[i] = __builtin_fmaf((float)u.y, sd + bfhi(pds), m);
      asm volatile("" : "+v"(hb0[i]), "+v"(hb1[i]));
    }
    SBAR();
    v4u f2a, f2b;
#define F2LD() do { f2a = ld16(rs, l16, WS_F2 + fgW * 2048); f2b = ld16(rs, l16, WS_F2 + fgW * 2048 + 1024); } while (0)
    if (F2P == 0) F2LD();
    FENCE();
    MM2(0, 0); SBAR();
    LDB(3); SBAR();
    MM2(0, 1); SBAR();
    LDA(3); SBAR();
    MM2(1, 0); SBAR();
    st4(tag, rs, (bid == 0 && tid == 0) ? WS_EPOCH : OOB, 0);
    SBAR();
    MM2(1, 1); SBAR();
    *(LAS f32x4*)(redw) = t[0]; *(LAS f32x4*)(redw + 8192) = t[1];
    FENCE();
    arrive<C_S1>(cnt);
    if (F2P == 1) F2LD();
    FENCE();
    MM2(0, 2); MM2(2, 0); SBAR();
    TS(10);
    if (w < 2) conv_out(C_S1);
    FENCE();
    MM2(1, 2); MM2(2, 1); SBAR();
    LDB(4); SBAR();
    TS(43);
#define FMISS() anylane((neq3(f2a.y, f2a.w, tag) | neq3(f2b.y, f2b.w, tag)) != 0u)
    if (FMISS()) {
      int it = 0;
      do {
        asm volatile("" ::: "memory");
        F2LD();
      } while (FMISS() && ++it < SPIN_CAP);
    }
    FENCE();
    TS(11);
#define WA(n) do { if ((n) % WST == 0) { wa[(n) / WST] = WALD((n) / WST); SBAR(); } } while (0)
    WA(0);
    MM2(0, 3); SBAR();
    WA(1);
    MM2(3, 0); SBAR();
    WA(2); LDA(4); SBAR();
    MM2(1, 3); SBAR();
    *(LAS f32x4*)(redw + 16384) = t[2]; *(LAS f32x4*)(redw + 24576) = t[3];
    FENCE();
    arrive<C_S2>(cnt);
    WA(3);
    MM2(2, 2); SBAR();
    WA(4);
    *(LAS float*)(L + L_CSW + w * 128 + 8 * n16 + 4 * (l >> 5)) = x16_add(fold32(asf(f2a.x) + asf(f2b.x), asf(f2a.z) + asf(f2b.z)));
    SBAR();
    MM2(3, 1); SBAR();
    WA(5);
    if (w == 2 || w == 3) conv_out(C_S2);
    FENCE();
    MM2(2, 3); SBAR();
    WA(6);
    MM2(3, 2); SBAR();
    WA(7);
    MM2(3, 3); SBAR();
    WA(8); LDB(5); SBAR();
    MM2(3, 4); SBAR();
    WA(9);
    MM2(2, 4); SBAR();
    WA(10); LDA(5); SBAR();
    MM2(1, 4); SBAR();
    WA(11);
    MM2(0, 4); SBAR();
    WA(12);
    MM2(4, 3); SBAR();
    WA(13); LDB(6); SBAR();
    MM2(4, 2); SBAR();
    WA(14);
    MM2(4, 1); SBAR();
    WA(15); LDA(6); SBAR();
    MM2(4, 0); SBAR();
    WA(16);
    MM2(2, 5); SBAR();
    *(LAS f32x4*)(redw + 32768) = t[4]; WA(17); LDB(7); SBAR();
    MM2(1, 5); SBAR();
    WA(18);
    MM2(0, 5); SBAR();
    WA(19); LDA(7); SBAR();
    MM2(5, 0); SBAR();
    WA(20);
    MM2(5, 1); SBAR();
    *(LAS f32x4*)(redw + 40960) = t[5]; WA(21);
    MM2(5, 2); SBAR();
    WA(22);
    MM2(0, 6); SBAR();
    WA(23);
    MM2(6, 0); SBAR();
    WA(24);
    MM2(1, 6); SBAR();
    *(LAS f32x4*)(redw + 49152) = t[6]; WA(25);
    MM2(6, 1); SBAR();
    WA(26);
    MM2(0, 7); SBAR();
    WA(27);
    MM2(7, 0); SBAR();
    WA(28);
    *(LAS f32x4*)(redw + 57344) = t[7];
  }
  FENCE();
  arrive<C_S3>(cnt);
  TS(12);
  const int tso = WS_HT + rb * 32768;
  auto tile_dma = [&](int s, int c) {
    const int so = tso + s * (16 * 32768) + ((c * 4096) ^ rot);
    const unsigned d = lds0 + s * 32768 + c * 4096;
    dma<0>(rs, l16, so, d); dma<1024>(rs, l16, so, d); dma<2048>(rs, l16, so, d); dma<3072>(rs, l16, so, d);
  };
  auto tile_peek = [&](int s, int c) {
    lptr q = L + s * 32768 + c * 4096 + l16;
    return v4u{*(volatile LAS unsigned*)(q), *(volatile LAS unsigned*)(q + 1024), *(volatile LAS unsigned*)(q + 2048), *(volatile LAS unsigned*)(q + 3072)};
  };
#define TMISS(m) anylane(umax(umax(m.x, m.y), umax(m.z, m.w)) == SENT)
  auto tile_check = [&](v4u m, int s, int c) {
    if (TMISS(m)) {
      int it = 0;
      do {
        tile_dma(s, c);
        VMWAIT(0);
        m = tile_peek(s, c);
      } while (TMISS(m) && ++it < SPIN_CAP);
    }
    FENCE();
  };
  if (cw == 0) { tile_dma(0, 2 * w); tile_dma(0, 2 * w + 1); }
  FENCE();
#pragma unroll
  for (int j = WC; j < 32; ++j) { wa[j] = WALD(j); SBAR(); }
  v2u cA;
  {
    const float c = l < 32 ? *(LAS float*)(L + L_CSW + w * 128 + 4 * (l & 31)) : 0.f;
    const float hi = rbf(c), mid = rbf(c - hi);
    cA = v2u{pkbf(hi, mid), pkbf((c - hi) - mid, 0.f)};
    PIN(cA);
  }
  FENCE();
  if (cw) {
    arrive<C_T0>(cnt);
    conv_out(C_S3);
    FENCE();
    TS(13);
  } else {
    TS(13);
    VMW(32 - WC);
    FENCE();
    const v4u m0 = tile_peek(0, 2 * w), m1 = tile_peek(0, 2 * w + 1);
    tile_check(m0, 0, 2 * w); tile_check(m1, 0, 2 * w + 1);
    arrive<C_T0>(cnt);
  }
  FENCE();
  TS(14);
  const int g2r = xcd * 4 + (idx >> 3), db = idx & 7, tbo = g2r >> 1, th = g2r & 1;
  const int wdo = WS_WDF + (db * 56 + 7 * w) * 4096;
  const int hvo = (g4 >> 1) * 1024 + ((g4 & 1) * 32 + th * 16 + n16) * 16;
  const int hso = WS_HF + (tbo * 112 + 14 * w) * 1024;
  const int rch = 64 * db + 16 * rg + (l >> 2);
  const int hro = WS_HT + (16 * cw + tbo) * 32768 + (rch >> 1) * 128 + (((4 * th) ^ ((rch >> 1) & 4)) + p4) * 16 + (rch & 1) * 8;
#define WDSO(j) (wdo + ((j) >> 2) * 4096 + ((((j) & 3) + rg) & 3) * 1024)
#define WDLD(j) ld16(rs, l16, WDSO(j))
#define SLOT(j) ((j) < 12 ? L_KX1 - 2048 + w * 2048 + ((j) - 10) * 1024 : ((j) == 12 ? L_XC - 1024 + w * 1024 : ((j) - 13) / 4 * 32768 + w * 4096 + ((j) - 13) % 4 * 1024))
#define HLD(s, ks) ld16(rs, hvo, hso + (s) * HF_S + (ks) * 2048)
#define FHLD() ld4(rs, 4 * (n16 < 14 ? n16 : 13), WS_FH + tbo * 512 + 56 * w)
#define FHST() st4(tag, rs, l == 0 ? 0 : OOB, WS_FH + rb * 512 + 4 * fgW)
  v4u wd[28], hb[3][7];
  v2u hres[2];
  unsigned fh;
  auto fh_check = [&]() {
    if (anylane(fh != tag)) {
      int it = 0;
      do {
        asm volatile("" ::: "memory");
        fh = FHLD();
      } while (anylane(fh != tag) && ++it < SPIN_CAP);
    }
    FENCE();
  };
  auto tile_wait = [&](unsigned v, int s) {
    lptr c = L + L_CNT + 4 * (C_T0 + s);
    if (below(v, 8)) {
      int it = 0;
      do __builtin_amdgcn_s_sleep(1); while (below(*(volatile LAS unsigned*)c, 8) && ++it < (1 << 22));
    }
    FENCE();
  };
#define TCNT(s) (*(volatile LAS unsigned*)(L + L_CNT + 4 * (C_T0 + (s))))
  if (w > 0) {
    lptr bt = L + (4 * (g4 >> 1) + (q4 >> 1)) * 128 + (4 * ((g4 ^ (g4 >> 1)) & 1) + p4) * 16 + (q4 & 1) * 8, bt2 = bt + 65536;
    lptr stp = L + L_ST2 + 16 * (l & 31);
    PIN(bt); PIN(bt2); PIN(stp);
#define BLD(g) cat(trd(((g) < 64 ? bt : bt2) + ((g) & 63) * 1024), trd(((g) < 64 ? bt : bt2) + ((g) & 63) * 1024 + 256))
    if (PR4 == 1 && w == 4) PRIO(1);
    if (PR4 == 2 && cw) PRIO(1);
    tile_wait(TCNT(0), 0);
    TS(15);
    f32x16 acc[2];
    v4u bf[4];
    f32x4 st;
    float gt[2], gu[2], ge[2], go[2];
    unsigned ho[4], tc, t5;
#define DMA5(t) dma<0>(rs, l16, tso + (t) * (16 * 32768) + rot + rg * 1024, lds0 + (t) * 32768 + rg * 1024)
#define PEEK5(t) (*(volatile LAS unsigned*)(L + (t) * 32768 + rg * 1024 + l16))
    v4u tm;
    u64 tq[8];
    auto st_check = [&](int s) {
      if (anylane(st.w == 0.f)) {
        int it = 0;
        do {
          __builtin_amdgcn_s_sleep(1);
          st = *(volatile LAS f32x4*)(stp + s * 512);
        } while (anylane(st.w == 0.f) && ++it < (1 << 22));
      }
      FENCE();
      acc[s & 1] = mfma32k8(cA, v2u{asu(st.x), asu(st.x)}, acc[s & 1]);
    };
    auto epi = [&](int s, int j, int stage) {
      const float g0 = acc[s & 1][2 * j], g1 = acc[s & 1][2 * j + 1];
      if (stage == 0) { gt[0] = __builtin_fmaf(g0 * g0, st.z, st.y); gt[1] = __builtin_fmaf(g1 * g1, st.z, st.y); PIN(gt[0]); PIN(gt[1]); }
      if (stage == 1) { gt[0] *= g0; gt[1] *= g1; gu[0] = g0 * acc[s & 1][8 + 2 * j]; PIN(gt[0]); PIN(gt[1]); PIN(gu[0]); }
      if (stage == 2) { ge[0] = __builtin_amdgcn_exp2f(gt[0]); gu[1] = g1 * acc[s & 1][9 + 2 * j]; PIN(ge[0]); PIN(gu[1]); }
      if (stage == 3) { ge[1] = __builtin_amdgcn_exp2f(gt[1]); ge[0] = __builtin_fmaf(ge[0], st.w, st.w); PIN(ge[0]); PIN(ge[1]); }
      if (stage == 4) { ge[0] = __builtin_amdgcn_rcpf(ge[0]); ge[1] = __builtin_fmaf(ge[1], st.w, st.w); PIN(ge[0]); PIN(ge[1]); }
      if (stage == 5) { ge[1] = __builtin_amdgcn_rcpf(ge[1]); go[0] = gu[0] * ge[0]; PIN(go[0]); PIN(ge[1]); }
      if (stage == 6) { go[1] = gu[1] * ge[1]; PIN(go[1]); }
      if (stage == 7) { ho[j] = pkbf(go[0], go[1]); PIN(ho[j]); }
    };
#define HST(s) st16(v4u{umin(ho[0], SENT - 1), ho[1], ho[2], ho[3]}, rs, hfo, (s) * HF_S)
#pragma unroll
    for (int g = 0; g < 3; ++g) bf[g] = BLD(g);
    SBAR();
    sfor<128>([&](auto G) __attribute__((always_inline)) {
      constexpr int g = G.v, s = g >> 5, ks = g & 31;
      if constexpr (ks == 0) {
#pragma unroll
        for (int r = 0; r < 16; ++r) acc[s & 1][r] = 0.f;
      }
      if constexpr (g + 3 < 128) bf[(g + 3) & 3] = BLD(g + 3);
      acc[s & 1] = mfma32b(wa[ks], bf[g & 3], acc[s & 1]);
      SBAR();
      if constexpr (s > 0) {
        constexpr int ke = ks - EO;
        if constexpr (ke == (EO ? -1 : 0)) st = *(volatile LAS f32x4*)(stp + (s - 1) * 512);
        if constexpr (ke == 1) st_check(s - 1);
        constexpr int n = ES[s];
        if constexpr (ke >= 3 && ke < hstep(s) - EO) {
          sfor<n>([&](auto R) __attribute__((always_inline)) {
            constexpr int e = (ke - 3) * n + R.v, j = e / 6, q = e % 6;
            if constexpr (q < 2 && j > 0 && j < 5) epi(s - 1, j - 1, 6 + q);
            if constexpr (j < 4) epi(s - 1, j, q);
          });
        }
        if constexpr (ks == hstep(s)) HST(s - 1);
      }
      constexpr int v = g < G_V[1] + 3 ? 1 : (g < G_V[2] + 3 ? 2 : 3), u = g <= G_W[1] ? 1 : (g <= G_W[2] ? 2 : 3);
      if constexpr (g == G_V[v]) { VMW(younger(G_T[v] + 4 * D_T[v], g)); tm = tile_peek(v, w); t5 = PEEK5(v); }
      if constexpr (g == G_V[v] + 2) {
        if (anylane(umax(umax(umax(tm.x, tm.y), umax(tm.z, tm.w)), t5) == SENT)) {
          int it = 0;
          do {
            tile_dma(v, w); DMA5(v);
            VMWAIT(0);
            tm = tile_peek(v, w); t5 = PEEK5(v);
          } while (anylane(umax(umax(umax(tm.x, tm.y), umax(tm.z, tm.w)), t5) == SENT) && ++it < SPIN_CAP);
        }
        FENCE();
        arrive<C_T0 + v>(cnt);
      }
      if constexpr (g == G_W[u] - 2) tc = TCNT(u);
      if constexpr (g == G_W[u]) tile_wait(tc, u);
      if constexpr (op_tile(g) >= 0) {
        constexpr int t = op_tile(g) >> 3;
        if constexpr ((op_tile(g) & 7) < 4) dma<(op_tile(g) & 3) * 1024>(rs, l16, tso + t * (16 * 32768) + ((w * 4096) ^ rot), lds0 + t * 32768 + w * 4096);
        else DMA5(t);
      }
      if constexpr (op_mark(g) >= 0) st16(sent4, rs, hfo, op_mark(g) * HF_S);
      if constexpr (g == G_FLAG) { VMW(younger(12, g)); FHST(); }
      if constexpr (op_wd(g) >= 0) wd[op_wd(g)] = WDLD(op_wd(g));
      if constexpr (op_stage(g) >= 0) dma<0>(rs, l16, WDSO(op_stage(g)), lds0 + SLOT(op_stage(g)));
      if constexpr (g == G_FH) fh = FHLD();
      if constexpr (g == 103) fh_check();
      if constexpr (op_h(g) >= 0) hb[op_h(g) / 7][op_h(g) % 7] = HLD(op_h(g) / 7, op_h(g) % 7);
      if constexpr (g == 105) VMW(younger(91, g));
      if constexpr (op_back(g) >= 0) wd[op_back(g)] = *(LAS v4u*)(L + SLOT(op_back(g)) + l16);
      if constexpr (g == 125) st = *(volatile LAS f32x4*)(stp + 3 * 512);
      if constexpr (g == 95) arrive<C_M2>(cnt);
      if constexpr (ks == 31) TS(16 + 2 * s);
      if constexpr (s == LS && (ks & 3) == 3) TQ(tq[ks >> 2]);
      if constexpr (s == LS && ks == 31) { TW(tq[0], 50); TW(tq[1], 51); TW(tq[2], 52); TW(tq[3], 53); TW(tq[4], 54); TW(tq[5], 55); TW(tq[6], 56); TW(tq[7], 57); }
      FENCE();
    });
    arrive<C_M3>(cnt);
    st_check(3);
    {
      float o[8];
#pragma unroll
      for (int j = 0; j < 8; ++j) {
        float g = acc[1][j];
        const float e = __builtin_amdgcn_exp2f(g * __builtin_fmaf(g * g, st.z, st.y));
        o[j] = (g * acc[1][8 + j]) * __builtin_amdgcn_rcpf(__builtin_fmaf(e, st.w, st.w));
      }
#pragma unroll
      for (int j = 0; j < 4; ++j) ho[j] = pkbf_s(o[2 * j], o[2 * j + 1]);
      HST(3);
    }
    FENCE();
    TS(21);
#pragma unroll
    for (int j = NH; j < 14; ++j) hb[j / 7][j % 7] = HLD(j / 7, j % 7);
    FENCE();
  } else {
    lptr b0 = L + g4 * 512 + (q4 >> 1) * 128 + (g4 & 1) * 64 + p4 * 16 + (q4 & 1) * 8, b1 = L + g4 * 512 + (q4 >> 1) * 128 + ((g4 & 1) ^ 1) * 64 + p4 * 16 + (q4 & 1) * 8;
    v4u ones = {ONES, ONES, ONES, ONES};
    int sel = (l & 1) ? 0xeeeeeeee : 0x44444444;
    PIN(ones); PIN(sel);
    v2u f[4][8];
#define GLD(o, G, i) f[(G) & 3][i] = trd(((i) < 4 ? b0 : b1) + (o) + (G) * 4096 + ((i) & 2) * 1024 + ((i) & 1) * 256)
    arrive<C_T1>(cnt);
    tile_wait(TCNT(0), 0);
    TS(15);
#pragma unroll
    for (int i = 0; i < 16; ++i) GLD(0, i >> 3, i & 7);
    FENCE();
#pragma unroll 1
    for (int s = 0; s < 4; ++s) {
      const int ot = s * 32768, on = ((s + 1) & 3) * 32768;
      f32x4 gq[2] = {{0.f, 0.f, 0.f, 0.f}, {0.f, 0.f, 0.f, 0.f}}, gs[2] = {{0.f, 0.f, 0.f, 0.f}, {0.f, 0.f, 0.f, 0.f}};
      unsigned tc;
#pragma unroll
      for (int G = 0; G < 8; ++G) {
        const v2u* e = f[G & 3];
#define SQ(h, j) do { const v4u t_ = cat(e[4 * (h) + 2 * (j)], e[4 * (h) + 2 * (j) + 1]); gq[h] = mfma16b(t_, t_, gq[h]); SBAR(); } while (0)
#define SM(h) do { const v8u t_ = {e[4 * (h)].x, e[4 * (h)].y, e[4 * (h) + 1].x, e[4 * (h) + 1].y, e[4 * (h) + 2].x, e[4 * (h) + 2].y, e[4 * (h) + 3].x, e[4 * (h) + 3].y}; \
        gs[h] = __builtin_amdgcn_smfmac_f32_16x16x64_bf16(__builtin_bit_cast(b16x8, ones), __builtin_bit_cast(b16x16, t_), gs[h], sel, 0, 0); SBAR(); } while (0)
#define NX(i) do { if (G < 6) GLD(ot, G + 2, i); else GLD(on, G - 6, i); SBAR(); } while (0)
#define BB(n) do { if (BUB && (6 * G + (n)) % (BUB ? BUB : 1) == 0) { asm volatile("s_nop 2"); SBAR(); } } while (0)
        SQ(0, 0); NX(0); BB(1);
        SQ(1, 0); NX(1); BB(2);
        SQ(0, 1); NX(4); NX(5); BB(3);
        SM(0); NX(2); BB(4);
        SQ(1, 1); NX(3); BB(5);
        SM(1); NX(6); NX(7); BB(6);
        if (G == 5) { FENCE(); arrive<C_T2>(cnt + 4 * s); tc = TCNT((s + 1) & 3); FENCE(); }
        if ((G & 1) == 0 && s == HWS) {
          FENCE();
#pragma unroll
          for (int j = 0; j < 7; ++j) wd[7 * (G >> 1) + j] = WDLD(7 * (G >> 1) + j);
          FENCE();
        }
        SBAR();
      }
      const float C1 = -2.f * 0.7978845608028654f * 1.4426950408889634f, C2c = C1 * 0.044715f;
      float q[2];
#pragma unroll
      for (int hh = 0; hh < 2; ++hh) q[hh] = (p4 & 2) ? ((p4 & 1) ? gq[hh][3] : gq[hh][2]) : ((p4 & 1) ? gq[hh][1] : gq[hh][0]);
      const auto sw = __builtin_amdgcn_permlane32_swap(asu(q[1]), asu(q[1]), false, false);
      const bool first = q4 == g4;
      const float mean = (first ? gs[0][0] + gs[0][1] : gs[1][0] + gs[1][1]) * (1.f / DM);
      const float var = rbf(fmaxf((first ? q[0] : asf(l < 32 ? sw[1] : sw[0])) * (1.f / DM) - mean * mean, 0.f));
      const float sd = rbf(__builtin_amdgcn_sqrtf(rbf(var + 1e-5f)));
      const float r = __builtin_amdgcn_rcpf(sd);
      if (((q4 ^ g4) & 1) == 0) *(LAS f32x4*)(L + L_ST2 + s * 512 + (first ? 0 : 256) + 16 * n16) = f32x4{asf(pkbf(-mean, -mean)), C1 * r, C2c * r * r * r, sd * sd};
      FENCE();
      if (s == 0) TS(16); else if (s == 1) TS(18); else if (s == 2) TS(20); else TS(22);
      if (below(tc, 8)) {
        tile_wait(tc, s + 1);
#pragma unroll
        for (int i = 0; i < 16; ++i) GLD(on, i >> 3, i & 7);
      }
      FENCE();
    }
    fh = FHLD();
    await<C_M2>(L, 8);
    fh_check();
#pragma unroll
    for (int j = 0; j < 14; ++j) hb[j / 7][j % 7] = HLD(j / 7, j % 7);
    await<C_M3>(L, 8);
  }
  FENCE();
  {
    hres[0] = ld8(rs, hro, 0);
    hres[1] = ld8(rs, hro, 32 * 32768);
#pragma unroll
    for (int ks = 0; ks < 7; ++ks) hb[2][ks] = HLD(2, ks);
    FENCE();
    TS(23);
    unsigned pw[4];
#pragma unroll
    for (int j = 0; j < 4; ++j) pw[j] = lds0 + w * 4096 + n16 * 256 + (((((j + rg) & 3) * 4 + g4) ^ n16) * 16);
    const int tk = 2 * w + (l >> 5), c2 = l & 31;
    lptr prd = L + tk * 256 + (((c2 >> 1) ^ tk) * 16) + (c2 & 1) * 8;
    GAS unsigned* op = (GAS unsigned*)((GAS unsigned char*)p.out + (unsigned)((16 * g2r + tk) * 1024 + 128 * db + 4 * c2));
    auto reduce = [&](int s) {
      f32x2 v[8];
#pragma unroll
      for (int j = 0; j < 8; ++j) v[j] = *(LAS f32x2*)(prd + s * 32768 + j * 4096);
      float a = v[0].x, b = v[0].y;
#pragma unroll
      for (int j = 1; j < 8; ++j) { a += v[j].x; b += v[j].y; }
      op[s * (512 * 256)] = pkbf_s(a, b);
    };
#pragma unroll
    for (int s = 0; s < 4; ++s) {
      v4u* h = hb[s % 3];
      {
        int it = 0;
        while (umax(umax(umax(h[0].x, h[1].x), umax(h[2].x, h[3].x)), umax(umax(h[4].x, h[5].x), umax(h[6].x, hres[s >> 1].x))) == SENT && ++it < SPIN_CAP) {
          asm volatile("" ::: "memory");
#pragma unroll
          for (int ks = 0; ks < 7; ++ks) h[ks] = HLD(s, ks);
          hres[s >> 1] = ld8(rs, hro, (s >> 1) * 32 * 32768);
        }
      }
      FENCE();
      TS(45 + s);
      f32x4 acc[4];
#pragma unroll
      for (int dt = 0; dt < 4; ++dt) acc[dt] = f32x4{0.f, 0.f, 0.f, 0.f};
      if (cw == (s & 1)) {
        *(LAS v2u*)(L + L_HX + w * 512 + 8 * l) = hres[s >> 1];
        const v2u ht = trd(L + L_HX + w * 512 + 8 * l);
        acc[0] = f32x4{bflo(ht.x), bfhi(ht.x), bflo(ht.y), bfhi(ht.y)};
      }
#pragma unroll
      for (int ks = 0; ks < 7; ++ks)
#pragma unroll
        for (int dt = 3; dt >= 0; --dt) acc[dt] = mfma16b(wd[4 * ks + dt], h[ks], acc[dt]);
      FENCE();
      if (s == 1) {
#pragma unroll
        for (int ks = 0; ks < 7; ++ks) hb[0][ks] = HLD(3, ks);
        FENCE();
      }
      if (s == 2) await<C_M2>(L, 8);
      if (s == 3) await<C_M3>(L, 8);
#pragma unroll
      for (int dt = 3; dt >= 0; --dt) *(LAS f32x4*)(uintptr_t)(pw[dt] + s * 32768) = acc[dt];
      FENCE();
      if (s == 0) arrive<C_P0>(cnt);
      if (s == 1) arrive<C_P1>(cnt);
      if (s == 2) { arrive<C_P2>(cnt); await<C_P0>(L, 8); reduce(0); await<C_P1>(L, 8); reduce(1); await<C_P2>(L, 8); reduce(2); }
      if (s == 3) { arrive<C_P3>(cnt); await<C_P3>(L, 8); reduce(3); }
      FENCE();
      TS(24 + s);
    }
  }
  asm volatile(".Lcode_end:");
}

extern "C" void kernel_launch(void* const* d_in, const int* in_sizes, int n_in, void* d_out, int out_size, void* d_ws,
                              size_t ws_size, hipStream_t stream) {
  Params p;
  p.x = (const u16*)d_in[0]; p.logA = (const u16*)d_in[1]; p.aim = (const u16*)d_in[2]; p.cre = (const u16*)d_in[3];
  p.cim = (const u16*)d_in[4]; p.logdt = (const u16*)d_in[5]; p.dskip = (const u16*)d_in[6];
  p.wg = (const u16*)d_in[8]; p.wu = (const u16*)d_in[9]; p.wd = (const u16*)d_in[10];
  p.out = (u16*)d_out; p.ws = (unsigned char*)d_ws;
  (void)hipFuncSetAttribute((const void*)fused_kernel, hipFuncAttributeMaxDynamicSharedMemorySize, L_BYTES);
  fused_kernel<<<dim3(256), dim3(512), L_BYTES, stream>>>(p);
}
```

```cpp
#include <hip/hip_runtime.h>
#include <stdint.h>

typedef unsigned short u16;
typedef unsigned long long u64;
typedef unsigned v4u __attribute__((ext_vector_type(4)));
typedef unsigned v2u __attribute__((ext_vector_type(2)));
typedef float f32x2 __attribute__((ext_vector_type(2)));
typedef float f32x4 __attribute__((ext_vector_type(4)));
typedef float f32x16 __attribute__((ext_vector_type(16)));
typedef short s16x4 __attribute__((ext_vector_type(4)));
typedef _Float16 f16x8 __attribute__((ext_vector_type(8)));
typedef _Float16 f16x2 __attribute__((ext_vector_type(2)));
typedef __bf16 b16x8 __attribute__((ext_vector_type(8)));
typedef __bf16 b16x2 __attribute__((ext_vector_type(2)));
typedef __bf16 b16x16 __attribute__((ext_vector_type(16)));
typedef unsigned v8u __attribute__((ext_vector_type(8)));
#define LAS __attribute__((address_space(3)))
#define GAS __attribute__((address_space(1)))
typedef LAS unsigned char* lptr;

#define CAS __attribute__((address_space(4)))

#define STAMP 0
#define LS 2
#define RP 1
#define F2P 0
#define PUB 1
#define EW 0
#define PR4 0
#define BUB 0
#define EO 0
#define HWS 2
#define ESA 1
#define ESB 2
#define KO 0
#define PFW 0
#define HP 1
#define GH3 48
#define DH3 4
#define DH2 5
#define RO 0
#define MK 0
#define WDL 0
#define WST 2

constexpr int SEQ = 2048, DM = 512, NMODE = 128, FF = 1792;
constexpr int SC1 = 16;
constexpr int SPIN_CAP = 20000;
constexpr int OOB = 0x40000000;
constexpr unsigned SENT = 0xffffffffu;

constexpr int WS_EPOCH = 0;
constexpr int WS_FH = 4096;
constexpr int WS_F2 = 65536;
constexpr int WS_HT = 1 << 20;
constexpr int WS_W1F = 3 << 20;
constexpr int WS_WDF = 7 << 20;
constexpr int WS_HF = 10 << 20;
constexpr int HF_S = 16 * 112 * 1024;
constexpr int WS_XT = 17 << 20;
constexpr int XT_Q = 32768 + 256;
constexpr int WS_DBG = 22 << 20;
constexpr int WS_BYTES = 24 << 20;
static_assert(WS_XT + 128 * XT_Q <= WS_DBG, "ws map");

constexpr int L_RT = 0, L_RD = 16384, L_DUMP = 28672;
constexpr int KX_CP = 4160, KX_CH = 4 * KX_CP;
constexpr int L_KX0 = 32768, L_U = L_KX0 + KX_CH, U_CH = 4576, L_KC = L_U + 2 * U_CH;
constexpr int KC0 = 0x3f78, KC_CH = 2 * (0x4548 - KC0);
constexpr int L_T = 65536, T_ROW = 544, T_B = 41, T_CH = 57 * T_ROW;
constexpr int L_RED = 65536;
constexpr int L_KX1 = 131072;
constexpr int L_CNT = L_KX1 + KX_CH;
constexpr int L_ST2 = L_CNT + 128;
constexpr int L_CSW = L_ST2 + 2048;
constexpr int L_INV = L_CSW + 1024;
constexpr int L_HX = L_INV + 16;
constexpr int L_XC = L_HX + 4096, XC_ROW = 1056;
constexpr int L_BYTES = L_XC + 8 * XC_ROW;
static_assert(L_BYTES <= 163840, "lds");
static_assert(L_KC + 2 * KC_CH <= 65536 && 2 * T_CH <= 65536, "lds");
static_assert(L_U % 16 == 0 && L_KC % 16 == 0 && L_CNT % 16 == 0 && T_CH % 16 == 0, "align");
enum { C_S1 = 0, C_S2, C_S3, C_T0, C_T1, C_T2, C_T3, C_M2, C_M3, C_P0, C_P1, C_P2, C_P3 };

struct Params {
  const u16 *x, *logA, *aim, *cre, *cim, *logdt, *dskip, *wg, *wu, *wd;
  u16* out;
  unsigned char* ws;
};

__device__ __forceinline__ float asf(unsigned v) { return __builtin_bit_cast(float, v); }
__device__ __forceinline__ unsigned asu(float v) { return __builtin_bit_cast(unsigned, v); }
__device__ __forceinline__ float bflo(unsigned v) { return asf(v << 16); }
__device__ __forceinline__ float bfhi(unsigned v) { return asf(v & 0xffff0000u); }
__device__ __forceinline__ unsigned pkbf(float lo, float hi) {
  b16x2 r = {(__bf16)lo, (__bf16)hi};
  return __builtin_bit_cast(unsigned, r);
}
__device__ __forceinline__ unsigned pkbf_s(float lo, float hi) {
  asm("" : "+v"(lo));
  asm("" : "+v"(hi));
  return pkbf(lo, hi);
}
__device__ __forceinline__ float rbf(float f) { return bflo(pkbf(f, f)); }
__device__ __forceinline__ unsigned pkh(float lo, float hi) {
  f16x2 r = {(_Float16)lo, (_Float16)hi};
  return __builtin_bit_cast(unsigned, r);
}
template <int C> __device__ __forceinline__ unsigned dppu(unsigned v) {
  return (unsigned)__builtin_amdgcn_update_dpp(0, (int)v, C, 0xf, 0xf, true);
}
template <int C> __device__ __forceinline__ float dppf(float v) { return asf(dppu<C>(asu(v))); }
__device__ __forceinline__ float x16_add(float v) {
  auto s = __builtin_amdgcn_permlane16_swap(asu(v), asu(v), false, false);
  return asf(s[0]) + asf(s[1]);
}
__device__ __forceinline__ float fold32(float a, float b) {
  auto s = __builtin_amdgcn_permlane32_swap(asu(a), asu(b), false, false);
  return asf(s[0]) + asf(s[1]);
}
__device__ __forceinline__ float dot2acc(unsigned a, unsigned b, float c) {
  return __builtin_amdgcn_fdot2_f32_bf16(__builtin_bit_cast(b16x2, a), __builtin_bit_cast(b16x2, b), c, false);
}
__device__ __forceinline__ unsigned umax(unsigned a, unsigned b) { return a > b ? a : b; }
__device__ __forceinline__ unsigned umin(unsigned a, unsigned b) { return a < b ? a : b; }
constexpr unsigned ONES = 0x3f803f80u;
#define BAR() asm volatile("s_waitcnt lgkmcnt(0)\n\ts_barrier" ::: "memory")
#define VMWAIT(N) asm volatile("s_waitcnt vmcnt(" #N ")" ::: "memory")
#define SBAR() __builtin_amdgcn_sched_barrier(0)
#define FENCE() do { asm volatile("" ::: "memory"); __builtin_amdgcn_sched_barrier(0); } while (0)
#define PRIO(n) __builtin_amdgcn_s_setprio(n)

__device__ __forceinline__ void glds1(const void* g, unsigned dst) {
  unsigned sv;
  asm volatile("s_mov_b32 %0, m0\n\ts_mov_b32 m0, %2\n\ts_nop 0\n\tglobal_load_lds_dwordx4 %1, off\n\ts_mov_b32 m0, %0"
               : "=&s"(sv) : "v"(g), "s"(dst) : "memory");
}
__device__ __forceinline__ v2u trd(lptr p) {
  return __builtin_bit_cast(v2u, __builtin_amdgcn_ds_read_tr16_b64_v4i16((LAS s16x4*)p));
}
__device__ __forceinline__ v4u cat(v2u a, v2u b) { return v4u{a.x, a.y, b.x, b.y}; }
__device__ __forceinline__ f32x4 mfma16h(v4u a, v4u b, f32x4 c) {
  return __builtin_amdgcn_mfma_f32_16x16x32_f16(__builtin_bit_cast(f16x8, a), __builtin_bit_cast(f16x8, b), c, 0, 0, 0);
}
__device__ __forceinline__ f32x4 mfma16b(v4u a, v4u b, f32x4 c) {
  return __builtin_amdgcn_mfma_f32_16x16x32_bf16(__builtin_bit_cast(b16x8, a), __builtin_bit_cast(b16x8, b), c, 0, 0, 0);
}
__device__ __forceinline__ f32x16 mfma32b(v4u a, v4u b, f32x16 c) {
  return __builtin_amdgcn_mfma_f32_32x32x16_bf16(__builtin_bit_cast(b16x8, a), __builtin_bit_cast(b16x8, b), c, 0, 0, 0);
}
typedef __amdgpu_buffer_rsrc_t rsrc_t;
__device__ __forceinline__ v4u ld16(rsrc_t rs, int voff, int soff) { return __builtin_amdgcn_raw_buffer_load_b128(rs, voff, soff, SC1); }
__device__ __forceinline__ unsigned ld4(rsrc_t rs, int voff, int soff) { return __builtin_amdgcn_raw_buffer_load_b32(rs, voff, soff, SC1); }
__device__ __forceinline__ v2u ld8(rsrc_t rs, int voff, int soff) { return __builtin_amdgcn_raw_buffer_load_b64(rs, voff, soff, SC1); }
__device__ __forceinline__ void st16(v4u v, rsrc_t rs, int voff, int soff) { __builtin_amdgcn_raw_buffer_store_b128(v, rs, voff + soff, 0, SC1); }
__device__ __forceinline__ void st8(v2u v, rsrc_t rs, int voff, int soff) { __builtin_amdgcn_raw_buffer_store_b64(v, rs, voff, soff, SC1); }
__device__ __forceinline__ void st4(unsigned v, rsrc_t rs, int voff, int soff) { __builtin_amdgcn_raw_buffer_store_b32(v, rs, voff, soff, SC1); }
template <int OFF> __device__ __forceinline__ void dma(rsrc_t rs, int voff, int soff, unsigned dst) {
  asm volatile("s_mov_b32 m0, %3\n\ts_nop 0\n\tbuffer_load_dwordx4 %0, %1, %2 offen offset:%4 sc1 lds"
               :: "v"(voff), "s"(rs), "s"(soff), "s"(dst), "n"(OFF) : "memory");
}
__device__ __forceinline__ f32x16 mfma32k8(v2u a, v2u b, f32x16 c) {
  return __builtin_amdgcn_mfma_f32_32x32x8bf16_1k(__builtin_bit_cast(s16x4, a), __builtin_bit_cast(s16x4, b), c, 0, 0, 0);
}
template <int V> struct ic { static constexpr int v = V; };
template <class T, T... I> struct iseq {
  template <class F> static __device__ __forceinline__ void run(F&& f) { (f(ic<I>{}), ...); }
};
template <int N, class F> __device__ __forceinline__ void sfor(F&& f) { __make_integer_seq<iseq, int, N>::run(f); }
constexpr int WC = 28 / WST + 1;
constexpr int every(int g, int a, int d, int n) { return g >= a && (g - a) % d == 0 && (g - a) / d < n ? (g - a) / d : -1; }
constexpr int G_T[4] = {0, 0, 29, 35}, D_T[4] = {1, 1, 1, 2};
constexpr int G_V[4] = {0, 16, 46, 66}, G_W[4] = {0, 28, 60, 84};
constexpr int op_tile(int g) { for (int s = 1; s < 4; ++s) if (every(g, G_T[s], D_T[s], 5) >= 0) return 8 * s + every(g, G_T[s], D_T[s], 5); return -1; }
constexpr int op_mark(int g) { return MK ? -1 : every(g, 6, 2, 4); }
constexpr int G_FLAG = MK ? -1 : 27, G_FH = MK ? -1 : 93;
constexpr int op_wd(int g) {
  if (every(g, 55, 2, 3) >= 0) return every(g, 55, 2, 3);
  if (every(g, 69, 2, 7) >= 0) return 3 + every(g, 69, 2, 7);
  if (every(g, 97, 1, 7) >= 0) return 21 + every(g, 97, 1, 7);
  return -1;
}
constexpr int op_stage(int g) {
  if (every(g, 49, 2, 3) >= 0) return 10 + every(g, 49, 2, 3);
  if (every(g, 61, 2, 4) >= 0) return 13 + every(g, 61, 2, 4);
  if (every(g, 85, 2, 4) >= 0) return 17 + every(g, 85, 2, 4);
  return -1;
}
constexpr int op_h(int g) {
  if (every(g, 104, 2, 4) >= 0) return every(g, 104, 2, 4);
  if (every(g, 111, 2, 9) >= 0) return 4 + every(g, 111, 2, 9);
  return -1;
}
constexpr int op_back(int g) {
  if (every(g, 105, 2, 3) >= 0) return 10 + every(g, 105, 2, 3);
  if (every(g, 112, 2, 8) >= 0) return 13 + every(g, 112, 2, 8);
  return -1;
}
constexpr int NH = 13, G_H3 = GH3, D_H3 = DH3, D_H2 = DH2;
constexpr int G_R0 = 124, G_R1 = 126, G_S3 = 121;
constexpr int ES[4] = {1, ESA, ESA, ESB};
constexpr int hstep(int s) { return (EO + 3 + (26 + ES[s] - 1) / ES[s] + 1) & ~1; }
constexpr int nv(int g) {
  return (op_tile(g) >= 0) + (op_mark(g) >= 0) + (g == G_FLAG) + (g == G_FH) + (g == G_R0) + (g == G_R1) + (g > 32 && (g & 31) == hstep(g >> 5)) + (op_wd(g) >= 0) + (op_stage(g) >= 0) + (op_h(g) >= 0);
}
constexpr bool single() { for (int g = 0; g < 128; ++g) if (nv(g) > 1) return false; return true; }
static_assert(single(), "schedule");
constexpr int younger(int a, int b) { int n = 0; for (int g = a + 1; g < b; ++g) n += nv(g); return n; }
#define VMW(N) asm volatile("s_waitcnt vmcnt(%0)" :: "n"(N) : "memory")
#define VMWC(N) __builtin_amdgcn_s_waitcnt(0x0f70 | (N))
#define PIN(x) asm volatile("" : "+v"(x))
template <class T> __device__ __forceinline__ T gld(const void* base, unsigned off) { return *(const GAS T*)((const GAS unsigned char*)base + off); }

template <int C> __device__ __forceinline__ void arrive(unsigned cnt) {
  asm volatile("s_mov_b64 exec, 1\n\tds_add_u32 %0, %1 offset:%2\n\ts_mov_b64 exec, -1"
               :: "v"(cnt), "v"(1u), "n"(4 * C) : "memory");
  __builtin_amdgcn_sched_barrier(0);
}
template <int C> __device__ __forceinline__ unsigned peek(lptr L) { return *(volatile LAS unsigned*)(L + L_CNT + 4 * C); }
__device__ __forceinline__ bool below(unsigned v, unsigned n) { return (unsigned)__builtin_amdgcn_readfirstlane(v) < n; }
template <int C> __device__ __forceinline__ void await(lptr L, unsigned n) {
  if (below(peek<C>(L), n)) {
    int it = 0;
    do __builtin_amdgcn_s_sleep(1); while (below(peek<C>(L), n) && ++it < (1 << 22));
  }
  asm volatile("" ::: "memory");
}
__device__ __forceinline__ unsigned neq3(unsigned a, unsigned b, unsigned t) { return __builtin_amdgcn_bitop3_b32(a, b, t, 0x7e); }
__device__ __forceinline__ bool anylane(bool c) { return __builtin_amdgcn_ballot_w64(c) != 0; }

__device__ __forceinline__ void tabaddr(unsigned c, unsigned base, unsigned& lo, unsigned& hi) {
  asm volatile("v_mad_u32_u16 %0, %1, 2, %2" : "=v"(lo) : "v"(c), "s"(base));
  asm volatile("v_mad_u32_u16 %0, %1, 2, %2 op_sel:[1,0,0,0]" : "=v"(hi) : "v"(c), "s"(base));
}
__device__ __forceinline__ f32x2 cmul(f32x2 a, f32x2 b) {
  return __builtin_elementwise_fma(f32x2{a.y, a.y}, f32x2{-b.y, b.x}, f32x2{a.x, a.x} * b);
}
__device__ __forceinline__ f32x2 cexp2(f32x2 z, float p) {
  const f32x2 v = z * p;
  const float f = __builtin_amdgcn_fractf(v.y);
  return f32x2{__builtin_amdgcn_cosf(f), __builtin_amdgcn_sinf(f)} * __builtin_amdgcn_exp2f(v.x);
}
__device__ __forceinline__ void put(lptr t, int row, f32x2 v) { *(volatile LAS unsigned*)(t + row * T_ROW) = pkh(v.x, v.y); }
__device__ __forceinline__ void putc(lptr t, int row, f32x2 v) { *(volatile LAS unsigned*)(t + row * T_ROW) = pkh(v.x, -v.y); }
template <int C> __device__ __forceinline__ unsigned dppmax(unsigned v) {
  return umax(v, (unsigned)__builtin_amdgcn_update_dpp(0, (int)v, C, 0xf, 0xf, true));
}

#define TS(i) do {} while (0)
#define TQ(v) do {} while (0)
#define TW(v, i) do {} while (0)

__global__ void __launch_bounds__(512, 2) fused_kernel(Params p) {
  extern __shared__ __attribute__((aligned(16))) unsigned char smem[];
  lptr L = (lptr)smem;
  const unsigned lds0 = (unsigned)(uintptr_t)L;
  const int tid = threadIdx.x, l = tid & 63, w = __builtin_amdgcn_readfirstlane(tid >> 6);
  const int bid = blockIdx.x, xcd = bid & 7, idx = bid >> 3;
  const int cq = (idx >> 1) * 8 + xcd, cp = 2 * cq + (idx & 1), cw = w >> 2, rg = w & 3, ch = 2 * cp + cw;
  const int g4 = l >> 4, q4 = (l >> 2) & 3, p4 = l & 3, n16 = l & 15;
  const int l16 = 16 * l;
  TS(0);
  if (w < 2) {
    u64 pc; unsigned len;
    asm volatile("s_getpc_b64 %0\n.Lcode_here:\n\ts_mov_b32 %1, .Lcode_end-.Lcode_here" : "=s"(pc), "=s"(len));
    const unsigned o = (unsigned)((idx + 32 * w) * 1024 + l16);
    glds1((const void*)((pc & ~15ull) + (o < len ? o : 0u)), lds0 + L_DUMP);
  }
  FENCE();
  asm volatile("" :: "s"(p.x), "s"(p.ws));
  rsrc_t rs = __builtin_amdgcn_make_buffer_rsrc((void*)p.ws, (short)0, WS_BYTES, 0x00020000);

  TS(32);
  v4u xrow;
  asm volatile("global_load_dwordx4 %0, %1, %2" : "=v"(xrow) : "v"((32 * (bid >> 2) + (bid & 3) + 4 * w) * 1024 + l16), "s"(p.x));
  unsigned ep = *(const CAS unsigned*)(p.ws + WS_EPOCH);
  FENCE();
  const v4u sent4 = {SENT, SENT, SENT, SENT};
  const int hto = (l >> 3) * 32768 + ((l & 7) ^ (cp & 4)) * 16 + WS_HT + w * 8 * 32768 + cp * 128;
  st16(sent4, rs, w < 2 ? hto : OOB, 0);
  FENCE();
  unsigned pA, pB, pCr, pCi;
  asm volatile("global_load_dword %0, %4, %5\n\tglobal_load_dword %1, %4, %6\n\tglobal_load_dword %2, %4, %7\n\tglobal_load_dword %3, %4, %8"
               : "=&v"(pA), "=&v"(pB), "=&v"(pCr), "=&v"(pCi) : "v"(ch * 256 + 4 * l), "s"(p.logA), "s"(p.aim), "s"(p.cre), "s"(p.cim));
  unsigned pdt = *(const CAS unsigned*)(p.logdt + 2 * cp);
  unsigned pds = *(const CAS unsigned*)(p.dskip + 2 * cp);
  FENCE();
  const int rb = (xcd & 1) * 8 + (idx >> 2), cb = (xcd >> 1) * 4 + (idx & 3);
  const int fgW = 7 * cb + (w > 0 ? w - 1 : 0);
  int hfo = (w > 0 ? l16 : OOB) + WS_HF + (rb * 112 + fgW) * 1024;
#define HMARK() do { st16(sent4, rs, hfo, 0); st16(sent4, rs, hfo, HF_S); st16(sent4, rs, hfo, 2 * HF_S); st16(sent4, rs, hfo, 3 * HF_S); FENCE(); } while (0)
  const int role = cw ? 3 - rg : rg;
  const bool retile = bid < 224;
  const int rbid = retile ? bid : 0, cg = rbid >> 3;
  auto rawdma = [&]() {
    const int w1o = (64 * (rbid & 7) + 16 * (w >> 1) + (l >> 3)) * (FF * 2) + 128 * cg + 16 * (l & 7);
    const int wdo = (16 * (rbid >> 1) + 8 * (l >> 5) + 4 * (rbid & 1) + ((l >> 3) & 3)) * (DM * 2) + 128 * w + 16 * (l & 7);
    unsigned sv;
    asm volatile("s_mov_b32 %0, m0\n\ts_mov_b32 m0, %6\n\ts_nop 0\n\tglobal_load_lds_dwordx4 %1, %4\n\ts_add_u32 m0, m0, 1024\n\ts_nop 0\n\t"
                 "global_load_lds_dwordx4 %2, %4\n\ts_mov_b32 m0, %7\n\ts_nop 0\n\tglobal_load_lds_dwordx4 %3, %5\n\ts_mov_b32 m0, %0"
                 : "=&s"(sv) : "v"(w1o), "v"(w1o + 8 * FF * 2), "v"(wdo), "s"((w & 1) ? p.wu : p.wg), "s"(p.wd),
                    "s"(lds0 + L_RT + (w & 1) * 8192 + (w >> 1) * 2048), "s"(lds0 + L_RD + w * 1024) : "memory", "scc");
  };
  if (EW) { rawdma(); FENCE(); }
  {
    const v4u z = {0u, 0u, 0u, 0u};
    *(LAS v4u*)(L + L_CNT + 16 * (tid & 255)) = z;
    const int za = (cw ? L_KX1 : L_KX0) + 4096 + rg * KX_CP + p4 * 16;
    const int zb = ((rg & 1) ? L_U - 512 + cw * U_CH : L_KC + cw * KC_CH) + (l16 & ((rg & 1) ? 0x3f0 : 0));
    *(LAS v4u*)(L + (l < 32 ? za : zb)) = z;
  }
  unsigned eo[12], kco[4];
  {
    const unsigned kcb = lds0 + L_KC + cw * KC_CH - 2 * KC0;
    float lf = (float)(2047 - 8 * (tid & 255));
    asm("" : "+v"(lf));
    const float Z0 = asf(0x3f7f0000u), ZN = asf(0x3f7e0000u);
#pragma unroll
    for (int j = 0; j < 12; j += 2) {
      float a = lf - (float)j, b = lf - (float)(j + 1);
      if (j == 6) b = fmaxf(b, Z0);
      if (j >= 8) { a = fmaxf(a, ZN); b = j < 10 ? fmaxf(b, ZN) : a; }
      tabaddr(pkbf_s(a, b), kcb, eo[j], eo[j + 1]);
    }
    const u64 pb = role == 0 ? 0x0084008000040000ull : (role == 1 ? 0x0300020001020100ull : 0x0a01080106010401ull);
    const float d1 = role == 0 ? 1.f : (role == 1 ? 4.f : 128.f), d2 = role == 0 ? 2.f : (role == 1 ? 128.f : 256.f);
    const float p0 = (float)(((unsigned)(pb >> (l & 48)) & 0xffffu) + 8 * n16), p2 = p0 + d2;
    tabaddr(pkbf_s(fmaxf(p0, Z0), p0 + d1), kcb, kco[0], kco[1]);
    tabaddr(pkbf_s(p2, p2 + d1), kcb, kco[2], kco[3]);
  }
  lptr xcw = L + L_XC + w * XC_ROW + l16, xcr = L + L_XC + (l & 7) * XC_ROW + 128 * w + 16 * (l >> 3);
  int xto = (l >> 3) * (2 * XT_Q) + (l & 7) * 16 + WS_XT + 16 * w * XT_Q + ((4 * (bid >> 5) + (bid & 3)) * 8 + ((bid >> 2) & 7)) * 128;
  lptr xcs = L + L_XC + w * XC_ROW + 1024, xct = L + L_XC + (l & 7) * XC_ROW + 1024;
  int xto2 = xto + XT_Q;
  lptr rta = L + L_RT + (w & 1) * 8192 + (w >> 1) * 2048 + (8 * (g4 >> 1) + q4) * 128 + 32 * (g4 & 1) + 8 * p4;
  int rto = (retile ? (g4 & 1) * 32768 + ((g4 >> 1) * 32 + n16) * 16 : OOB) + WS_W1F + (4 * cg * 32 + 4 * (rbid & 7) + (w >> 1)) * 1024 + (w & 1) * 256;
  asm volatile("" : "+v"(xcw), "+v"(xcr), "+v"(xto), "+v"(xcs), "+v"(xct), "+v"(xto2), "+v"(rta), "+v"(rto));
  FENCE();
  if (!EW) rawdma();
  FENCE();
  TS(1);
  asm volatile("s_waitcnt vmcnt(8)" : "+v"(xrow));
  PRIO(3);
  TS(33);
  {
    float s = dot2acc(xrow.x, ONES, 0.f), q = dot2acc(xrow.x, xrow.x, 0.f);
    s = dot2acc(xrow.y, ONES, s); q = dot2acc(xrow.y, xrow.y, q);
    s = dot2acc(xrow.z, ONES, s); q = dot2acc(xrow.z, xrow.z, q);
    s = dot2acc(xrow.w, ONES, s); q = dot2acc(xrow.w, xrow.w, q);
    f32x2 xf[4] = {{bflo(xrow.x), bfhi(xrow.x)}, {bflo(xrow.y), bfhi(xrow.y)}, {bflo(xrow.z), bfhi(xrow.z)}, {bflo(xrow.w), bfhi(xrow.w)}};
    float v = fold32(s, q);
    v += dppf<0xB1>(v); v += dppf<0x4E>(v); v += dppf<0x141>(v); v += dppf<0x140>(v);
    v = x16_add(v) * (1.f / DM);
    const auto sw = __builtin_amdgcn_permlane32_swap(asu(v), asu(v), false, false);
    const float mean = asf(sw[0]);
    const float var = rbf(fmaxf(asf(sw[1]) - mean * mean, 0.f));
    unsigned pk = pkbf(mean, __builtin_amdgcn_sqrtf(rbf(var + 1e-5f)));
    asm("" : "+v"(pk));
    const float rsd = __builtin_amdgcn_rcpf(bfhi(pk));
    f32x2 rn = {rsd, -bflo(pk) * rsd};
    asm("" : "+v"(rn));
    unsigned us[4];
#pragma unroll
    for (int i = 0; i < 4; ++i) {
      const f32x2 u = __builtin_elementwise_fma(xf[i], f32x2{rn.x, rn.x}, f32x2{rn.y, rn.y});
      us[i] = pkh(u.x, u.y);
    }
    *(LAS v4u*)(xcw) = v4u{us[0], us[1], us[2], us[3]};
    *(LAS unsigned*)(xcs) = pk;
  }
  FENCE();
  TS(34);
  VMWAIT(7);
  TS(35);
  BAR();
  asm volatile("" : "+s"(ep));
  const unsigned tag = ep + 1u;
  TS(36);
  {
    const v2u ua = *(volatile LAS v2u*)(xcr), ub = *(volatile LAS v2u*)(xcr + 8);
    const unsigned sa = *(volatile LAS unsigned*)(xct), sb = *(volatile LAS unsigned*)(xct);
    st16(v4u{ua.x, ua.y, sa, tag}, rs, xto, 0);
    st16(v4u{ub.x, ub.y, sb, tag}, rs, xto2, 0);
  }
  FENCE();
  TS(2);
  PRIO(0);
  asm volatile("s_waitcnt vmcnt(5)" : "+v"(pA), "+v"(pB), "+v"(pCr), "+v"(pCi), "+s"(pdt), "+s"(pds));
  TS(44);
  v4u rf[2];
  auto retile_a = [&]() {
    FENCE();
    VMWAIT(3);
    TS(58);
    rf[0] = cat(trd(rta), trd(rta + 512)); rf[1] = cat(trd(rta + 64), trd(rta + 576));
    FENCE();
  };
  auto retile_b = [&]() {
    FENCE();
    st16(rf[0], rs, rto, 0); st16(rf[1], rs, rto, 65536);
    st16(sent4, rs, w < 2 ? OOB : hto, 0);
    if (MK == 2) HMARK();
    float c[2];
#pragma unroll
    for (int i = 0; i < 2; ++i) c[i] = dot2acc(rf[i].w, ONES, dot2acc(rf[i].z, ONES, dot2acc(rf[i].y, ONES, dot2acc(rf[i].x, ONES, 0.f))));
    *(LAS float*)(L + L_HX + 2048 + 4 * tid) = fold32(c[0], c[1]);
    FENCE();
  };
  if (RP == 0) { retile_a(); retile_b(); }
  {
    const float LOG2E = 1.4426950408889634f, INV2PI = 0.15915494309189535f;
    const unsigned sel = (rg & 1) ? 0x03020c0cu : 0x01000c0cu;
#define HALF(v) asf(__builtin_amdgcn_perm(0u, v, sel))
    const float dt = rbf(__builtin_amdgcn_exp2f((cw ? bfhi(pdt) : bflo(pdt)) * LOG2E));
    const f32x2 z = f32x2{rbf(__builtin_amdgcn_exp2f(HALF(pA) * LOG2E)), HALF(pB)} * dt * f32x2{-LOG2E, INV2PI};
    lptr t = L + L_T + cw * T_CH + 256 * (rg & 1) + 4 * l;
    if (((rg >> 1) ^ cw) == 0) {
      f32x2 e[8];
      e[1] = cexp2(z, 1.f);
      const f32x2 e128 = cexp2(z, 128.f);
      put(t, 0, f32x2{1.f, 0.f}); put(t, 8, e128);
      const f32x2 e256 = cmul(e128, e128);
      e[2] = cmul(e[1], e[1]);
      const f32x2 e384 = cmul(e256, e128), e512 = cmul(e256, e256);
      e[3] = cmul(e[2], e[1]); e[4] = cmul(e[2], e[2]);
      const f32x2 e640 = cmul(e512, e128), e768 = cmul(e512, e256), e896 = cmul(e512, e384);
      e[5] = cmul(e[4], e[1]); e[6] = cmul(e[4], e[2]); e[7] = cmul(e[4], e[3]);
#pragma unroll
      for (int i = 1; i < 8; ++i) {
        put(t, i, e[i]); put(t, 8 + i, cmul(e[i], e128));
        if (RP == 1 && i == 2) retile_a();
        if (RP == 1 && i == 5) retile_b();
      }
      put(t, 16, e256); put(t, 17, cmul(e256, e[4])); put(t, 18, e384); put(t, 19, cmul(e384, e[4]));
      put(t, 20, cmul(e256, e[2])); put(t, 21, cmul(e256, e[6])); put(t, 22, cmul(e384, e[2])); put(t, 23, cmul(e384, e[6]));
      put(t, 24, e512); put(t, 25, cmul(e512, e[4])); put(t, 26, e640); put(t, 27, cmul(e640, e[4]));
      put(t, 28, e768); put(t, 29, cmul(e768, e[4])); put(t, 30, e896); put(t, 31, cmul(e896, e[4]));
    } else {
      unsigned m = umax(umax(pCr << 1, pCr << 17), umax(pCi << 1, pCi << 17));
      m = dppmax<0xB1>(m); m = dppmax<0x4E>(m); m = dppmax<0x141>(m); m = dppmax<0x140>(m);
      m = dppmax<0x142>(m); m = dppmax<0x143>(m);
      const f32x2 e8 = cexp2(z, 8.f), e64 = cexp2(z, 64.f);
      f32x2 a = cexp2(z, 1024.f);
      const f32x2 e128 = cmul(e64, e64), e16 = cmul(e8, e8), e256 = cmul(e128, e128);
      int ec = (int)((unsigned)__builtin_amdgcn_readlane((int)m, 63) >> 24);
      ec = ec < 5 ? 5 : ec;
      const unsigned db = (unsigned)__builtin_amdgcn_readfirstlane((int)asu(dt));
      int ei = ec + (int)(db >> 23) - 131;
      ei = ei < 0 ? 0 : (ei > 254 ? 254 : ei);
      *(LAS unsigned*)(L + L_INV + 4 * cw) = (unsigned)ei << 23;
      f32x2 v = f32x2{HALF(pCr), HALF(pCi)} * asf((db & 0x007fffffu) | ((unsigned)(259 - ec) << 23));
      f32x2 v1 = cmul(v, e8), a1 = cmul(a, e128);
      if (RP == 1) retile_a();
#pragma unroll
      for (int j = 0; j < 4; ++j) {
        putc(t, T_B + 2 * j, v); putc(t, T_B + 2 * j + 1, v1);
        putc(t, T_B + 8 + 2 * j, cmul(v, e64)); putc(t, T_B + 9 + 2 * j, cmul(v1, e64));
        put(t, 32 + 2 * j, a); put(t, 33 + 2 * j, a1);
        a = cmul(a, e256);
        if (j < 3) { v = cmul(v, e16); v1 = cmul(v1, e16); a1 = cmul(a1, e256); }
        if (RP == 1 && j == 0) retile_b();
      }
      put(t, 40, a);
    }
  }
  FENCE();
  TS(41);
  if (RP == 2) { retile_a(); retile_b(); }
  TS(3);
  lptr ta = L + L_T + cw * T_CH + (16 * role + n16) * T_ROW + 16 * g4, tb = L + L_T + cw * T_CH + (T_B + n16) * T_ROW + 16 * g4;
  if (role == 3) tb = L + L_HX + 2048 + (w & 1) * 256 + 4 * l;
  asm volatile("" : "+v"(ta), "+v"(tb));
  if (!PUB) VMWAIT(1);
  TS(4);
  BAR();
  TS(38);
  float csum;
  int f2o = OOB;
  if (role < 3) {
    v4u fr[8], fb[8];
#pragma unroll
    for (int ks = 0; ks < 8; ++ks) { fr[ks] = *(LAS v4u*)(ta + 64 * ks); fb[ks] = *(LAS v4u*)(tb + 64 * ks); }
    SBAR();
    f32x4 acc = {0.f, 0.f, 0.f, 0.f};
#pragma unroll
    for (int ks = 0; ks < 8; ++ks) acc = mfma16h(fr[ks], fb[ks], acc);
#pragma unroll
    for (int r = 0; r < 4; ++r) *(LAS _Float16*)(uintptr_t)kco[r] = (_Float16)acc[r];
    csum = 0.f;
  } else {
    csum = (*(LAS float*)(tb) + *(LAS float*)(tb + 512)) + (*(LAS float*)(tb + 1024) + *(LAS float*)(tb + 1536));
    if (retile) f2o = g4 * 2048 + n16 * 8;
  }
  FENCE();
#define F2ST() st8(v2u{asu(csum), tag}, rs, f2o, WS_F2 + cg * 8192 + (bid & 7) * 256 + (w & 1) * 128)
  if (!PUB) F2ST();
  FENCE();
  TS(39);
  const int gxs = WS_XT + cq * XT_Q + 4096 * w;
  v4u gx[4];
  asm volatile("buffer_load_dwordx4 %0, %4, %5, %6 offen sc1\n\tbuffer_load_dwordx4 %1, %4, %5, %6 offen offset:1024 sc1\n\t"
               "buffer_load_dwordx4 %2, %4, %5, %6 offen offset:2048 sc1\n\tbuffer_load_dwordx4 %3, %4, %5, %6 offen offset:3072 sc1"
               : "=&v"(gx[0]), "=&v"(gx[1]), "=&v"(gx[2]), "=&v"(gx[3]) : "v"(l16), "s"(rs), "s"(gxs) : "memory");
  FENCE();
  TS(40);
  if (PUB) VMW(MK == 2 ? 9 : 5);
  TS(37);
  BAR();
  if (PUB) F2ST();
  FENCE();
  TS(5);
  {
    unsigned P[6], Q[5];
#pragma unroll
    for (int j = 0; j < 6; ++j) {
      const f16x2 e = {*(LAS _Float16*)(uintptr_t)eo[2 * j], j < 5 ? *(LAS _Float16*)(uintptr_t)eo[2 * j + 1] : (_Float16)0.f};
      P[j] = __builtin_bit_cast(unsigned, e);
    }
#pragma unroll
    for (int j = 0; j < 5; ++j) Q[j] = __builtin_amdgcn_alignbit(P[j + 1], P[j], 16);
    lptr kx = L + (cw ? L_KX1 : L_KX0) + 16 * (tid & 255);
    *(LAS v4u*)(kx) = v4u{P[0], P[1], P[2], P[3]};
    *(LAS v4u*)(kx + KX_CP) = v4u{Q[0], Q[1], Q[2], Q[3]};
    *(LAS v4u*)(kx + 2 * KX_CP) = v4u{P[1], P[2], P[3], P[4]};
    *(LAS v4u*)(kx + 3 * KX_CP) = v4u{Q[1], Q[2], Q[3], Q[4]};
  }
  const v4u wdf = cat(trd(L + L_RD + w * 1024 + q4 * 128 + 32 * g4 + 8 * p4), trd(L + L_RD + w * 1024 + q4 * 128 + 32 * g4 + 8 * p4 + 512));
  FENCE();
  TS(6);
  asm volatile("s_waitcnt vmcnt(%4)" : "+v"(gx[0]), "+v"(gx[1]), "+v"(gx[2]), "+v"(gx[3]) : "n"(PUB));
  TS(7);
#define GMISS() ((neq3(gx[0].w, gx[1].w, tag) | neq3(gx[2].w, gx[3].w, tag)) != 0u)
  if (anylane(GMISS())) {
    int it = 0;
    do {
      asm volatile("" ::: "memory");
      if (GMISS()) {
#pragma unroll
        for (int i = 0; i < 4; ++i) gx[i] = ld16(rs, l16 + 1024 * i, gxs);
      }
    } while (anylane(GMISS()) && ++it < SPIN_CAP);
  }
  FENCE();
  TS(8);
  unsigned up[4];
  {
#pragma unroll
    for (int i = 0; i < 4; ++i) up[i] = (cp & 1) ? gx[i].y : gx[i].x;
    lptr uw = L + L_U + 480 + 512 * w + 8 * l;
    *(LAS v2u*)(uw) = v2u{__builtin_amdgcn_perm(up[1], up[0], 0x05040100u), __builtin_amdgcn_perm(up[3], up[2], 0x05040100u)};
    *(LAS v2u*)(uw + U_CH) = v2u{__builtin_amdgcn_perm(up[1], up[0], 0x07060302u), __builtin_amdgcn_perm(up[3], up[2], 0x07060302u)};
  }
  FENCE();
  st16(wdf, rs, (retile ? g4 * 1024 + n16 * 16 : OOB) + WS_WDF + (w * 56 + (rbid >> 2)) * 4096 + (rbid & 3) * 256, 0);
  FENCE();
  if (MK == 1) HMARK();
  const unsigned cnt = lds0 + L_CNT;
  lptr kA = L + (cw ? L_KX1 : L_KX0) + (3 - (n16 & 3)) * KX_CP + 2 * (12 - 4 * (n16 >> 2) + 8 * g4) + 64 * (6 - 2 * rg);
  lptr uB = L + L_U + cw * U_CH + 32 * n16 + 16 * g4 + 384 - 128 * rg;
  lptr redw = L + L_RED + w * 1024 + 16 * (4 * n16 + (g4 ^ ((n16 >> 1) & 3)));
  lptr redr = L + L_RED + w * 8192 + 16 * ((l & ~3) + (p4 ^ ((l >> 3) & 3)));
  int wvo = (w > 0 ? l16 : OOB) + WS_W1F + fgW * 32768;
  asm volatile("" : "+v"(kA), "+v"(uB), "+v"(redw), "+v"(redr), "+v"(wvo), "+v"(hfo));
  TS(42);
  BAR();
  TS(9);
#define AFRAG(a, rr) cat(*(volatile LAS v2u*)(kA + 64 * (57 - 8 * (a) - (rr))), *(volatile LAS v2u*)(kA + 64 * (57 - 8 * (a) - (rr)) + 8))
#define BFRAG(b, rr) (*(LAS v4u*)(uB + 512 * (b) + 64 * (1 - (rr))))
  const int rot = (idx >> 2) * 4096;
#define WALD(j) ld16(rs, wvo + ((j) & 3) * 1024, (((j) >> 2) * 4096) ^ rot)
  v4u wa[32];
  float hb0[4], hb1[4];
  auto conv_out = [&](int c) {
    const f32x2 invS = *(LAS f32x2*)(L + L_INV);
    f32x4 y[8];
    int it = 0;
    unsigned done;
    do {
      done = *(volatile LAS unsigned*)(L + L_CNT + 4 * c);
      FENCE();
#pragma unroll
      for (int v = 0; v < 8; ++v) y[v] = *(LAS f32x4*)(redr + 1024 * v);
      FENCE();
    } while (below(done, 8) && ++it < (1 << 22));
    float h0[4], h1[4];
#pragma unroll
    for (int i = 0; i < 4; ++i) {
      h0[i] = __builtin_fmaf(((y[0][i] + y[1][i]) + y[2][i]) + y[3][i], invS.x, hb0[i]);
      h1[i] = __builtin_fmaf(((y[4][i] + y[5][i]) + y[6][i]) + y[7][i], invS.y, hb1[i]);
    }
    st16(v4u{umin(pkbf_s(h0[0], h0[1]), SENT - 1), pkbf_s(h0[2], h0[3]), umin(pkbf_s(h1[0], h1[1]), SENT - 1), pkbf_s(h1[2], h1[3])}, rs, hto, 0);
  };
  {
    v4u A[2][8], B[2][8];
    f32x4 t[8];
#pragma unroll
    for (int i = 0; i < 8; ++i) t[i] = f32x4{0.f, 0.f, 0.f, 0.f};
#define MM(a, b, rr) t[(a) + (b)] = mfma16h(A[rr][a], B[rr][b], t[(a) + (b)])
#define MM2(a, b) do { MM(a, b, 0); MM(a, b, 1); } while (0)
#define LDA(a) do { A[0][a] = AFRAG(a, 0); A[1][a] = AFRAG(a, 1); } while (0)
#define LDB(b) do { B[0][b] = BFRAG(b, 0); B[1][b] = BFRAG(b, 1); } while (0)
    LDB(0); LDA(0); SBAR(); LDB(1); LDA(1); SBAR(); LDB(2); LDA(2); SBAR();
#pragma unroll
    for (int i = 0; i < 4; ++i) {
      const f16x2 u = __builtin_bit_cast(f16x2, up[i]);
      const float m = bflo(gx[i].z), sd = bfhi(gx[i].z);
      hb0[i] = __builtin_fmaf((float)u.x, sd + bflo(pds), m); hb1[i] = __builtin_fmaf((float)u.y, sd + bfhi(pds), m);
      asm volatile("" : "+v"(hb0[i]), "+v"(hb1[i]));
    }
    SBAR();
    v4u f2a, f2b;
#define F2LD() do { f2a = ld16(rs, l16, WS_F2 + fgW * 2048); f2b = ld16(rs, l16, WS_F2 + fgW * 2048 + 1024); } while (0)
    if (F2P == 0) F2LD();
    FENCE();
    MM2(0, 0); SBAR();
    LDB(3); SBAR();
    MM2(0, 1); SBAR();
    LDA(3); SBAR();
    MM2(1, 0); SBAR();
    st4(tag, rs, (bid == 0 && tid == 0) ? WS_EPOCH : OOB, 0);
    SBAR();
    MM2(1, 1); SBAR();
    *(LAS f32x4*)(redw) = t[0]; *(LAS f32x4*)(redw + 8192) = t[1];
    FENCE();
    arrive<C_S1>(cnt);
    if (F2P == 1) F2LD();
    FENCE();
    MM2(0, 2); MM2(2, 0); SBAR();
    TS(10);
    if (w < 2) conv_out(C_S1);
    FENCE();
    MM2(1, 2); MM2(2, 1); SBAR();
    LDB(4); SBAR();
    TS(43);
#define FMISS() anylane((neq3(f2a.y, f2a.w, tag) | neq3(f2b.y, f2b.w, tag)) != 0u)
    if (FMISS()) {
      int it = 0;
      do {
        asm volatile("" ::: "memory");
        F2LD();
      } while (FMISS() && ++it < SPIN_CAP);
    }
    FENCE();
    TS(11);
#define WA(n) do { if ((n) % WST == 0) { wa[(n) / WST] = WALD((n) / WST); SBAR(); } } while (0)
    WA(0);
    MM2(0, 3); SBAR();
    WA(1);
    MM2(3, 0); SBAR();
    WA(2); LDA(4); SBAR();
    MM2(1, 3); SBAR();
    *(LAS f32x4*)(redw + 16384) = t[2]; *(LAS f32x4*)(redw + 24576) = t[3];
    FENCE();
    arrive<C_S2>(cnt);
    WA(3);
    MM2(2, 2); SBAR();
    WA(4);
    *(LAS float*)(L + L_CSW + w * 128 + 8 * n16 + 4 * (l >> 5)) = x16_add(fold32(asf(f2a.x) + asf(f2b.x), asf(f2a.z) + asf(f2b.z)));
    SBAR();
    MM2(3, 1); SBAR();
    WA(5);
    if (w == 2 || w == 3) conv_out(C_S2);
    FENCE();
    MM2(2, 3); SBAR();
    WA(6);
    MM2(3, 2); SBAR();
    WA(7);
    MM2(3, 3); SBAR();
    WA(8); LDB(5); SBAR();
    MM2(3, 4); SBAR();
    WA(9);
    MM2(2, 4); SBAR();
    WA(10); LDA(5); SBAR();
    MM2(1, 4); SBAR();
    WA(11);
    MM2(0, 4); SBAR();
    WA(12);
    MM2(4, 3); SBAR();
    WA(13); LDB(6); SBAR();
    MM2(4, 2); SBAR();
    WA(14);
    MM2(4, 1); SBAR();
    WA(15); LDA(6); SBAR();
    MM2(4, 0); SBAR();
    WA(16);
    MM2(2, 5); SBAR();
    *(LAS f32x4*)(redw + 32768) = t[4]; WA(17); LDB(7); SBAR();
    MM2(1, 5); SBAR();
    WA(18);
    MM2(0, 5); SBAR();
    WA(19); LDA(7); SBAR();
    MM2(5, 0); SBAR();
    WA(20);
    MM2(5, 1); SBAR();
    *(LAS f32x4*)(redw + 40960) = t[5]; WA(21);
    MM2(5, 2); SBAR();
    WA(22);
    MM2(0, 6); SBAR();
    WA(23);
    MM2(6, 0); SBAR();
    WA(24);
    MM2(1, 6); SBAR();
    *(LAS f32x4*)(redw + 49152) = t[6]; WA(25);
    MM2(6, 1); SBAR();
    WA(26);
    MM2(0, 7); SBAR();
    WA(27);
    MM2(7, 0); SBAR();
    WA(28);
    *(LAS f32x4*)(redw + 57344) = t[7];
  }
  FENCE();
  arrive<C_S3>(cnt);
  TS(12);
  const int tso = WS_HT + rb * 32768;
  auto tile_dma = [&](int s, int c) {
    const int so = tso + s * (16 * 32768) + ((c * 4096) ^ rot);
    const unsigned d = lds0 + s * 32768 + c * 4096;
    dma<0>(rs, l16, so, d); dma<1024>(rs, l16, so, d); dma<2048>(rs, l16, so, d); dma<3072>(rs, l16, so, d);
  };
  auto tile_peek = [&](int s, int c) {
    lptr q = L + s * 32768 + c * 4096 + l16;
    return v4u{*(volatile LAS unsigned*)(q), *(volatile LAS unsigned*)(q + 1024), *(volatile LAS unsigned*)(q + 2048), *(volatile LAS unsigned*)(q + 3072)};
  };
#define TMISS(m) anylane(umax(umax(m.x, m.y), umax(m.z, m.w)) == SENT)
  auto tile_check = [&](v4u m, int s, int c) {
    if (TMISS(m)) {
      int it = 0;
      do {
        tile_dma(s, c);
        VMWAIT(0);
        m = tile_peek(s, c);
      } while (TMISS(m) && ++it < SPIN_CAP);
    }
    FENCE();
  };
  if (cw == 0) { tile_dma(0, 2 * w); tile_dma(0, 2 * w + 1); }
  FENCE();
#pragma unroll
  for (int j = WC; j < 32; ++j) { wa[j] = WALD(j); SBAR(); }
  v2u cA;
  {
    const float c = l < 32 ? *(LAS float*)(L + L_CSW + w * 128 + 4 * (l & 31)) : 0.f;
    const float hi = rbf(c), mid = rbf(c - hi);
    cA = v2u{pkbf(hi, mid), pkbf((c - hi) - mid, 0.f)};
    PIN(cA);
  }
  FENCE();
  if (cw) {
    arrive<C_T0>(cnt);
    conv_out(C_S3);
    FENCE();
    TS(13);
  } else {
    TS(13);
    VMW(32 - WC);
    FENCE();
    const v4u m0 = tile_peek(0, 2 * w), m1 = tile_peek(0, 2 * w + 1);
    tile_check(m0, 0, 2 * w); tile_check(m1, 0, 2 * w + 1);
    arrive<C_T0>(cnt);
  }
  FENCE();
  TS(14);
  const int g2r = xcd * 4 + (idx >> 3), db = idx & 7, tbo = g2r >> 1, th = g2r & 1;
  const int wdo = WS_WDF + (db * 56 + 7 * w) * 4096;
  const int hvo = (g4 >> 1) * 1024 + ((g4 & 1) * 32 + th * 16 + n16) * 16;
  const int hso = WS_HF + (tbo * 112 + 14 * w) * 1024;
  const int rch = 64 * db + 16 * rg + (l >> 2);
  const int hro = WS_HT + (16 * cw + tbo) * 32768 + (rch >> 1) * 128 + (((4 * th) ^ ((rch >> 1) & 4)) + p4) * 16 + (rch & 1) * 8;
#define WDSO(j) (wdo + ((j) >> 2) * 4096 + ((((j) & 3) + rg) & 3) * 1024)
#define WDLD(j) ld16(rs, (KO & 2) ? OOB : l16, WDSO(j))
#define SLOT(j) ((j) < 12 ? L_KX1 - 2048 + w * 2048 + ((j) - 10) * 1024 : ((j) == 12 ? L_XC - 1024 + w * 1024 : ((j) - 13) / 4 * 32768 + w * 4096 + ((j) - 13) % 4 * 1024))
#define HLD(s, ks) ld16(rs, (KO & 4) ? OOB : hvo, hso + (s) * HF_S + (ks) * 2048)
#define FHLD() ld4(rs, 4 * (n16 < 14 ? n16 : 13), WS_FH + tbo * 512 + 56 * w)
#define FHST() st4(tag, rs, l == 0 ? 0 : OOB, WS_FH + rb * 512 + 4 * fgW)
  v4u wd[28], hb[3][7];
  v2u hres[2];
  unsigned fh;
  auto fh_check = [&]() {
    if (anylane(fh != tag)) {
      int it = 0;
      do {
        asm volatile("" ::: "memory");
        fh = FHLD();
      } while (anylane(fh != tag) && ++it < SPIN_CAP);
    }
    FENCE();
  };
  auto tile_wait = [&](unsigned v, int s) {
    lptr c = L + L_CNT + 4 * (C_T0 + s);
    if (below(v, 8)) {
      int it = 0;
      do __builtin_amdgcn_s_sleep(1); while (below(*(volatile LAS unsigned*)c, 8) && ++it < (1 << 22));
    }
    FENCE();
  };
#define TCNT(s) (*(volatile LAS unsigned*)(L + L_CNT + 4 * (C_T0 + (s))))
  if (w > 0) {
    lptr bt = L + (4 * (g4 >> 1) + (q4 >> 1)) * 128 + (4 * ((g4 ^ (g4 >> 1)) & 1) + p4) * 16 + (q4 & 1) * 8, bt2 = bt + 65536;
    lptr stp = L + L_ST2 + 16 * (l & 31);
    PIN(bt); PIN(bt2); PIN(stp);
#define BLD(g) cat(trd(((g) < 64 ? bt : bt2) + ((g) & 63) * 1024), trd(((g) < 64 ? bt : bt2) + ((g) & 63) * 1024 + 256))
    PRIO(1);
    if (PR4 == 2 && cw) PRIO(2);
    tile_wait(TCNT(0), 0);
    TS(15);
    f32x16 acc[2];
    v4u bf[4];
    f32x4 st;
    float gt[2], gu[2], ge[2], go[2];
    unsigned ho[4], tc, t5;
#define DMA5(t) dma<0>(rs, l16, tso + (t) * (16 * 32768) + rot + rg * 1024, lds0 + (t) * 32768 + rg * 1024)
#define PEEK5(t) (*(volatile LAS unsigned*)(L + (t) * 32768 + rg * 1024 + l16))
    v4u tm;
    u64 tq[8];
    auto st_check = [&](int s) {
      if (anylane(st.w == 0.f)) {
        int it = 0;
        do {
          __builtin_amdgcn_s_sleep(1);
          st = *(volatile LAS f32x4*)(stp + s * 512);
        } while (anylane(st.w == 0.f) && ++it < (1 << 22));
      }
      FENCE();
      acc[s & 1] = mfma32k8(cA, v2u{asu(st.x), asu(st.x)}, acc[s & 1]);
    };
    auto epi = [&](int s, int j, int stage) {
      const float g0 = acc[s & 1][2 * j], g1 = acc[s & 1][2 * j + 1];
      if (stage == 0) { gt[0] = __builtin_fmaf(g0 * g0, st.z, st.y); gt[1] = __builtin_fmaf(g1 * g1, st.z, st.y); PIN(gt[0]); PIN(gt[1]); }
      if (stage == 1) { gt[0] *= g0; gt[1] *= g1; gu[0] = g0 * acc[s & 1][8 + 2 * j]; PIN(gt[0]); PIN(gt[1]); PIN(gu[0]); }
      if (stage == 2) { ge[0] = __builtin_amdgcn_exp2f(gt[0]); gu[1] = g1 * acc[s & 1][9 + 2 * j]; PIN(ge[0]); PIN(gu[1]); }
      if (stage == 3) { ge[1] = __builtin_amdgcn_exp2f(gt[1]); ge[0] = __builtin_fmaf(ge[0], st.w, st.w); PIN(ge[0]); PIN(ge[1]); }
      if (stage == 4) { ge[0] = __builtin_amdgcn_rcpf(ge[0]); ge[1] = __builtin_fmaf(ge[1], st.w, st.w); PIN(ge[0]); PIN(ge[1]); }
      if (stage == 5) { ge[1] = __builtin_amdgcn_rcpf(ge[1]); go[0] = gu[0] * ge[0]; PIN(go[0]); PIN(ge[1]); }
      if (stage == 6) { go[1] = gu[1] * ge[1]; PIN(go[1]); }
      if (stage == 7) { ho[j] = pkbf(go[0], go[1]); PIN(ho[j]); }
    };
#define HST(s) st16(v4u{umin(ho[0], SENT - 1), ho[1], ho[2], ho[3]}, rs, hfo, (s) * HF_S)
#pragma unroll
    for (int g = 0; g < 3; ++g) bf[g] = BLD(g);
    SBAR();
    sfor<128>([&](auto G) __attribute__((always_inline)) {
      constexpr int g = G.v, s = g >> 5, ks = g & 31;
      if constexpr (ks == 0) {
#pragma unroll
        for (int r = 0; r < 16; ++r) acc[s & 1][r] = 0.f;
      }
      if constexpr (g + 3 < 128) bf[(g + 3) & 3] = BLD(g + 3);
      acc[s & 1] = mfma32b(wa[ks], bf[g & 3], acc[s & 1]);
      SBAR();
      if constexpr (s > 0) {
        constexpr int ke = ks - EO;
        if constexpr (ke == (EO ? -1 : 0)) st = *(volatile LAS f32x4*)(stp + (s - 1) * 512);
        if constexpr (ke == 1) st_check(s - 1);
        constexpr int n = ES[s];
        if constexpr (ke >= 3 && ke < hstep(s) - EO) {
          sfor<n>([&](auto R) __attribute__((always_inline)) {
            constexpr int e = (ke - 3) * n + R.v, j = e / 6, q = e % 6;
            if constexpr (q < 2 && j > 0 && j < 5) epi(s - 1, j - 1, 6 + q);
            if constexpr (j < 4) epi(s - 1, j, q);
          });
        }
        if constexpr (ks == hstep(s)) HST(s - 1);
      }
      constexpr int v = g < G_V[1] + 3 ? 1 : (g < G_V[2] + 3 ? 2 : 3), u = g <= G_W[1] ? 1 : (g <= G_W[2] ? 2 : 3);
      if constexpr (g == G_V[v]) { VMW(younger(G_T[v] + 4 * D_T[v], g)); tm = tile_peek(v, w); t5 = PEEK5(v); }
      if constexpr (g == G_V[v] + 2) {
        if (anylane(umax(umax(umax(tm.x, tm.y), umax(tm.z, tm.w)), t5) == SENT)) {
          int it = 0;
          do {
            tile_dma(v, w); DMA5(v);
            VMWAIT(0);
            tm = tile_peek(v, w); t5 = PEEK5(v);
          } while (anylane(umax(umax(umax(tm.x, tm.y), umax(tm.z, tm.w)), t5) == SENT) && ++it < SPIN_CAP);
        }
        FENCE();
        arrive<C_T0 + v>(cnt);
      }
      if constexpr (g == G_W[u] - 2) tc = TCNT(u);
      if constexpr (g == G_W[u]) tile_wait(tc, u);
      if constexpr (op_tile(g) >= 0) {
        constexpr int t = op_tile(g) >> 3;
        if constexpr ((op_tile(g) & 7) < 4) dma<(op_tile(g) & 3) * 1024>(rs, l16, tso + t * (16 * 32768) + ((w * 4096) ^ rot), lds0 + t * 32768 + w * 4096);
        else DMA5(t);
      }
      if constexpr (op_mark(g) >= 0) st16(sent4, rs, (KO & 1) ? OOB : hfo, op_mark(g) * HF_S);
      if constexpr (g == G_FLAG) { VMW(younger(12, g)); FHST(); }
      if constexpr (op_wd(g) >= 0) wd[op_wd(g)] = WDLD(op_wd(g));
      if constexpr (op_stage(g) >= 0) dma<0>(rs, (KO & 2) ? OOB : l16, WDSO(op_stage(g)), lds0 + SLOT(op_stage(g)));
      if constexpr (g == G_FH) fh = FHLD();
      if constexpr (g == 103 && !MK) fh_check();
      if constexpr (op_h(g) >= 0) hb[op_h(g) / 7][op_h(g) % 7] = HLD(op_h(g) / 7, op_h(g) % 7);
      if constexpr (g == 105) VMW(younger(91, g));
      if constexpr (op_back(g) >= 0) wd[op_back(g)] = *(LAS v4u*)(L + SLOT(op_back(g)) + l16);
      if constexpr (g == G_S3) st = *(volatile LAS f32x4*)(stp + 3 * 512);
      if constexpr (g == G_S3 + 2) st_check(3);
      if constexpr (g == G_R0) hres[0] = ld8(rs, hro, 0);
      if constexpr (g == G_R1) hres[1] = ld8(rs, hro, 32 * 32768);
      if constexpr (g == 95) arrive<C_M2>(cnt);
      if constexpr (ks == 31) TS(16 + 2 * s);
      if constexpr (s == LS && (ks & 3) == 3) TQ(tq[ks >> 2]);
      if constexpr (s == LS && ks == 31) { TW(tq[0], 50); TW(tq[1], 51); TW(tq[2], 52); TW(tq[3], 53); TW(tq[4], 54); TW(tq[5], 55); TW(tq[6], 56); TW(tq[7], 57); }
      FENCE();
    });
    arrive<C_M3>(cnt);
    {
      float t[8], u[8];
#define EP(x) _Pragma("unroll") for (int j = 0; j < 8; ++j) { x; } SBAR()
      EP(t[j] = acc[1][j] * acc[1][j]; PIN(t[j]));
      EP(t[j] = __builtin_fmaf(t[j], st.z, st.y); PIN(t[j]));
      EP(t[j] *= acc[1][j]; PIN(t[j]));
      EP(t[j] = __builtin_amdgcn_exp2f(t[j]); u[j] = acc[1][j] * acc[1][8 + j]; PIN(t[j]); PIN(u[j]));
      EP(t[j] = __builtin_fmaf(t[j], st.w, st.w); PIN(t[j]));
      EP(t[j] = __builtin_amdgcn_rcpf(t[j]); PIN(t[j]));
      EP(t[j] *= u[j]; PIN(t[j]));
#pragma unroll
      for (int j = 0; j < 4; ++j) ho[j] = pkbf(t[2 * j], t[2 * j + 1]);
      HST(3);
    }
    FENCE();
    TS(21);
  } else {
    lptr b0 = L + g4 * 512 + (q4 >> 1) * 128 + (g4 & 1) * 64 + p4 * 16 + (q4 & 1) * 8, b1 = L + g4 * 512 + (q4 >> 1) * 128 + ((g4 & 1) ^ 1) * 64 + p4 * 16 + (q4 & 1) * 8;
    v4u ones = {ONES, ONES, ONES, ONES};
    int sel = (l & 1) ? 0xeeeeeeee : 0x44444444;
    PIN(ones); PIN(sel);
    v2u f[4][8];
#define GLD(o, G, i) f[(G) & 3][i] = trd(((i) < 4 ? b0 : b1) + (o) + (G) * 4096 + ((i) & 2) * 1024 + ((i) & 1) * 256)
    if (HP) PRIO(HP);
    arrive<C_T1>(cnt);
    tile_wait(TCNT(0), 0);
    TS(15);
#pragma unroll
    for (int i = 0; i < 16; ++i) GLD(0, i >> 3, i & 7);
    FENCE();
#pragma unroll 1
    for (int s = 0; s < 4; ++s) {
      const int ot = s * 32768, on = ((s + 1) & 3) * 32768;
      f32x4 gq[2] = {{0.f, 0.f, 0.f, 0.f}, {0.f, 0.f, 0.f, 0.f}}, gs[2] = {{0.f, 0.f, 0.f, 0.f}, {0.f, 0.f, 0.f, 0.f}};
      unsigned tc;
#pragma unroll
      for (int G = 0; G < 8; ++G) {
        const v2u* e = f[G & 3];
#define SQ(h, j) do { const v4u t_ = cat(e[4 * (h) + 2 * (j)], e[4 * (h) + 2 * (j) + 1]); gq[h] = mfma16b(t_, t_, gq[h]); SBAR(); } while (0)
#define SM(h) do { const v8u t_ = {e[4 * (h)].x, e[4 * (h)].y, e[4 * (h) + 1].x, e[4 * (h) + 1].y, e[4 * (h) + 2].x, e[4 * (h) + 2].y, e[4 * (h) + 3].x, e[4 * (h) + 3].y}; \
        if (!(KO & 8)) gs[h] = __builtin_amdgcn_smfmac_f32_16x16x64_bf16(__builtin_bit_cast(b16x8, ones), __builtin_bit_cast(b16x16, t_), gs[h], sel, 0, 0); SBAR(); } while (0)
#define NX(i) do { if (G < 6) GLD(ot, G + 2, i); else GLD(on, G - 6, i); SBAR(); } while (0)
#define BB(n) do { if (BUB && (6 * G + (n)) % (BUB ? BUB : 1) == 0) { asm volatile("s_nop 2"); SBAR(); } } while (0)
        SQ(0, 0); NX(0); BB(1);
        SQ(1, 0); NX(1); BB(2);
        SQ(0, 1); NX(4); NX(5); BB(3);
        SM(0); NX(2); BB(4);
        SQ(1, 1); NX(3); BB(5);
        SM(1); NX(6); NX(7); BB(6);
        if (G == 5) { FENCE(); arrive<C_T2>(cnt + 4 * s); tc = TCNT((s + 1) & 3); FENCE(); }
        if ((G & 1) == 0 && s == HWS) {
          FENCE();
#pragma unroll
          for (int j = 0; j < 7; ++j) wd[7 * (G >> 1) + j] = WDLD(7 * (G >> 1) + j);
          FENCE();
        }
        SBAR();
      }
      const float C1 = -2.f * 0.7978845608028654f * 1.4426950408889634f, C2c = C1 * 0.044715f;
      float q[2];
#pragma unroll
      for (int hh = 0; hh < 2; ++hh) q[hh] = (p4 & 2) ? ((p4 & 1) ? gq[hh][3] : gq[hh][2]) : ((p4 & 1) ? gq[hh][1] : gq[hh][0]);
      const auto sw = __builtin_amdgcn_permlane32_swap(asu(q[1]), asu(q[1]), false, false);
      const bool first = q4 == g4;
      const float mean = (first ? gs[0][0] + gs[0][1] : gs[1][0] + gs[1][1]) * (1.f / DM);
      const float var = rbf(fmaxf((first ? q[0] : asf(l < 32 ? sw[1] : sw[0])) * (1.f / DM) - mean * mean, 0.f));
      const float sd = rbf(__builtin_amdgcn_sqrtf(rbf(var + 1e-5f)));
      const float r = __builtin_amdgcn_rcpf(sd);
      if (((q4 ^ g4) & 1) == 0) *(LAS f32x4*)(L + L_ST2 + s * 512 + (first ? 0 : 256) + 16 * n16) = f32x4{asf(pkbf(-mean, -mean)), C1 * r, C2c * r * r * r, sd * sd};
      FENCE();
      if (s == 0) TS(16); else if (s == 1) TS(18); else if (s == 2) TS(20); else TS(22);
      if (below(tc, 8)) {
        tile_wait(tc, s + 1);
#pragma unroll
        for (int i = 0; i < 16; ++i) GLD(on, i >> 3, i & 7);
      }
      FENCE();
    }
    if (HP) PRIO(0);
    if (!MK) fh = FHLD();
    await<C_M2>(L, 8);
    if (!MK) fh_check();
#pragma unroll
    for (int j = 0; j < NH; ++j) hb[j / 7][j % 7] = HLD(j / 7, j % 7);
    hres[0] = ld8(rs, hro, 0);
    hres[1] = ld8(rs, hro, 32 * 32768);
  }
  FENCE();
  PRIO(0);
  {
    unsigned pw[4];
#pragma unroll
    for (int j = 0; j < 4; ++j) pw[j] = lds0 + w * 4096 + n16 * 256 + (((((j + rg) & 3) * 4 + g4) ^ n16) * 16);
    lptr hx = L + L_HX + w * 512 + 8 * l;
    PIN(hx); PIN(pw[0]); PIN(pw[1]); PIN(pw[2]); PIN(pw[3]);
    lptr pr3;
    unsigned oo3;
    f32x4 acc[4];
    v2u ht[2];
#define HMAX(h) umax(umax(umax(h[0].x, h[1].x), umax(h[2].x, h[3].x)), umax(umax(h[4].x, h[5].x), h[6].x))
    auto h_check = [&](unsigned m, int s) {
      v4u* h = hb[s % 3];
      if (anylane(m == SENT)) {
        int it = 0;
        do {
          asm volatile("" ::: "memory");
          if (m == SENT) {
#pragma unroll
            for (int ks = 0; ks < 7; ++ks) h[ks] = HLD(s, ks);
          }
          m = HMAX(h);
        } while (anylane(m == SENT) && ++it < SPIN_CAP);
      }
      FENCE();
    };
#define PWR(c) *(LAS f32x4*)(uintptr_t)(pw[3 - ((c) & 3)] + ((c) >> 2) * 32768) = acc[(c) & 3]
    unsigned hm = HMAX(hb[0]), mc;
    TS(23);
    auto stream = [&](auto M) __attribute__((always_inline)) {
      constexpr int m = M.v, q = m % 14, c = m / 14 * 2 + (m & 1), ks = q / 2, s = c >> 2, dt = 3 - (c & 3);
      if constexpr (m % 28 == 0) { h_check(hm, s); TS(45 + s); }
      if constexpr (ks == 0 && dt > 0) asm volatile("v_mfma_f32_16x16x32_bf16 %0, %1, %2, 0" : "=v"(acc[c & 3]) : "v"(wd[dt]), "v"(hb[s % 3][0]));
      else asm volatile("v_mfma_f32_16x16x32_bf16 %0, %1, %2, %0" : "+v"(acc[c & 3]) : "v"(wd[4 * ks + dt]), "v"(hb[s % 3][ks]));
      SBAR();
      if constexpr (m % 28 == 10) {
        const unsigned lo = cw == (s & 1) ? 0x01000c0cu : 0x0c0c0c0cu, hi = cw == (s & 1) ? 0x03020c0cu : 0x0c0c0c0cu;
        const v2u t = ht[s >> 1];
        acc[3] = f32x4{asf(__builtin_amdgcn_perm(0u, t.x, lo)), asf(__builtin_amdgcn_perm(0u, t.x, hi)), asf(__builtin_amdgcn_perm(0u, t.y, lo)), asf(__builtin_amdgcn_perm(0u, t.y, hi))};
        PIN(acc[3]);
      }
      if constexpr (m == 64) mc = peek<C_M2>(L);
      if constexpr (m == 71) { if (below(mc, 8)) await<C_M2>(L, 8); FENCE(); }
      if constexpr (m == 80) mc = peek<C_M3>(L);
      if constexpr (m == 85) { const int tk = 2 * w + (l >> 5); pr3 = L + 3 * 32768 + tk * 256 + ((((l & 31) >> 1) ^ tk) * 16) + (l & 1) * 8; PIN(pr3); }
      if constexpr (m == 88) { oo3 = (1536 + 16 * g2r + 2 * w + (l >> 5)) * 1024 + 128 * db + 4 * (l & 31); PIN(oo3); }
      if constexpr ((q == 2 || q == 5) && m >= 14 && m / 14 != 6) PWR(c - 2);
      if constexpr (m == 34) { FENCE(); arrive<C_P0>(cnt); }
      if constexpr (m == 62) { FENCE(); arrive<C_P1>(cnt); }
      if constexpr (every(m, 1, D_H2, 21 - NH) >= 0) { constexpr int j = NH + every(m, 1, D_H2, 21 - NH); hb[j / 7][j % 7] = HLD(j / 7, j % 7); }
      if constexpr (m == 2) {
        if (anylane(umax(hres[0].x, hres[1].x) == SENT)) {
          int it = 0;
          do {
            asm volatile("" ::: "memory");
            hres[0] = ld8(rs, hro, 0);
            hres[1] = ld8(rs, hro, 32 * 32768);
          } while (anylane(umax(hres[0].x, hres[1].x) == SENT) && ++it < SPIN_CAP);
        }
        FENCE();
        *(LAS v2u*)(hx) = hres[0];
        FENCE();
        ht[0] = trd(hx);
      }
      if constexpr (m == 3) {
        *(LAS v2u*)(hx) = hres[1];
        FENCE();
        ht[1] = trd(hx);
      }
      if constexpr (every(m, G_H3, D_H3, 7) >= 0) hb[0][every(m, G_H3, D_H3, 7)] = HLD(3, every(m, G_H3, D_H3, 7));
      if constexpr (m == 26 || m == 54) { hm = HMAX(hb[(s + 1) % 3]); PIN(hm); }
      FENCE();
    };
    sfor<84>(stream);
    asm volatile("s_nop 7");
    FENCE();
    PWR(10); PWR(11);
    FENCE();
    arrive<C_P2>(cnt);
    TS(24);
    const int tk = 4 * rg + g4;
    lptr prd = L + tk * 256 + ((n16 ^ tk) * 16);
    GAS v2u* op = (GAS v2u*)((GAS unsigned char*)p.out + (unsigned)((16 * g2r + tk) * 1024 + 128 * db + 8 * n16));
    auto reduce = [&](int s) {
      f32x4 v[8];
#pragma unroll
      for (int j = 0; j < 8; ++j) v[j] = *(LAS f32x4*)(prd + s * 32768 + j * 4096);
      float a[4];
#pragma unroll
      for (int i = 0; i < 4; ++i) {
        a[i] = v[0][i];
#pragma unroll
        for (int j = 1; j < 8; ++j) a[i] += v[j][i];
      }
      op[s * (512 * 128)] = v2u{pkbf_s(a[0], a[1]), pkbf_s(a[2], a[3])};
      FENCE();
    };
    if (cw == 0) {
      await<C_P0>(L, 8);
      reduce(0);
      await<C_P1>(L, 8);
      reduce(1);
      hm = HMAX(hb[0]);
      PIN(hm);
    } else {
      if (below(mc, 8)) await<C_M3>(L, 8);
      hm = HMAX(hb[0]);
      PIN(hm);
    }
    FENCE();
    TS(25);
    sfor<28>([&](auto M) __attribute__((always_inline)) { stream(ic<84 + M.v>{}); });
    asm volatile("s_nop 7");
    FENCE();
    PWR(14); PWR(15);
    FENCE();
    arrive<C_P3>(cnt);
    TS(26);
    if (cw == 0) { await<C_P2>(L, 8); reduce(2); }
    TS(49);
    {
      f32x2 v[8];
      int it = 0;
      unsigned done;
      do {
        done = peek<C_P3>(L);
        FENCE();
#pragma unroll
        for (int j = 0; j < 8; ++j) v[j] = *(volatile LAS f32x2*)(pr3 + j * 4096);
        FENCE();
      } while (below(done, 8) && ++it < (1 << 22));
      const f32x2 a = ((v[0] + v[1]) + (v[2] + v[3])) + ((v[4] + v[5]) + (v[6] + v[7]));
      *(GAS unsigned*)((GAS unsigned char*)p.out + oo3) = pkbf(a.x, a.y);
    }
    TS(27);
  }
  asm volatile(".Lcode_end:");
}

extern "C" void kernel_launch(void* const* d_in, const int* in_sizes, int n_in, void* d_out, int out_size, void* d_ws,
                              size_t ws_size, hipStream_t stream) {
  Params p;
  p.x = (const u16*)d_in[0]; p.logA = (const u16*)d_in[1]; p.aim = (const u16*)d_in[2]; p.cre = (const u16*)d_in[3];
  p.cim = (const u16*)d_in[4]; p.logdt = (const u16*)d_in[5]; p.dskip = (const u16*)d_in[6];
  p.wg = (const u16*)d_in[8]; p.wu = (const u16*)d_in[9]; p.wd = (const u16*)d_in[10];
  p.out = (u16*)d_out; p.ws = (unsigned char*)d_ws;
  (void)hipFuncSetAttribute((const void*)fused_kernel, hipFuncAttributeMaxDynamicSharedMemorySize, L_BYTES);
  fused_kernel<<<dim3(256), dim3(512), L_BYTES, stream>>>(p);
}
```

```cpp
#include <hip/hip_runtime.h>
#include <stdint.h>

typedef unsigned short u16;
typedef unsigned long long u64;
typedef unsigned v4u __attribute__((ext_vector_type(4)));
typedef unsigned v2u __attribute__((ext_vector_type(2)));
typedef float f32x2 __attribute__((ext_vector_type(2)));
typedef float f32x4 __attribute__((ext_vector_type(4)));
typedef float f32x16 __attribute__((ext_vector_type(16)));
typedef short s16x4 __attribute__((ext_vector_type(4)));
typedef _Float16 f16x8 __attribute__((ext_vector_type(8)));
typedef _Float16 f16x2 __attribute__((ext_vector_type(2)));
typedef __bf16 b16x8 __attribute__((ext_vector_type(8)));
typedef __bf16 b16x2 __attribute__((ext_vector_type(2)));
typedef __bf16 b16x16 __attribute__((ext_vector_type(16)));
typedef unsigned v8u __attribute__((ext_vector_type(8)));
#define LAS __attribute__((address_space(3)))
#define GAS __attribute__((address_space(1)))
typedef LAS unsigned char* lptr;

#define CAS __attribute__((address_space(4)))

#define STAMP 0
#define LS 2
#define RP 1
#define F2P 0
#define PUB 1
#define EW 0
#define PR4 0
#define BUB 0
#define EO 0
#define HWS 2
#define ESA 1
#define ESB 2
#define KO 0
#define PFW 0
#define HP 1
#define GH3 48
#define DH3 4
#define DH2 5
#define RO 0
#define MK 0
#define WDL 3
#define ND 0
#define GT1 0
#define DT1 1
#define GV1 22
#define GV2 54
#define GV3 66
#define GFL 45
#define GFH 83
#define GMK 6
#define DMK 2
#define WN 1
#define WD_ 2
#define TDS 99
#define HSK 1

constexpr int SEQ = 2048, DM = 512, NMODE = 128, FF = 1792;
constexpr int SC1 = 16;
constexpr int SPIN_CAP = 20000;
constexpr int OOB = 0x40000000;
constexpr unsigned SENT = 0xffffffffu;

constexpr int WS_EPOCH = 0;
constexpr int WS_FH = 4096;
constexpr int WS_F2 = 65536;
constexpr int WS_HT = 1 << 20;
constexpr int WS_W1F = 3 << 20;
constexpr int WS_WDF = 7 << 20;
constexpr int WS_HF = 10 << 20;
constexpr int HF_S = 16 * 112 * 1024;
constexpr int WS_XT = 17 << 20;
constexpr int XT_Q = 32768 + 256;
constexpr int WS_DBG = 22 << 20;
constexpr int WS_BYTES = 24 << 20;
static_assert(WS_XT + 128 * XT_Q <= WS_DBG, "ws map");

constexpr int L_RT = 0, L_RD = 16384, L_DUMP = 28672;
constexpr int KX_CP = 4160, KX_CH = 4 * KX_CP;
constexpr int L_KX0 = 32768, L_U = L_KX0 + KX_CH, U_CH = 4576, L_KC = L_U + 2 * U_CH;
constexpr int KC0 = 0x3f78, KC_CH = 2 * (0x4548 - KC0);
constexpr int L_T = 65536, T_ROW = 544, T_B = 41, T_CH = 57 * T_ROW;
constexpr int L_RED = 65536;
constexpr int L_KX1 = 131072;
constexpr int L_CNT = L_KX1 + KX_CH;
constexpr int L_ST2 = L_CNT + 128;
constexpr int L_CSW = L_ST2 + 2048;
constexpr int L_INV = L_CSW + 1024;
constexpr int L_HX = L_INV + 16;
constexpr int L_XC = L_HX + 4096, XC_ROW = 1056;
constexpr int L_BYTES = L_XC + 8 * XC_ROW;
static_assert(L_BYTES <= 163840, "lds");
static_assert(L_KC + 2 * KC_CH <= 65536 && 2 * T_CH <= 65536, "lds");
static_assert(L_U % 16 == 0 && L_KC % 16 == 0 && L_CNT % 16 == 0 && T_CH % 16 == 0, "align");
enum { C_S1 = 0, C_S2, C_S3, C_T0, C_T1, C_T2, C_T3, C_M2, C_M3, C_P0, C_P1, C_P2, C_P3 };

struct Params {
  const u16 *x, *logA, *aim, *cre, *cim, *logdt, *dskip, *wg, *wu, *wd;
  u16* out;
  unsigned char* ws;
};

__device__ __forceinline__ float asf(unsigned v) { return __builtin_bit_cast(float, v); }
__device__ __forceinline__ unsigned asu(float v) { return __builtin_bit_cast(unsigned, v); }
__device__ __forceinline__ float bflo(unsigned v) { return asf(v << 16); }
__device__ __forceinline__ float bfhi(unsigned v) { return asf(v & 0xffff0000u); }
__device__ __forceinline__ unsigned pkbf(float lo, float hi) {
  b16x2 r = {(__bf16)lo, (__bf16)hi};
  return __builtin_bit_cast(unsigned, r);
}
__device__ __forceinline__ unsigned pkbf_s(float lo, float hi) {
  asm("" : "+v"(lo));
  asm("" : "+v"(hi));
  return pkbf(lo, hi);
}
__device__ __forceinline__ float rbf(float f) { return bflo(pkbf(f, f)); }
__device__ __forceinline__ unsigned pkh(float lo, float hi) {
  f16x2 r = {(_Float16)lo, (_Float16)hi};
  return __builtin_bit_cast(unsigned, r);
}
template <int C> __device__ __forceinline__ unsigned dppu(unsigned v) {
  return (unsigned)__builtin_amdgcn_update_dpp(0, (int)v, C, 0xf, 0xf, true);
}
template <int C> __device__ __forceinline__ float dppf(float v) { return asf(dppu<C>(asu(v))); }
__device__ __forceinline__ float x16_add(float v) {
  auto s = __builtin_amdgcn_permlane16_swap(asu(v), asu(v), false, false);
  return asf(s[0]) + asf(s[1]);
}
__device__ __forceinline__ float fold32(float a, float b) {
  auto s = __builtin_amdgcn_permlane32_swap(asu(a), asu(b), false, false);
  return asf(s[0]) + asf(s[1]);
}
__device__ __forceinline__ float dot2acc(unsigned a, unsigned b, float c) {
  return __builtin_amdgcn_fdot2_f32_bf16(__builtin_bit_cast(b16x2, a), __builtin_bit_cast(b16x2, b), c, false);
}
__device__ __forceinline__ unsigned umax(unsigned a, unsigned b) { return a > b ? a : b; }
__device__ __forceinline__ unsigned umin(unsigned a, unsigned b) { return a < b ? a : b; }
constexpr unsigned ONES = 0x3f803f80u;
#define BAR() asm volatile("s_waitcnt lgkmcnt(0)\n\ts_barrier" ::: "memory")
#define VMWAIT(N) asm volatile("s_waitcnt vmcnt(" #N ")" ::: "memory")
#define SBAR() __builtin_amdgcn_sched_barrier(0)
#define FENCE() do { asm volatile("" ::: "memory"); __builtin_amdgcn_sched_barrier(0); } while (0)
#define PRIO(n) __builtin_amdgcn_s_setprio(n)

__device__ __forceinline__ void glds1(const void* g, unsigned dst) {
  unsigned sv;
  asm volatile("s_mov_b32 %0, m0\n\ts_mov_b32 m0, %2\n\ts_nop 0\n\tglobal_load_lds_dwordx4 %1, off\n\ts_mov_b32 m0, %0"
               : "=&s"(sv) : "v"(g), "s"(dst) : "memory");
}
__device__ __forceinline__ v2u trd(lptr p) {
  return __builtin_bit_cast(v2u, __builtin_amdgcn_ds_read_tr16_b64_v4i16((LAS s16x4*)p));
}
__device__ __forceinline__ v4u cat(v2u a, v2u b) { return v4u{a.x, a.y, b.x, b.y}; }
__device__ __forceinline__ f32x4 mfma16h(v4u a, v4u b, f32x4 c) {
  return __builtin_amdgcn_mfma_f32_16x16x32_f16(__builtin_bit_cast(f16x8, a), __builtin_bit_cast(f16x8, b), c, 0, 0, 0);
}
__device__ __forceinline__ f32x4 mfma16b(v4u a, v4u b, f32x4 c) {
  return __builtin_amdgcn_mfma_f32_16x16x32_bf16(__builtin_bit_cast(b16x8, a), __builtin_bit_cast(b16x8, b), c, 0, 0, 0);
}
__device__ __forceinline__ f32x16 mfma32b(v4u a, v4u b, f32x16 c) {
  return __builtin_amdgcn_mfma_f32_32x32x16_bf16(__builtin_bit_cast(b16x8, a), __builtin_bit_cast(b16x8, b), c, 0, 0, 0);
}
typedef __amdgpu_buffer_rsrc_t rsrc_t;
__device__ __forceinline__ v4u ld16(rsrc_t rs, int voff, int soff) { return __builtin_amdgcn_raw_buffer_load_b128(rs, voff, soff, SC1); }
__device__ __forceinline__ unsigned ld4(rsrc_t rs, int voff, int soff) { return __builtin_amdgcn_raw_buffer_load_b32(rs, voff, soff, SC1); }
__device__ __forceinline__ v2u ld8(rsrc_t rs, int voff, int soff) { return __builtin_amdgcn_raw_buffer_load_b64(rs, voff, soff, SC1); }
__device__ __forceinline__ void st16(v4u v, rsrc_t rs, int voff, int soff) { __builtin_amdgcn_raw_buffer_store_b128(v, rs, voff + soff, 0, SC1); }
__device__ __forceinline__ void st8(v2u v, rsrc_t rs, int voff, int soff) { __builtin_amdgcn_raw_buffer_store_b64(v, rs, voff, soff, SC1); }
__device__ __forceinline__ void st4(unsigned v, rsrc_t rs, int voff, int soff) { __builtin_amdgcn_raw_buffer_store_b32(v, rs, voff, soff, SC1); }
template <int OFF> __device__ __forceinline__ void dma(rsrc_t rs, int voff, int soff, unsigned dst) {
  asm volatile("s_mov_b32 m0, %3\n\ts_nop 0\n\tbuffer_load_dwordx4 %0, %1, %2 offen offset:%4 sc1 lds"
               :: "v"(voff), "s"(rs), "s"(soff), "s"(dst), "n"(OFF) : "memory");
}
__device__ __forceinline__ f32x16 mfma32k8(v2u a, v2u b, f32x16 c) {
  return __builtin_amdgcn_mfma_f32_32x32x8bf16_1k(__builtin_bit_cast(s16x4, a), __builtin_bit_cast(s16x4, b), c, 0, 0, 0);
}
template <int V> struct ic { static constexpr int v = V; };
template <class T, T... I> struct iseq {
  template <class F> static __device__ __forceinline__ void run(F&& f) { (f(ic<I>{}), ...); }
};
template <int N, class F> __device__ __forceinline__ void sfor(F&& f) { __make_integer_seq<iseq, int, N>::run(f); }
constexpr int wcnt(int n) { return n < 0 ? 0 : (n * WN / WD_ + 1 < 32 ? n * WN / WD_ + 1 : 32); }
constexpr int WC = wcnt(28);
constexpr int every(int g, int a, int d, int n) { return g >= a && (g - a) % d == 0 && (g - a) / d < n ? (g - a) / d : -1; }
constexpr int G_T[4] = {0, GT1, 29, 35}, D_T[4] = {1, DT1, 1, 2};
constexpr int G_V[4] = {0, GV1, GV2, GV3}, G_W[4] = {0, 28, 60, 84};
constexpr int op_tile(int g) { for (int s = 1; s < 4; ++s) if (every(g, G_T[s], D_T[s], 5) >= 0) return 8 * s + every(g, G_T[s], D_T[s], 5); return -1; }
constexpr int op_wa(int g) {
  if (op_tile(g) >= 0) return -1;
  int n = 0;
  for (int i = 0; i < g; ++i) n += op_tile(i) < 0;
  return n < ND ? 32 - ND + n : -1;
}
constexpr int wa_at(int j) { for (int g = 0; g < 32; ++g) if (op_wa(g) == j) return g; return -1; }
constexpr int op_mark(int g) { return MK ? -1 : every(g, GMK, DMK, 4); }
constexpr int G_FLAG = MK ? -1 : GFL, G_FH = MK ? -1 : GFH;
constexpr int op_wd(int g) {
  if (every(g, 55, 2, 3) >= 0) return every(g, 55, 2, 3);
  if (every(g, 69, 2, 7) >= 0) return 3 + every(g, 69, 2, 7);
  if (every(g, 97, 1, 7) >= 0) return 21 + every(g, 97, 1, 7);
  return -1;
}
constexpr int op_stage(int g) {
  if (every(g, 49, 2, 3) >= 0) return 10 + every(g, 49, 2, 3);
  if (every(g, 61, 2, 4) >= 0) return 13 + every(g, 61, 2, 4);
  if (every(g, 85, 2, 4) >= 0) return 17 + every(g, 85, 2, 4);
  return -1;
}
constexpr int op_h(int g) {
  if (every(g, 104, 2, 4) >= 0) return every(g, 104, 2, 4);
  if (every(g, 111, 2, 9) >= 0) return 4 + every(g, 111, 2, 9);
  return -1;
}
constexpr int op_back(int g) {
  if (every(g, 105, 2, 3) >= 0) return 10 + every(g, 105, 2, 3);
  if (every(g, 112, 2, 8) >= 0) return 13 + every(g, 112, 2, 8);
  return -1;
}
constexpr int NH = 13, G_H3 = GH3, D_H3 = DH3, D_H2 = DH2;
constexpr int G_R0 = 124, G_R1 = 126, G_S3 = 121;
constexpr int ES[4] = {1, ESA, ESA, ESB};
constexpr int hstep(int s) { return (EO + 3 + (26 + ES[s] - 1) / ES[s] + 1) & ~1; }
constexpr int nv(int g) {
  return (op_tile(g) >= 0) + (op_wa(g) >= 0) + (op_mark(g) >= 0) + (g == G_FLAG) + (g == G_FH) + (g == G_R0) + (g == G_R1) + (g > 32 && (g & 31) == hstep(g >> 5)) + (op_wd(g) >= 0) + (op_stage(g) >= 0) + (op_h(g) >= 0);
}
constexpr bool single() { for (int g = 0; g < 128; ++g) if (nv(g) > 1) return false; return true; }
static_assert(single(), "schedule");
constexpr int younger(int a, int b) { int n = 0; for (int g = a + 1; g < b; ++g) n += nv(g); return n; }
#define VMW(N) asm volatile("s_waitcnt vmcnt(%0)" :: "n"(N) : "memory")
#define VMWC(N) __builtin_amdgcn_s_waitcnt(0x0f70 | (N))
#define PIN(x) asm volatile("" : "+v"(x))
template <class T> __device__ __forceinline__ T gld(const void* base, unsigned off) { return *(const GAS T*)((const GAS unsigned char*)base + off); }

template <int C> __device__ __forceinline__ void arrive(unsigned cnt) {
  asm volatile("s_mov_b64 exec, 1\n\tds_add_u32 %0, %1 offset:%2\n\ts_mov_b64 exec, -1"
               :: "v"(cnt), "v"(1u), "n"(4 * C) : "memory");
  __builtin_amdgcn_sched_barrier(0);
}
template <int C> __device__ __forceinline__ unsigned peek(lptr L) { return *(volatile LAS unsigned*)(L + L_CNT + 4 * C); }
__device__ __forceinline__ bool below(unsigned v, unsigned n) { return (unsigned)__builtin_amdgcn_readfirstlane(v) < n; }
template <int C> __device__ __forceinline__ void await(lptr L, unsigned n) {
  if (below(peek<C>(L), n)) {
    int it = 0;
    do __builtin_amdgcn_s_sleep(1); while (below(peek<C>(L), n) && ++it < (1 << 22));
  }
  asm volatile("" ::: "memory");
}
__device__ __forceinline__ unsigned neq3(unsigned a, unsigned b, unsigned t) { return __builtin_amdgcn_bitop3_b32(a, b, t, 0x7e); }
__device__ __forceinline__ bool anylane(bool c) { return __builtin_amdgcn_ballot_w64(c) != 0; }

__device__ __forceinline__ void tabaddr(unsigned c, unsigned base, unsigned& lo, unsigned& hi) {
  asm volatile("v_mad_u32_u16 %0, %1, 2, %2" : "=v"(lo) : "v"(c), "s"(base));
  asm volatile("v_mad_u32_u16 %0, %1, 2, %2 op_sel:[1,0,0,0]" : "=v"(hi) : "v"(c), "s"(base));
}
__device__ __forceinline__ f32x2 cmul(f32x2 a, f32x2 b) {
  return __builtin_elementwise_fma(f32x2{a.y, a.y}, f32x2{-b.y, b.x}, f32x2{a.x, a.x} * b);
}
__device__ __forceinline__ f32x2 cexp2(f32x2 z, float p) {
  const f32x2 v = z * p;
  const float f = __builtin_amdgcn_fractf(v.y);
  return f32x2{__builtin_amdgcn_cosf(f), __builtin_amdgcn_sinf(f)} * __builtin_amdgcn_exp2f(v.x);
}
__device__ __forceinline__ void put(lptr t, int row, f32x2 v) { *(volatile LAS unsigned*)(t + row * T_ROW) = pkh(v.x, v.y); }
__device__ __forceinline__ void putc(lptr t, int row, f32x2 v) { *(volatile LAS unsigned*)(t + row * T_ROW) = pkh(v.x, -v.y); }
template <int C> __device__ __forceinline__ unsigned dppmax(unsigned v) {
  return umax(v, (unsigned)__builtin_amdgcn_update_dpp(0, (int)v, C, 0xf, 0xf, true));
}

#define TS(i) do {} while (0)
#define TR(i) do {} while (0)
#define TQ(v) do {} while (0)
#define TW(v, i) do {} while (0)

__global__ void __launch_bounds__(512, 2) fused_kernel(Params p) {
  extern __shared__ __attribute__((aligned(16))) unsigned char smem[];
  lptr L = (lptr)smem;
  const unsigned lds0 = (unsigned)(uintptr_t)L;
  const int tid = threadIdx.x, l = tid & 63, w = __builtin_amdgcn_readfirstlane(tid >> 6);
  const int bid = blockIdx.x, xcd = bid & 7, idx = bid >> 3;
  const int cq = (idx >> 1) * 8 + xcd, cp = 2 * cq + (idx & 1), cw = w >> 2, rg = w & 3, ch = 2 * cp + cw;
  const int g4 = l >> 4, q4 = (l >> 2) & 3, p4 = l & 3, n16 = l & 15;
  const int l16 = 16 * l;
  TS(0);
  if (w < 2) {
    u64 pc; unsigned len;
    asm volatile("s_getpc_b64 %0\n.Lcode_here:\n\ts_mov_b32 %1, .Lcode_end-.Lcode_here" : "=s"(pc), "=s"(len));
    const unsigned o = (unsigned)((idx + 32 * w) * 1024 + l16);
    glds1((const void*)((pc & ~15ull) + (o < len ? o : 0u)), lds0 + L_DUMP);
  }
  FENCE();
  asm volatile("" :: "s"(p.x), "s"(p.ws));
  rsrc_t rs = __builtin_amdgcn_make_buffer_rsrc((void*)p.ws, (short)0, WS_BYTES, 0x00020000);

  TS(32);
  v4u xrow;
  asm volatile("global_load_dwordx4 %0, %1, %2" : "=v"(xrow) : "v"((32 * (bid >> 2) + (bid & 3) + 4 * w) * 1024 + l16), "s"(p.x));
  unsigned ep = *(const CAS unsigned*)(p.ws + WS_EPOCH);
  FENCE();
  const v4u sent4 = {SENT, SENT, SENT, SENT};
  const int hto = (l >> 3) * 32768 + ((l & 7) ^ (cp & 4)) * 16 + WS_HT + w * 8 * 32768 + cp * 128;
  st16(sent4, rs, w < 2 ? hto : OOB, 0);
  FENCE();
  unsigned pA, pB, pCr, pCi;
  asm volatile("global_load_dword %0, %4, %5\n\tglobal_load_dword %1, %4, %6\n\tglobal_load_dword %2, %4, %7\n\tglobal_load_dword %3, %4, %8"
               : "=&v"(pA), "=&v"(pB), "=&v"(pCr), "=&v"(pCi) : "v"(ch * 256 + 4 * l), "s"(p.logA), "s"(p.aim), "s"(p.cre), "s"(p.cim));
  unsigned pdt = *(const CAS unsigned*)(p.logdt + 2 * cp);
  unsigned pds = *(const CAS unsigned*)(p.dskip + 2 * cp);
  FENCE();
  const int rb = (xcd & 1) * 8 + (idx >> 2), cb = (xcd >> 1) * 4 + (idx & 3);
  const int fgW = 7 * cb + (w > 0 ? w - 1 : 0);
  int hfo = (w > 0 ? l16 : OOB) + WS_HF + (rb * 112 + fgW) * 1024;
#define HMARK() do { st16(sent4, rs, hfo, 0); st16(sent4, rs, hfo, HF_S); st16(sent4, rs, hfo, 2 * HF_S); st16(sent4, rs, hfo, 3 * HF_S); FENCE(); } while (0)
  const int role = cw ? 3 - rg : rg;
  const bool retile = bid < 224;
  const int rbid = retile ? bid : 0, cg = rbid >> 3;
  int wdr = (16 * (rbid >> 1) + 8 * (l >> 5) + 4 * (rbid & 1) + ((l >> 3) & 3)) * (DM * 2) + 128 * w + 16 * (l & 7);
  auto rawwd = [&]() {
    unsigned sv;
    asm volatile("s_mov_b32 %0, m0\n\ts_mov_b32 m0, %3\n\ts_nop 0\n\tglobal_load_lds_dwordx4 %1, %2\n\ts_mov_b32 m0, %0"
                 : "=&s"(sv) : "v"(wdr), "s"(p.wd), "s"(lds0 + L_RD + w * 1024) : "memory");
  };
  auto rawdma = [&]() {
    const int w1o = (64 * (rbid & 7) + 16 * (w >> 1) + (l >> 3)) * (FF * 2) + 128 * cg + 16 * (l & 7);
    unsigned sv;
    asm volatile("s_mov_b32 %0, m0\n\ts_mov_b32 m0, %4\n\ts_nop 0\n\tglobal_load_lds_dwordx4 %1, %3\n\ts_add_u32 m0, m0, 1024\n\ts_nop 0\n\t"
                 "global_load_lds_dwordx4 %2, %3\n\ts_mov_b32 m0, %0"
                 : "=&s"(sv) : "v"(w1o), "v"(w1o + 8 * FF * 2), "s"((w & 1) ? p.wu : p.wg),
                    "s"(lds0 + L_RT + (w & 1) * 8192 + (w >> 1) * 2048) : "memory", "scc");
    if (WDL == 0) rawwd();
  };
  if (EW) { rawdma(); FENCE(); }
  {
    const v4u z = {0u, 0u, 0u, 0u};
    *(LAS v4u*)(L + L_CNT + 16 * (tid & 255)) = z;
    const int za = (cw ? L_KX1 : L_KX0) + 4096 + rg * KX_CP + p4 * 16;
    const int zb = ((rg & 1) ? L_U - 512 + cw * U_CH : L_KC + cw * KC_CH) + (l16 & ((rg & 1) ? 0x3f0 : 0));
    *(LAS v4u*)(L + (l < 32 ? za : zb)) = z;
  }
  unsigned eo[12], kco[4];
  {
    const unsigned kcb = lds0 + L_KC + cw * KC_CH - 2 * KC0;
    float lf = (float)(2047 - 8 * (tid & 255));
    asm("" : "+v"(lf));
    const float Z0 = asf(0x3f7f0000u), ZN = asf(0x3f7e0000u);
#pragma unroll
    for (int j = 0; j < 12; j += 2) {
      float a = lf - (float)j, b = lf - (float)(j + 1);
      if (j == 6) b = fmaxf(b, Z0);
      if (j >= 8) { a = fmaxf(a, ZN); b = j < 10 ? fmaxf(b, ZN) : a; }
      tabaddr(pkbf_s(a, b), kcb, eo[j], eo[j + 1]);
    }
    const u64 pb = role == 0 ? 0x0084008000040000ull : (role == 1 ? 0x0300020001020100ull : 0x0a01080106010401ull);
    const float d1 = role == 0 ? 1.f : (role == 1 ? 4.f : 128.f), d2 = role == 0 ? 2.f : (role == 1 ? 128.f : 256.f);
    const float p0 = (float)(((unsigned)(pb >> (l & 48)) & 0xffffu) + 8 * n16), p2 = p0 + d2;
    tabaddr(pkbf_s(fmaxf(p0, Z0), p0 + d1), kcb, kco[0], kco[1]);
    tabaddr(pkbf_s(p2, p2 + d1), kcb, kco[2], kco[3]);
  }
  lptr xcw = L + L_XC + w * XC_ROW + l16, xcr = L + L_XC + (l & 7) * XC_ROW + 128 * w + 16 * (l >> 3);
  int xto = (l >> 3) * (2 * XT_Q) + (l & 7) * 16 + WS_XT + 16 * w * XT_Q + ((4 * (bid >> 5) + (bid & 3)) * 8 + ((bid >> 2) & 7)) * 128;
  lptr xcs = L + L_XC + w * XC_ROW + 1024, xct = L + L_XC + (l & 7) * XC_ROW + 1024;
  int xto2 = xto + XT_Q;
  lptr rta = L + L_RT + (w & 1) * 8192 + (w >> 1) * 2048 + (8 * (g4 >> 1) + q4) * 128 + 32 * (g4 & 1) + 8 * p4;
  int rto = (retile ? (g4 & 1) * 32768 + ((g4 >> 1) * 32 + n16) * 16 : OOB) + WS_W1F + (4 * cg * 32 + 4 * (rbid & 7) + (w >> 1)) * 1024 + (w & 1) * 256;
  asm volatile("" : "+v"(xcw), "+v"(xcr), "+v"(xto), "+v"(xcs), "+v"(xct), "+v"(xto2), "+v"(rta), "+v"(rto));
  FENCE();
  if (!EW) rawdma();
  if (WDL == 2) rawwd();
  FENCE();
  TS(1);
  asm volatile("s_waitcnt vmcnt(%1)" : "+v"(xrow) : "n"(8 - (WDL & 1)));
  PRIO(3);
  TS(33);
  {
    float s = dot2acc(xrow.x, ONES, 0.f), q = dot2acc(xrow.x, xrow.x, 0.f);
    s = dot2acc(xrow.y, ONES, s); q = dot2acc(xrow.y, xrow.y, q);
    s = dot2acc(xrow.z, ONES, s); q = dot2acc(xrow.z, xrow.z, q);
    s = dot2acc(xrow.w, ONES, s); q = dot2acc(xrow.w, xrow.w, q);
    f32x2 xf[4] = {{bflo(xrow.x), bfhi(xrow.x)}, {bflo(xrow.y), bfhi(xrow.y)}, {bflo(xrow.z), bfhi(xrow.z)}, {bflo(xrow.w), bfhi(xrow.w)}};
    float v = fold32(s, q);
    v += dppf<0xB1>(v); v += dppf<0x4E>(v); v += dppf<0x141>(v); v += dppf<0x140>(v);
    v = x16_add(v) * (1.f / DM);
    const auto sw = __builtin_amdgcn_permlane32_swap(asu(v), asu(v), false, false);
    const float mean = asf(sw[0]);
    const float var = rbf(fmaxf(asf(sw[1]) - mean * mean, 0.f));
    unsigned pk = pkbf(mean, __builtin_amdgcn_sqrtf(rbf(var + 1e-5f)));
    asm("" : "+v"(pk));
    const float rsd = __builtin_amdgcn_rcpf(bfhi(pk));
    f32x2 rn = {rsd, -bflo(pk) * rsd};
    asm("" : "+v"(rn));
    unsigned us[4];
#pragma unroll
    for (int i = 0; i < 4; ++i) {
      const f32x2 u = __builtin_elementwise_fma(xf[i], f32x2{rn.x, rn.x}, f32x2{rn.y, rn.y});
      us[i] = pkh(u.x, u.y);
    }
    *(LAS v4u*)(xcw) = v4u{us[0], us[1], us[2], us[3]};
    *(LAS unsigned*)(xcs) = pk;
  }
  FENCE();
  TS(34);
  VMW(7 - (WDL & 1));
  TS(35);
  BAR();
  asm volatile("" : "+s"(ep));
  const unsigned tag = ep + 1u;
  TS(36);
  {
    const v2u ua = *(volatile LAS v2u*)(xcr), ub = *(volatile LAS v2u*)(xcr + 8);
    const unsigned sa = *(volatile LAS unsigned*)(xct), sb = *(volatile LAS unsigned*)(xct);
    st16(v4u{ua.x, ua.y, sa, tag}, rs, xto, 0);
    st16(v4u{ub.x, ub.y, sb, tag}, rs, xto2, 0);
  }
  FENCE();
  TS(2);
  PRIO(0);
  asm volatile("s_waitcnt vmcnt(%6)" : "+v"(pA), "+v"(pB), "+v"(pCr), "+v"(pCi), "+s"(pdt), "+s"(pds) : "n"(5 - (WDL & 1)));
  TS(44);
  v4u rf[2];
  auto retile_a = [&]() {
    FENCE();
    VMW(3 - (WDL & 1));
    TS(58);
    if (WDL == 1) rawwd();
    rf[0] = cat(trd(rta), trd(rta + 512)); rf[1] = cat(trd(rta + 64), trd(rta + 576));
    FENCE();
  };
  auto retile_b = [&]() {
    FENCE();
    st16(rf[0], rs, rto, 0); st16(rf[1], rs, rto, 65536);
    st16(sent4, rs, w < 2 ? OOB : hto, 0);
    if (MK == 2) HMARK();
    if (WDL == 3) { FENCE(); rawwd(); FENCE(); }
    float c[2];
#pragma unroll
    for (int i = 0; i < 2; ++i) c[i] = dot2acc(rf[i].w, ONES, dot2acc(rf[i].z, ONES, dot2acc(rf[i].y, ONES, dot2acc(rf[i].x, ONES, 0.f))));
    *(LAS float*)(L + L_HX + 2048 + 4 * tid) = fold32(c[0], c[1]);
    FENCE();
  };
  if (RP == 0) { retile_a(); retile_b(); }
  {
    const float LOG2E = 1.4426950408889634f, INV2PI = 0.15915494309189535f;
    const unsigned sel = (rg & 1) ? 0x03020c0cu : 0x01000c0cu;
#define HALF(v) asf(__builtin_amdgcn_perm(0u, v, sel))
    const float dt = rbf(__builtin_amdgcn_exp2f((cw ? bfhi(pdt) : bflo(pdt)) * LOG2E));
    const f32x2 z = f32x2{rbf(__builtin_amdgcn_exp2f(HALF(pA) * LOG2E)), HALF(pB)} * dt * f32x2{-LOG2E, INV2PI};
    lptr t = L + L_T + cw * T_CH + 256 * (rg & 1) + 4 * l;
    if (((rg >> 1) ^ cw) == 0) {
      f32x2 e[8];
      e[1] = cexp2(z, 1.f);
      const f32x2 e128 = cexp2(z, 128.f);
      put(t, 0, f32x2{1.f, 0.f}); put(t, 8, e128);
      const f32x2 e256 = cmul(e128, e128);
      e[2] = cmul(e[1], e[1]);
      const f32x2 e384 = cmul(e256, e128), e512 = cmul(e256, e256);
      e[3] = cmul(e[2], e[1]); e[4] = cmul(e[2], e[2]);
      const f32x2 e640 = cmul(e512, e128), e768 = cmul(e512, e256), e896 = cmul(e512, e384);
      e[5] = cmul(e[4], e[1]); e[6] = cmul(e[4], e[2]); e[7] = cmul(e[4], e[3]);
#pragma unroll
      for (int i = 1; i < 8; ++i) {
        put(t, i, e[i]); put(t, 8 + i, cmul(e[i], e128));
        if (RP == 1 && i == 2) retile_a();
        if (RP == 1 && i == 5) retile_b();
      }
      put(t, 16, e256); put(t, 17, cmul(e256, e[4])); put(t, 18, e384); put(t, 19, cmul(e384, e[4]));
      put(t, 20, cmul(e256, e[2])); put(t, 21, cmul(e256, e[6])); put(t, 22, cmul(e384, e[2])); put(t, 23, cmul(e384, e[6]));
      put(t, 24, e512); put(t, 25, cmul(e512, e[4])); put(t, 26, e640); put(t, 27, cmul(e640, e[4]));
      put(t, 28, e768); put(t, 29, cmul(e768, e[4])); put(t, 30, e896); put(t, 31, cmul(e896, e[4]));
    } else {
      unsigned m = umax(umax(pCr << 1, pCr << 17), umax(pCi << 1, pCi << 17));
      m = dppmax<0xB1>(m); m = dppmax<0x4E>(m); m = dppmax<0x141>(m); m = dppmax<0x140>(m);
      m = dppmax<0x142>(m); m = dppmax<0x143>(m);
      const f32x2 e8 = cexp2(z, 8.f), e64 = cexp2(z, 64.f);
      f32x2 a = cexp2(z, 1024.f);
      const f32x2 e128 = cmul(e64, e64), e16 = cmul(e8, e8), e256 = cmul(e128, e128);
      int ec = (int)((unsigned)__builtin_amdgcn_readlane((int)m, 63) >> 24);
      ec = ec < 5 ? 5 : ec;
      const unsigned db = (unsigned)__builtin_amdgcn_readfirstlane((int)asu(dt));
      int ei = ec + (int)(db >> 23) - 131;
      ei = ei < 0 ? 0 : (ei > 254 ? 254 : ei);
      *(LAS unsigned*)(L + L_INV + 4 * cw) = (unsigned)ei << 23;
      f32x2 v = f32x2{HALF(pCr), HALF(pCi)} * asf((db & 0x007fffffu) | ((unsigned)(259 - ec) << 23));
      f32x2 v1 = cmul(v, e8), a1 = cmul(a, e128);
      if (RP == 1) retile_a();
#pragma unroll
      for (int j = 0; j < 4; ++j) {
        putc(t, T_B + 2 * j, v); putc(t, T_B + 2 * j + 1, v1);
        putc(t, T_B + 8 + 2 * j, cmul(v, e64)); putc(t, T_B + 9 + 2 * j, cmul(v1, e64));
        put(t, 32 + 2 * j, a); put(t, 33 + 2 * j, a1);
        a = cmul(a, e256);
        if (j < 3) { v = cmul(v, e16); v1 = cmul(v1, e16); a1 = cmul(a1, e256); }
        if (RP == 1 && j == 0) retile_b();
      }
      put(t, 40, a);
    }
  }
  FENCE();
  TS(41);
  if (RP == 2) { retile_a(); retile_b(); }
  TS(3);
  lptr ta = L + L_T + cw * T_CH + (16 * role + n16) * T_ROW + 16 * g4, tb = L + L_T + cw * T_CH + (T_B + n16) * T_ROW + 16 * g4;
  if (role == 3) tb = L + L_HX + 2048 + (w & 1) * 256 + 4 * l;
  asm volatile("" : "+v"(ta), "+v"(tb));
  if (!PUB) VMWAIT(1);
  TS(4);
  BAR();
  TS(38);
  float csum;
  int f2o = OOB;
  if (role < 3) {
    v4u fr[8], fb[8];
#pragma unroll
    for (int ks = 0; ks < 8; ++ks) { fr[ks] = *(LAS v4u*)(ta + 64 * ks); fb[ks] = *(LAS v4u*)(tb + 64 * ks); }
    SBAR();
    f32x4 acc = {0.f, 0.f, 0.f, 0.f};
#pragma unroll
    for (int ks = 0; ks < 8; ++ks) acc = mfma16h(fr[ks], fb[ks], acc);
#pragma unroll
    for (int r = 0; r < 4; ++r) *(LAS _Float16*)(uintptr_t)kco[r] = (_Float16)acc[r];
    csum = 0.f;
  } else {
    csum = (*(LAS float*)(tb) + *(LAS float*)(tb + 512)) + (*(LAS float*)(tb + 1024) + *(LAS float*)(tb + 1536));
    if (retile) f2o = g4 * 2048 + n16 * 8;
  }
  FENCE();
#define F2ST() st8(v2u{asu(csum), tag}, rs, f2o, WS_F2 + cg * 8192 + (bid & 7) * 256 + (w & 1) * 128)
  if (!PUB) F2ST();
  FENCE();
  TS(39);
  const int gxs = WS_XT + cq * XT_Q + 4096 * w;
  v4u gx[4];
  asm volatile("buffer_load_dwordx4 %0, %4, %5, %6 offen sc1\n\tbuffer_load_dwordx4 %1, %4, %5, %6 offen offset:1024 sc1\n\t"
               "buffer_load_dwordx4 %2, %4, %5, %6 offen offset:2048 sc1\n\tbuffer_load_dwordx4 %3, %4, %5, %6 offen offset:3072 sc1"
               : "=&v"(gx[0]), "=&v"(gx[1]), "=&v"(gx[2]), "=&v"(gx[3]) : "v"(l16), "s"(rs), "s"(gxs) : "memory");
  FENCE();
  TS(40);
  if (PUB) VMW((MK == 2 ? 9 : 5) + (WDL == 3));
  TS(37);
  BAR();
  if (PUB) F2ST();
  FENCE();
  TS(5);
  {
    unsigned P[6], Q[5];
#pragma unroll
    for (int j = 0; j < 6; ++j) {
      const f16x2 e = {*(LAS _Float16*)(uintptr_t)eo[2 * j], j < 5 ? *(LAS _Float16*)(uintptr_t)eo[2 * j + 1] : (_Float16)0.f};
      P[j] = __builtin_bit_cast(unsigned, e);
    }
#pragma unroll
    for (int j = 0; j < 5; ++j) Q[j] = __builtin_amdgcn_alignbit(P[j + 1], P[j], 16);
    lptr kx = L + (cw ? L_KX1 : L_KX0) + 16 * (tid & 255);
    *(LAS v4u*)(kx) = v4u{P[0], P[1], P[2], P[3]};
    *(LAS v4u*)(kx + KX_CP) = v4u{Q[0], Q[1], Q[2], Q[3]};
    *(LAS v4u*)(kx + 2 * KX_CP) = v4u{P[1], P[2], P[3], P[4]};
    *(LAS v4u*)(kx + 3 * KX_CP) = v4u{Q[1], Q[2], Q[3], Q[4]};
  }
#define WDF() cat(trd(L + L_RD + w * 1024 + q4 * 128 + 32 * g4 + 8 * p4), trd(L + L_RD + w * 1024 + q4 * 128 + 32 * g4 + 8 * p4 + 512))
  v4u wdf;
  if (WDL != 3) wdf = WDF();
  FENCE();
  TS(6);
  unsigned pfw;
#define PFW1() do { FENCE(); asm volatile("buffer_load_dword %0, %1, %2, %3 offen sc1" : "=v"(pfw) : "v"(128 * (l & 31)), "s"(rs), "s"(WS_W1F + fgW * 32768 + (idx >> 2) * 4096) : "memory"); FENCE(); } while (0)
  if (PFW == 1) PFW1();
  asm volatile("s_waitcnt vmcnt(%4)" : "+v"(gx[0]), "+v"(gx[1]), "+v"(gx[2]), "+v"(gx[3]) : "n"(PUB + (PFW == 1)));
  TS(7);
  if (WDL == 3) { FENCE(); wdf = WDF(); FENCE(); }
#define GMISS() ((neq3(gx[0].w, gx[1].w, tag) | neq3(gx[2].w, gx[3].w, tag)) != 0u)
  if (anylane(GMISS())) {
    int it = 0;
    do {
      asm volatile("" ::: "memory");
      if (GMISS()) {
#pragma unroll
        for (int i = 0; i < 4; ++i) gx[i] = ld16(rs, l16 + 1024 * i, gxs);
      }
    } while (anylane(GMISS()) && ++it < SPIN_CAP);
  }
  FENCE();
  TS(8);
  if (PFW == 2) PFW1();
  unsigned up[4];
  {
#pragma unroll
    for (int i = 0; i < 4; ++i) up[i] = (cp & 1) ? gx[i].y : gx[i].x;
    lptr uw = L + L_U + 480 + 512 * w + 8 * l;
    *(LAS v2u*)(uw) = v2u{__builtin_amdgcn_perm(up[1], up[0], 0x05040100u), __builtin_amdgcn_perm(up[3], up[2], 0x05040100u)};
    *(LAS v2u*)(uw + U_CH) = v2u{__builtin_amdgcn_perm(up[1], up[0], 0x07060302u), __builtin_amdgcn_perm(up[3], up[2], 0x07060302u)};
  }
  FENCE();
  st16(wdf, rs, (retile ? g4 * 1024 + n16 * 16 : OOB) + WS_WDF + (w * 56 + (rbid >> 2)) * 4096 + (rbid & 3) * 256, 0);
  FENCE();
  if (MK == 1) HMARK();
  const unsigned cnt = lds0 + L_CNT;
  lptr kA = L + (cw ? L_KX1 : L_KX0) + (3 - (n16 & 3)) * KX_CP + 2 * (12 - 4 * (n16 >> 2) + 8 * g4) + 64 * (6 - 2 * rg);
  lptr uB = L + L_U + cw * U_CH + 32 * n16 + 16 * g4 + 384 - 128 * rg;
  lptr redw = L + L_RED + w * 1024 + 16 * (4 * n16 + (g4 ^ ((n16 >> 1) & 3)));
  lptr redr = L + L_RED + w * 8192 + 16 * ((l & ~3) + (p4 ^ ((l >> 3) & 3)));
  int wvo = (w > 0 ? l16 : OOB) + WS_W1F + fgW * 32768;
  asm volatile("" : "+v"(kA), "+v"(uB), "+v"(redw), "+v"(redr), "+v"(wvo), "+v"(hfo));
  if (PFW == 3) PFW1();
  TS(42);
  BAR();
  TS(9); TR(61);
#define AFRAG(a, rr) cat(*(volatile LAS v2u*)(kA + 64 * (57 - 8 * (a) - (rr))), *(volatile LAS v2u*)(kA + 64 * (57 - 8 * (a) - (rr)) + 8))
#define BFRAG(b, rr) (*(LAS v4u*)(uB + 512 * (b) + 64 * (1 - (rr))))
  const int rot = (idx >> 2) * 4096;
#define WALD(j) ld16(rs, wvo + ((j) & 3) * 1024, (((j) >> 2) * 4096) ^ rot)
  const int tso = WS_HT + rb * 32768;
  auto tile_dma = [&](int s, int c) {
    const int so = tso + s * (16 * 32768) + ((c * 4096) ^ rot);
    const unsigned d = lds0 + s * 32768 + c * 4096;
    dma<0>(rs, l16, so, d); dma<1024>(rs, l16, so, d); dma<2048>(rs, l16, so, d); dma<3072>(rs, l16, so, d);
  };
  v4u wa[32];
#define WAL(j) asm volatile("s_cmp_eq_u32 %3, %4\n\ts_cbranch_scc1 .Lwa%=\n\tbuffer_load_dwordx4 %0, %1, %2, %5 offen offset:%6 sc1\n.Lwa%=:" \
                            : "=v"(wa[j]) : "v"(wvo), "s"(rs), "s"(w), "n"(HSK ? 0 : 9), "s"((((j) >> 2) * 4096) ^ rot), "n"(((j) & 3) * 1024) : "scc", "memory")
  float hb0[4], hb1[4];
  auto conv_out = [&](int c) {
    const f32x2 invS = *(LAS f32x2*)(L + L_INV);
    f32x4 y[8];
    int it = 0;
    unsigned done;
    do {
      done = *(volatile LAS unsigned*)(L + L_CNT + 4 * c);
      FENCE();
#pragma unroll
      for (int v = 0; v < 8; ++v) y[v] = *(LAS f32x4*)(redr + 1024 * v);
      FENCE();
    } while (below(done, 8) && ++it < (1 << 22));
    float h0[4], h1[4];
#pragma unroll
    for (int i = 0; i < 4; ++i) {
      h0[i] = __builtin_fmaf(((y[0][i] + y[1][i]) + y[2][i]) + y[3][i], invS.x, hb0[i]);
      h1[i] = __builtin_fmaf(((y[4][i] + y[5][i]) + y[6][i]) + y[7][i], invS.y, hb1[i]);
    }
    st16(v4u{umin(pkbf_s(h0[0], h0[1]), SENT - 1), pkbf_s(h0[2], h0[3]), umin(pkbf_s(h1[0], h1[1]), SENT - 1), pkbf_s(h1[2], h1[3])}, rs, hto, 0);
  };
  {
    v4u A[2][8], B[2][8];
    f32x4 t[8];
#pragma unroll
    for (int i = 0; i < 8; ++i) t[i] = f32x4{0.f, 0.f, 0.f, 0.f};
#define MM(a, b, rr) t[(a) + (b)] = mfma16h(A[rr][a], B[rr][b], t[(a) + (b)])
#define MM2(a, b) do { MM(a, b, 0); MM(a, b, 1); } while (0)
#define LDA(a) do { A[0][a] = AFRAG(a, 0); A[1][a] = AFRAG(a, 1); } while (0)
#define LDB(b) do { B[0][b] = BFRAG(b, 0); B[1][b] = BFRAG(b, 1); } while (0)
    LDB(0); LDA(0); SBAR(); LDB(1); LDA(1); SBAR(); LDB(2); LDA(2); SBAR();
#pragma unroll
    for (int i = 0; i < 4; ++i) {
      const f16x2 u = __builtin_bit_cast(f16x2, up[i]);
      const float m = bflo(gx[i].z), sd = bfhi(gx[i].z);
      hb0[i] = __builtin_fmaf((float)u.x, sd + bflo(pds), m); hb1[i] = __builtin_fmaf((float)u.y, sd + bfhi(pds), m);
      asm volatile("" : "+v"(hb0[i]), "+v"(hb1[i]));
    }
    SBAR();
    v4u f2a, f2b;
#define F2LD() do { f2a = ld16(rs, l16, WS_F2 + fgW * 2048); f2b = ld16(rs, l16, WS_F2 + fgW * 2048 + 1024); } while (0)
    if (PFW < 4 && F2P == 0) F2LD();
    if (PFW >= 4) {
      asm volatile("buffer_load_dwordx4 %0, %2, %3, %4 offen sc1\n\tbuffer_load_dwordx4 %1, %2, %3, %4 offen offset:1024 sc1"
                   : "=&v"(f2a), "=&v"(f2b) : "v"(l16), "s"(rs), "s"(WS_F2 + fgW * 2048) : "memory");
      PFW1();
    }
    FENCE();
    MM2(0, 0); SBAR();
    LDB(3); SBAR();
    MM2(0, 1); SBAR();
    LDA(3); SBAR();
    MM2(1, 0); SBAR();
    st4(tag, rs, (bid == 0 && tid == 0) ? WS_EPOCH : OOB, 0);
    SBAR();
    MM2(1, 1); SBAR();
    *(LAS f32x4*)(redw) = t[0]; *(LAS f32x4*)(redw + 8192) = t[1];
    FENCE();
    arrive<C_S1>(cnt);
    if (F2P == 1) F2LD();
    FENCE();
    MM2(0, 2); MM2(2, 0); SBAR();
    TS(10);
    if (w < 2) conv_out(C_S1);
    FENCE();
    MM2(1, 2); MM2(2, 1); SBAR();
    LDB(4); SBAR();
    TS(43);
    if (PFW >= 4) {
      if (w < 2) VMWAIT(3); else VMWAIT(2);
      asm volatile("" : "+v"(f2a), "+v"(f2b));
    }
#define FMISS() anylane((neq3(f2a.y, f2a.w, tag) | neq3(f2b.y, f2b.w, tag)) != 0u)
    if (FMISS()) {
      int it = 0;
      do {
        asm volatile("" ::: "memory");
        F2LD();
      } while (FMISS() && ++it < SPIN_CAP);
    }
    FENCE();
    if (PFW && PFW < 4) asm volatile("" :: "v"(pfw));
    TS(11);
#define WA(n) do { sfor<wcnt(n) - wcnt((n) - 1)>([&](auto J) __attribute__((always_inline)) { WAL(wcnt((n) - 1) + J.v); SBAR(); }); \
                   if ((n) == TDS) { FENCE(); if (cw == 0) { tile_dma(0, 2 * w); tile_dma(0, 2 * w + 1); } FENCE(); } } while (0)
    WA(0);
    MM2(0, 3); SBAR();
    WA(1);
    MM2(3, 0); SBAR();
    WA(2); LDA(4); SBAR();
    MM2(1, 3); SBAR();
    *(LAS f32x4*)(redw + 16384) = t[2]; *(LAS f32x4*)(redw + 24576) = t[3];
    FENCE();
    arrive<C_S2>(cnt);
    WA(3);
    MM2(2, 2); SBAR();
    WA(4);
    *(LAS float*)(L + L_CSW + w * 128 + 8 * n16 + 4 * (l >> 5)) = x16_add(fold32(asf(f2a.x) + asf(f2b.x), asf(f2a.z) + asf(f2b.z)));
    SBAR();
    MM2(3, 1); SBAR();
    WA(5);
    if (w == 2 || w == 3) conv_out(C_S2);
    FENCE();
    MM2(2, 3); SBAR();
    WA(6);
    MM2(3, 2); SBAR();
    WA(7);
    MM2(3, 3); SBAR();
    WA(8); LDB(5); SBAR();
    MM2(3, 4); SBAR();
    WA(9);
    MM2(2, 4); SBAR();
    WA(10); LDA(5); SBAR();
    MM2(1, 4); SBAR();
    WA(11);
    MM2(0, 4); SBAR();
    WA(12);
    MM2(4, 3); SBAR();
    WA(13); LDB(6); SBAR();
    MM2(4, 2); SBAR();
    WA(14);
    MM2(4, 1); SBAR();
    WA(15); LDA(6); SBAR();
    MM2(4, 0); SBAR();
    WA(16);
    MM2(2, 5); SBAR();
    *(LAS f32x4*)(redw + 32768) = t[4]; WA(17); LDB(7); SBAR();
    MM2(1, 5); SBAR();
    WA(18);
    MM2(0, 5); SBAR();
    WA(19); LDA(7); SBAR();
    MM2(5, 0); SBAR();
    WA(20);
    MM2(5, 1); SBAR();
    *(LAS f32x4*)(redw + 40960) = t[5]; WA(21);
    MM2(5, 2); SBAR();
    WA(22);
    MM2(0, 6); SBAR();
    WA(23);
    MM2(6, 0); SBAR();
    WA(24);
    MM2(1, 6); SBAR();
    *(LAS f32x4*)(redw + 49152) = t[6]; WA(25);
    MM2(6, 1); SBAR();
    WA(26);
    MM2(0, 7); SBAR();
    WA(27);
    MM2(7, 0); SBAR();
    WA(28);
    *(LAS f32x4*)(redw + 57344) = t[7];
  }
  FENCE();
  arrive<C_S3>(cnt);
  TS(12);
  auto tile_peek = [&](int s, int c) {
    lptr q = L + s * 32768 + c * 4096 + l16;
    return v4u{*(volatile LAS unsigned*)(q), *(volatile LAS unsigned*)(q + 1024), *(volatile LAS unsigned*)(q + 2048), *(volatile LAS unsigned*)(q + 3072)};
  };
#define TMISS(m) anylane(umax(umax(m.x, m.y), umax(m.z, m.w)) == SENT)
  auto tile_check = [&](v4u m, int s, int c) {
    if (TMISS(m)) {
      int it = 0;
      do {
        tile_dma(s, c);
        VMWAIT(0);
        m = tile_peek(s, c);
      } while (TMISS(m) && ++it < SPIN_CAP);
    }
    FENCE();
  };
  if (TDS > 28 && cw == 0) { tile_dma(0, 2 * w); tile_dma(0, 2 * w + 1); }
  FENCE();
  sfor<32 - ND - WC>([&](auto J) __attribute__((always_inline)) { WAL(WC + J.v); SBAR(); });
  v2u cA;
  {
    const float c = l < 32 ? *(LAS float*)(L + L_CSW + w * 128 + 4 * (l & 31)) : 0.f;
    const float hi = rbf(c), mid = rbf(c - hi);
    cA = v2u{pkbf(hi, mid), pkbf((c - hi) - mid, 0.f)};
    PIN(cA);
  }
  FENCE();
  if (cw) {
    arrive<C_T0>(cnt);
    conv_out(C_S3);
    FENCE();
    TS(13);
  } else {
    TS(13);
    if (HSK && w == 0) VMWAIT(0);
    VMW(32 - ND - wcnt(TDS > 28 ? 28 : TDS));
    FENCE();
    const v4u m0 = tile_peek(0, 2 * w), m1 = tile_peek(0, 2 * w + 1);
    tile_check(m0, 0, 2 * w); tile_check(m1, 0, 2 * w + 1);
    arrive<C_T0>(cnt);
  }
  FENCE();
  if (PFW >= 4) asm volatile("s_waitcnt vmcnt(%1)" :: "v"(pfw), "n"(32 - ND));
  TS(14); TR(59);
  const int g2r = xcd * 4 + (idx >> 3), db = idx & 7, tbo = g2r >> 1, th = g2r & 1;
  const int wdo = WS_WDF + (db * 56 + 7 * w) * 4096;
  const int hvo = (g4 >> 1) * 1024 + ((g4 & 1) * 32 + th * 16 + n16) * 16;
  const int hso = WS_HF + (tbo * 112 + 14 * w) * 1024;
  const int rch = 64 * db + 16 * rg + (l >> 2);
  const int hro = WS_HT + (16 * cw + tbo) * 32768 + (rch >> 1) * 128 + (((4 * th) ^ ((rch >> 1) & 4)) + p4) * 16 + (rch & 1) * 8;
#define WDSO(j) (wdo + ((j) >> 2) * 4096 + ((((j) & 3) + rg) & 3) * 1024)
#define WDLD(j) ld16(rs, (KO & 2) ? OOB : l16, WDSO(j))
#define SLOT(j) ((j) < 12 ? L_KX1 - 2048 + w * 2048 + ((j) - 10) * 1024 : ((j) == 12 ? L_XC - 1024 + w * 1024 : ((j) - 13) / 4 * 32768 + w * 4096 + ((j) - 13) % 4 * 1024))
#define HLD(s, ks) ld16(rs, (KO & 4) ? OOB : hvo, hso + (s) * HF_S + (ks) * 2048)
#define FHLD() ld4(rs, 4 * (n16 < 14 ? n16 : 13), WS_FH + tbo * 512 + 56 * w)
#define FHST() st4(tag, rs, l == 0 ? 0 : OOB, WS_FH + rb * 512 + 4 * fgW)
  v4u wd[28], hb[3][7];
  v2u hres[2];
  unsigned fh;
  auto fh_check = [&]() {
    if (anylane(fh != tag)) {
      int it = 0;
      do {
        asm volatile("" ::: "memory");
        fh = FHLD();
      } while (anylane(fh != tag) && ++it < SPIN_CAP);
    }
    FENCE();
  };
  auto tile_wait = [&](unsigned v, int s) {
    lptr c = L + L_CNT + 4 * (C_T0 + s);
    if (below(v, 8)) {
      int it = 0;
      do __builtin_amdgcn_s_sleep(1); while (below(*(volatile LAS unsigned*)c, 8) && ++it < (1 << 22));
    }
    FENCE();
  };
#define TCNT(s) (*(volatile LAS unsigned*)(L + L_CNT + 4 * (C_T0 + (s))))
  if (w > 0) {
    lptr bt = L + (4 * (g4 >> 1) + (q4 >> 1)) * 128 + (4 * ((g4 ^ (g4 >> 1)) & 1) + p4) * 16 + (q4 & 1) * 8, bt2 = bt + 65536;
    lptr stp = L + L_ST2 + 16 * (l & 31);
    PIN(bt); PIN(bt2); PIN(stp);
#define BLD(g) cat(trd(((g) < 64 ? bt : bt2) + ((g) & 63) * 1024), trd(((g) < 64 ? bt : bt2) + ((g) & 63) * 1024 + 256))
    PRIO(1);
    if (PR4 == 2 && cw) PRIO(2);
    tile_wait(TCNT(0), 0);
    TS(15);
    f32x16 acc[2];
    v4u bf[4];
    f32x4 st;
    float gt[2], gu[2], ge[2], go[2];
    unsigned ho[4], tc, t5;
#define DMA5(t) dma<0>(rs, l16, tso + (t) * (16 * 32768) + rot + rg * 1024, lds0 + (t) * 32768 + rg * 1024)
#define PEEK5(t) (*(volatile LAS unsigned*)(L + (t) * 32768 + rg * 1024 + l16))
    v4u tm;
    u64 tq[8];
    auto st_check = [&](int s) {
      if (anylane(st.w == 0.f)) {
        int it = 0;
        do {
          __builtin_amdgcn_s_sleep(1);
          st = *(volatile LAS f32x4*)(stp + s * 512);
        } while (anylane(st.w == 0.f) && ++it < (1 << 22));
      }
      FENCE();
      acc[s & 1] = mfma32k8(cA, v2u{asu(st.x), asu(st.x)}, acc[s & 1]);
    };
    auto epi = [&](int s, int j, int stage) {
      const float g0 = acc[s & 1][2 * j], g1 = acc[s & 1][2 * j + 1];
      if (stage == 0) { gt[0] = __builtin_fmaf(g0 * g0, st.z, st.y); gt[1] = __builtin_fmaf(g1 * g1, st.z, st.y); PIN(gt[0]); PIN(gt[1]); }
      if (stage == 1) { gt[0] *= g0; gt[1] *= g1; gu[0] = g0 * acc[s & 1][8 + 2 * j]; PIN(gt[0]); PIN(gt[1]); PIN(gu[0]); }
      if (stage == 2) { ge[0] = __builtin_amdgcn_exp2f(gt[0]); gu[1] = g1 * acc[s & 1][9 + 2 * j]; PIN(ge[0]); PIN(gu[1]); }
      if (stage == 3) { ge[1] = __builtin_amdgcn_exp2f(gt[1]); ge[0] = __builtin_fmaf(ge[0], st.w, st.w); PIN(ge[0]); PIN(ge[1]); }
      if (stage == 4) { ge[0] = __builtin_amdgcn_rcpf(ge[0]); ge[1] = __builtin_fmaf(ge[1], st.w, st.w); PIN(ge[0]); PIN(ge[1]); }
      if (stage == 5) { ge[1] = __builtin_amdgcn_rcpf(ge[1]); go[0] = gu[0] * ge[0]; PIN(go[0]); PIN(ge[1]); }
      if (stage == 6) { go[1] = gu[1] * ge[1]; PIN(go[1]); }
      if (stage == 7) { ho[j] = pkbf(go[0], go[1]); PIN(ho[j]); }
    };
#define HST(s) st16(v4u{umin(ho[0], SENT - 1), ho[1], ho[2], ho[3]}, rs, hfo, (s) * HF_S)
#pragma unroll
    for (int g = 0; g < 3; ++g) bf[g] = BLD(g);
    SBAR();
    sfor<128>([&](auto G) __attribute__((always_inline)) {
      constexpr int g = G.v, s = g >> 5, ks = g & 31;
      if constexpr (ks == 0) {
#pragma unroll
        for (int r = 0; r < 16; ++r) acc[s & 1][r] = 0.f;
      }
      if constexpr (g + 3 < 128) bf[(g + 3) & 3] = BLD(g + 3);
      if constexpr (g < 32 - ND && g <= G_V[1]) asm volatile("s_waitcnt vmcnt(%1)" : "+v"(wa[ks]) : "n"(31 - ND - g + younger(-1, g)));
      if constexpr (g >= 32 - ND && g < 32) asm volatile("s_waitcnt vmcnt(%1)" : "+v"(wa[ks]) : "n"(younger(wa_at(g), g)));
      acc[s & 1] = mfma32b(wa[ks], bf[g & 3], acc[s & 1]);
      SBAR();
      if constexpr (s > 0) {
        constexpr int ke = ks - EO;
        if constexpr (ke == (EO ? -1 : 0)) st = *(volatile LAS f32x4*)(stp + (s - 1) * 512);
        if constexpr (ke == 1) st_check(s - 1);
        constexpr int n = ES[s];
        if constexpr (ke >= 3 && ke < hstep(s) - EO) {
          sfor<n>([&](auto R) __attribute__((always_inline)) {
            constexpr int e = (ke - 3) * n + R.v, j = e / 6, q = e % 6;
            if constexpr (q < 2 && j > 0 && j < 5) epi(s - 1, j - 1, 6 + q);
            if constexpr (j < 4) epi(s - 1, j, q);
          });
        }
        if constexpr (ks == hstep(s)) HST(s - 1);
      }
      constexpr int v = g < G_V[1] + 3 ? 1 : (g < G_V[2] + 3 ? 2 : 3), u = g <= G_W[1] ? 1 : (g <= G_W[2] ? 2 : 3);
      if constexpr (g == G_V[v]) { VMW(younger(G_T[v] + 4 * D_T[v], g)); tm = tile_peek(v, w); t5 = PEEK5(v); }
      if constexpr (g == G_V[v] + 2) {
        if (anylane(umax(umax(umax(tm.x, tm.y), umax(tm.z, tm.w)), t5) == SENT)) {
          int it = 0;
          do {
            tile_dma(v, w); DMA5(v);
            VMWAIT(0);
            tm = tile_peek(v, w); t5 = PEEK5(v);
          } while (anylane(umax(umax(umax(tm.x, tm.y), umax(tm.z, tm.w)), t5) == SENT) && ++it < SPIN_CAP);
        }
        FENCE();
        arrive<C_T0 + v>(cnt);
      }
      if constexpr (g == G_W[u] - 2) tc = TCNT(u);
      if constexpr (g == G_W[u]) tile_wait(tc, u);
      if constexpr (op_tile(g) >= 0) {
        constexpr int t = op_tile(g) >> 3;
        if constexpr ((op_tile(g) & 7) < 4) dma<(op_tile(g) & 3) * 1024>(rs, l16, tso + t * (16 * 32768) + ((w * 4096) ^ rot), lds0 + t * 32768 + w * 4096);
        else DMA5(t);
      }
      if constexpr (op_wa(g) >= 0) asm volatile("buffer_load_dwordx4 %0, %1, %2, %3 offen offset:%4 sc1"
                                                : "=v"(wa[op_wa(g)]) : "v"(wvo), "s"(rs), "s"(((op_wa(g) >> 2) * 4096) ^ rot), "n"((op_wa(g) & 3) * 1024) : "memory");
      if constexpr (op_mark(g) >= 0) st16(sent4, rs, (KO & 1) ? OOB : hfo, op_mark(g) * HF_S);
      if constexpr (g == G_FLAG) { VMW(younger(GMK + 3 * DMK, g)); FHST(); }
      if constexpr (op_wd(g) >= 0) wd[op_wd(g)] = WDLD(op_wd(g));
      if constexpr (op_stage(g) >= 0) dma<0>(rs, (KO & 2) ? OOB : l16, WDSO(op_stage(g)), lds0 + SLOT(op_stage(g)));
      if constexpr (g == G_FH) fh = FHLD();
      if constexpr (g == 103 && !MK) fh_check();
      if constexpr (op_h(g) >= 0) hb[op_h(g) / 7][op_h(g) % 7] = HLD(op_h(g) / 7, op_h(g) % 7);
      if constexpr (g == 105) VMW(younger(91, g));
      if constexpr (op_back(g) >= 0) wd[op_back(g)] = *(LAS v4u*)(L + SLOT(op_back(g)) + l16);
      if constexpr (g == G_S3) st = *(volatile LAS f32x4*)(stp + 3 * 512);
      if constexpr (g == G_S3 + 2) st_check(3);
      if constexpr (g == G_R0) hres[0] = ld8(rs, hro, 0);
      if constexpr (g == G_R1) hres[1] = ld8(rs, hro, 32 * 32768);
      if constexpr (g == 95) arrive<C_M2>(cnt);
      if constexpr (ks == 31) TS(16 + 2 * s);
      if constexpr (g == 63) TR(62);
      if constexpr (s == LS && (ks & 3) == 3) TQ(tq[ks >> 2]);
      if constexpr (s == LS && ks == 31) { TW(tq[0], 50); TW(tq[1], 51); TW(tq[2], 52); TW(tq[3], 53); TW(tq[4], 54); TW(tq[5], 55); TW(tq[6], 56); TW(tq[7], 57); }
      FENCE();
    });
    arrive<C_M3>(cnt);
    {
      float t[8], u[8];
#define EP(x) _Pragma("unroll") for (int j = 0; j < 8; ++j) { x; } SBAR()
      EP(t[j] = acc[1][j] * acc[1][j]; PIN(t[j]));
      EP(t[j] = __builtin_fmaf(t[j], st.z, st.y); PIN(t[j]));
      EP(t[j] *= acc[1][j]; PIN(t[j]));
      EP(t[j] = __builtin_amdgcn_exp2f(t[j]); u[j] = acc[1][j] * acc[1][8 + j]; PIN(t[j]); PIN(u[j]));
      EP(t[j] = __builtin_fmaf(t[j], st.w, st.w); PIN(t[j]));
      EP(t[j] = __builtin_amdgcn_rcpf(t[j]); PIN(t[j]));
      EP(t[j] *= u[j]; PIN(t[j]));
#pragma unroll
      for (int j = 0; j < 4; ++j) ho[j] = pkbf(t[2 * j], t[2 * j + 1]);
      HST(3);
    }
    FENCE();
    TS(21);
  } else {
    lptr b0 = L + g4 * 512 + (q4 >> 1) * 128 + (g4 & 1) * 64 + p4 * 16 + (q4 & 1) * 8, b1 = L + g4 * 512 + (q4 >> 1) * 128 + ((g4 & 1) ^ 1) * 64 + p4 * 16 + (q4 & 1) * 8;
    v4u ones = {ONES, ONES, ONES, ONES};
    int sel = (l & 1) ? 0xeeeeeeee : 0x44444444;
    PIN(ones); PIN(sel);
    v2u f[4][8];
#define GLD(o, G, i) f[(G) & 3][i] = trd(((i) < 4 ? b0 : b1) + (o) + (G) * 4096 + ((i) & 2) * 1024 + ((i) & 1) * 256)
    if (HP) PRIO(HP);
    arrive<C_T1>(cnt);
    tile_wait(TCNT(0), 0);
    TS(15);
#pragma unroll
    for (int i = 0; i < 16; ++i) GLD(0, i >> 3, i & 7);
    FENCE();
#pragma unroll 1
    for (int s = 0; s < 4; ++s) {
      const int ot = s * 32768, on = ((s + 1) & 3) * 32768;
      f32x4 gq[2] = {{0.f, 0.f, 0.f, 0.f}, {0.f, 0.f, 0.f, 0.f}}, gs[2] = {{0.f, 0.f, 0.f, 0.f}, {0.f, 0.f, 0.f, 0.f}};
      unsigned tc;
#pragma unroll
      for (int G = 0; G < 8; ++G) {
        const v2u* e = f[G & 3];
#define SQ(h, j) do { const v4u t_ = cat(e[4 * (h) + 2 * (j)], e[4 * (h) + 2 * (j) + 1]); gq[h] = mfma16b(t_, t_, gq[h]); SBAR(); } while (0)
#define SM(h) do { const v8u t_ = {e[4 * (h)].x, e[4 * (h)].y, e[4 * (h) + 1].x, e[4 * (h) + 1].y, e[4 * (h) + 2].x, e[4 * (h) + 2].y, e[4 * (h) + 3].x, e[4 * (h) + 3].y}; \
        if (!(KO & 8)) gs[h] = __builtin_amdgcn_smfmac_f32_16x16x64_bf16(__builtin_bit_cast(b16x8, ones), __builtin_bit_cast(b16x16, t_), gs[h], sel, 0, 0); SBAR(); } while (0)
#define NX(i) do { if (G < 6) GLD(ot, G + 2, i); else GLD(on, G - 6, i); SBAR(); } while (0)
#define BB(n) do { if (BUB && (6 * G + (n)) % (BUB ? BUB : 1) == 0) { asm volatile("s_nop 2"); SBAR(); } } while (0)
        SQ(0, 0); NX(0); BB(1);
        SQ(1, 0); NX(1); BB(2);
        SQ(0, 1); NX(4); NX(5); BB(3);
        SM(0); NX(2); BB(4);
        SQ(1, 1); NX(3); BB(5);
        SM(1); NX(6); NX(7); BB(6);
        if (G == 5) { FENCE(); arrive<C_T2>(cnt + 4 * s); tc = TCNT((s + 1) & 3); FENCE(); }
        if ((G & 1) == 0 && s == HWS) {
          FENCE();
#pragma unroll
          for (int j = 0; j < 7; ++j) wd[7 * (G >> 1) + j] = WDLD(7 * (G >> 1) + j);
          FENCE();
        }
        SBAR();
      }
      const float C1 = -2.f * 0.7978845608028654f * 1.4426950408889634f, C2c = C1 * 0.044715f;
      float q[2];
#pragma unroll
      for (int hh = 0; hh < 2; ++hh) q[hh] = (p4 & 2) ? ((p4 & 1) ? gq[hh][3] : gq[hh][2]) : ((p4 & 1) ? gq[hh][1] : gq[hh][0]);
      const auto sw = __builtin_amdgcn_permlane32_swap(asu(q[1]), asu(q[1]), false, false);
      const bool first = q4 == g4;
      const float mean = (first ? gs[0][0] + gs[0][1] : gs[1][0] + gs[1][1]) * (1.f / DM);
      const float var = rbf(fmaxf((first ? q[0] : asf(l < 32 ? sw[1] : sw[0])) * (1.f / DM) - mean * mean, 0.f));
      const float sd = rbf(__builtin_amdgcn_sqrtf(rbf(var + 1e-5f)));
      const float r = __builtin_amdgcn_rcpf(sd);
      if (((q4 ^ g4) & 1) == 0) *(LAS f32x4*)(L + L_ST2 + s * 512 + (first ? 0 : 256) + 16 * n16) = f32x4{asf(pkbf(-mean, -mean)), C1 * r, C2c * r * r * r, sd * sd};
      FENCE();
      if (s == 0) TS(16); else if (s == 1) TS(18); else if (s == 2) TS(20); else TS(22);
      if (below(tc, 8)) {
        tile_wait(tc, s + 1);
#pragma unroll
        for (int i = 0; i < 16; ++i) GLD(on, i >> 3, i & 7);
      }
      FENCE();
    }
    if (HP) PRIO(0);
    if (!MK) fh = FHLD();
    await<C_M2>(L, 8);
    if (!MK) fh_check();
#pragma unroll
    for (int j = 0; j < NH; ++j) hb[j / 7][j % 7] = HLD(j / 7, j % 7);
    hres[0] = ld8(rs, hro, 0);
    hres[1] = ld8(rs, hro, 32 * 32768);
  }
  FENCE();
  PRIO(0);
  {
    unsigned pw[4];
#pragma unroll
    for (int j = 0; j < 4; ++j) pw[j] = lds0 + w * 4096 + n16 * 256 + (((((j + rg) & 3) * 4 + g4) ^ n16) * 16);
    lptr hx = L + L_HX + w * 512 + 8 * l;
    PIN(hx); PIN(pw[0]); PIN(pw[1]); PIN(pw[2]); PIN(pw[3]);
    lptr pr3;
    unsigned oo3;
    f32x4 acc[4];
    v2u ht[2];
#define HMAX(h) umax(umax(umax(h[0].x, h[1].x), umax(h[2].x, h[3].x)), umax(umax(h[4].x, h[5].x), h[6].x))
    auto h_check = [&](unsigned m, int s) {
      v4u* h = hb[s % 3];
      if (anylane(m == SENT)) {
        int it = 0;
        do {
          asm volatile("" ::: "memory");
          if (m == SENT) {
#pragma unroll
            for (int ks = 0; ks < 7; ++ks) h[ks] = HLD(s, ks);
          }
          m = HMAX(h);
        } while (anylane(m == SENT) && ++it < SPIN_CAP);
      }
      FENCE();
    };
#define PWR(c) *(LAS f32x4*)(uintptr_t)(pw[3 - ((c) & 3)] + ((c) >> 2) * 32768) = acc[(c) & 3]
    unsigned hm = HMAX(hb[0]), mc;
    TS(23); TR(60);
    auto stream = [&](auto M) __attribute__((always_inline)) {
      constexpr int m = M.v, q = m % 14, c = m / 14 * 2 + (m & 1), ks = q / 2, s = c >> 2, dt = 3 - (c & 3);
      if constexpr (m % 28 == 0) { h_check(hm, s); TS(45 + s); }
      if constexpr (ks == 0 && dt > 0) asm volatile("v_mfma_f32_16x16x32_bf16 %0, %1, %2, 0" : "=v"(acc[c & 3]) : "v"(wd[dt]), "v"(hb[s % 3][0]));
      else asm volatile("v_mfma_f32_16x16x32_bf16 %0, %1, %2, %0" : "+v"(acc[c & 3]) : "v"(wd[4 * ks + dt]), "v"(hb[s % 3][ks]));
      SBAR();
      if constexpr (m % 28 == 10) {
        const unsigned lo = cw == (s & 1) ? 0x01000c0cu : 0x0c0c0c0cu, hi = cw == (s & 1) ? 0x03020c0cu : 0x0c0c0c0cu;
        const v2u t = ht[s >> 1];
        acc[3] = f32x4{asf(__builtin_amdgcn_perm(0u, t.x, lo)), asf(__builtin_amdgcn_perm(0u, t.x, hi)), asf(__builtin_amdgcn_perm(0u, t.y, lo)), asf(__builtin_amdgcn_perm(0u, t.y, hi))};
        PIN(acc[3]);
      }
      if constexpr (m == 64) mc = peek<C_M2>(L);
      if constexpr (m == 71) { if (below(mc, 8)) await<C_M2>(L, 8); FENCE(); }
      if constexpr (m == 80) mc = peek<C_M3>(L);
      if constexpr (m == 85) { const int tk = 2 * w + (l >> 5); pr3 = L + 3 * 32768 + tk * 256 + ((((l & 31) >> 1) ^ tk) * 16) + (l & 1) * 8; PIN(pr3); }
      if constexpr (m == 88) { oo3 = (1536 + 16 * g2r + 2 * w + (l >> 5)) * 1024 + 128 * db + 4 * (l & 31); PIN(oo3); }
      if constexpr ((q == 2 || q == 5) && m >= 14 && m / 14 != 6) PWR(c - 2);
      if constexpr (m == 34) { FENCE(); arrive<C_P0>(cnt); }
      if constexpr (m == 62) { FENCE(); arrive<C_P1>(cnt); }
      if constexpr (every(m, 1, D_H2, 21 - NH) >= 0) { constexpr int j = NH + every(m, 1, D_H2, 21 - NH); hb[j / 7][j % 7] = HLD(j / 7, j % 7); }
      if constexpr (m == 2) {
        if (anylane(umax(hres[0].x, hres[1].x) == SENT)) {
          int it = 0;
          do {
            asm volatile("" ::: "memory");
            hres[0] = ld8(rs, hro, 0);
            hres[1] = ld8(rs, hro, 32 * 32768);
          } while (anylane(umax(hres[0].x, hres[1].x) == SENT) && ++it < SPIN_CAP);
        }
        FENCE();
        *(LAS v2u*)(hx) = hres[0];
        FENCE();
        ht[0] = trd(hx);
      }
      if constexpr (m == 3) {
        *(LAS v2u*)(hx) = hres[1];
        FENCE();
        ht[1] = trd(hx);
      }
      if constexpr (every(m, G_H3, D_H3, 7) >= 0) hb[0][every(m, G_H3, D_H3, 7)] = HLD(3, every(m, G_H3, D_H3, 7));
      if constexpr (m == 26 || m == 54) { hm = HMAX(hb[(s + 1) % 3]); PIN(hm); }
      FENCE();
    };
    sfor<84>(stream);
    asm volatile("s_nop 7");
    FENCE();
    PWR(10); PWR(11);
    FENCE();
    arrive<C_P2>(cnt);
    TS(24);
    const int tk = 4 * rg + g4;
    lptr prd = L + tk * 256 + ((n16 ^ tk) * 16);
    GAS v2u* op = (GAS v2u*)((GAS unsigned char*)p.out + (unsigned)((16 * g2r + tk) * 1024 + 128 * db + 8 * n16));
    auto reduce = [&](int s) {
      f32x4 v[8];
#pragma unroll
      for (int j = 0; j < 8; ++j) v[j] = *(LAS f32x4*)(prd + s * 32768 + j * 4096);
      float a[4];
#pragma unroll
      for (int i = 0; i < 4; ++i) {
        a[i] = v[0][i];
#pragma unroll
        for (int j = 1; j < 8; ++j) a[i] += v[j][i];
      }
      op[s * (512 * 128)] = v2u{pkbf_s(a[0], a[1]), pkbf_s(a[2], a[3])};
      FENCE();
    };
    if (cw == 0) {
      await<C_P0>(L, 8);
      reduce(0);
      await<C_P1>(L, 8);
      reduce(1);
      hm = HMAX(hb[0]);
      PIN(hm);
    } else {
      if (below(mc, 8)) await<C_M3>(L, 8);
      hm = HMAX(hb[0]);
      PIN(hm);
    }
    FENCE();
    TS(25);
    sfor<28>([&](auto M) __attribute__((always_inline)) { stream(ic<84 + M.v>{}); });
    asm volatile("s_nop 7");
    FENCE();
    PWR(14); PWR(15);
    FENCE();
    arrive<C_P3>(cnt);
    TS(26);
    if (cw == 0) { await<C_P2>(L, 8); reduce(2); }
    TS(49);
    {
      f32x2 v[8];
      int it = 0;
      unsigned done;
      do {
        done = peek<C_P3>(L);
        FENCE();
#pragma unroll
        for (int j = 0; j < 8; ++j) v[j] = *(volatile LAS f32x2*)(pr3 + j * 4096);
        FENCE();
      } while (below(done, 8) && ++it < (1 << 22));
      const f32x2 a = ((v[0] + v[1]) + (v[2] + v[3])) + ((v[4] + v[5]) + (v[6] + v[7]));
      *(GAS unsigned*)((GAS unsigned char*)p.out + oo3) = pkbf(a.x, a.y);
    }
    TS(27);
  }
  asm volatile(".Lcode_end:");
}

extern "C" void kernel_launch(void* const* d_in, const int* in_sizes, int n_in, void* d_out, int out_size, void* d_ws,
                              size_t ws_size, hipStream_t stream) {
  Params p;
  p.x = (const u16*)d_in[0]; p.logA = (const u16*)d_in[1]; p.aim = (const u16*)d_in[2]; p.cre = (const u16*)d_in[3];
  p.cim = (const u16*)d_in[4]; p.logdt = (const u16*)d_in[5]; p.dskip = (const u16*)d_in[6];
  p.wg = (const u16*)d_in[8]; p.wu = (const u16*)d_in[9]; p.wd = (const u16*)d_in[10];
  p.out = (u16*)d_out; p.ws = (unsigned char*)d_ws;
  (void)hipFuncSetAttribute((const void*)fused_kernel, hipFuncAttributeMaxDynamicSharedMemorySize, L_BYTES);
  fused_kernel<<<dim3(256), dim3(512), L_BYTES, stream>>>(p);
}
```

```cpp
#define ND 10
#define GT1 6
#define GMK 16
#define GV1 24
#define CO1 1
#define MP 1
#define M0 4
#include <hip/hip_runtime.h>
#include <stdint.h>

typedef unsigned short u16;
typedef unsigned long long u64;
typedef unsigned v4u __attribute__((ext_vector_type(4)));
typedef unsigned v2u __attribute__((ext_vector_type(2)));
typedef float f32x2 __attribute__((ext_vector_type(2)));
typedef float f32x4 __attribute__((ext_vector_type(4)));
typedef float f32x16 __attribute__((ext_vector_type(16)));
typedef short s16x4 __attribute__((ext_vector_type(4)));
typedef _Float16 f16x8 __attribute__((ext_vector_type(8)));
typedef _Float16 f16x2 __attribute__((ext_vector_type(2)));
typedef __bf16 b16x8 __attribute__((ext_vector_type(8)));
typedef __bf16 b16x2 __attribute__((ext_vector_type(2)));
typedef __bf16 b16x16 __attribute__((ext_vector_type(16)));
typedef unsigned v8u __attribute__((ext_vector_type(8)));
#define LAS __attribute__((address_space(3)))
#define GAS __attribute__((address_space(1)))
typedef LAS unsigned char* lptr;

#define CAS __attribute__((address_space(4)))

#define STAMP 0
#define LS 2
#define EO 0
#define HWS 2
#define ESA 1
#define ESB 2
#define KO 0
#define HP 1
#define GH3 48
#define DH3 4
#define DH2 5
#define DT1 1
#define GV2 54
#define GV3 66
#define GFL 45
#define GFH 83
#define DMK 2
#define WN 1
#define WD_ 2
#define T04 1
#define PA 0u
#define TDS 99

constexpr int SEQ = 2048, DM = 512, NMODE = 128, FF = 1792;
constexpr int SC1 = 16;
constexpr int SPIN_CAP = 20000;
constexpr int OOB = 0x40000000;
constexpr unsigned SENT = 0xffffffffu;

constexpr int WS_EPOCH = 0;
constexpr int WS_FH = 4096;
constexpr int WS_F2 = 65536;
constexpr int WS_HT = 1 << 20;
constexpr int WS_W1F = 3 << 20;
constexpr int WS_WDF = 7 << 20;
constexpr int WS_HF = 10 << 20;
constexpr int HF_S = 16 * 112 * 1024;
constexpr int WS_XT = 17 << 20;
constexpr int XT_Q = 32768 + 256;
constexpr int WS_DBG = 22 << 20;
constexpr int WS_BYTES = 24 << 20;
static_assert(WS_XT + 128 * XT_Q <= WS_DBG, "ws map");

constexpr int L_RT = 0, L_RD = 16384, L_DUMP = 28672;
constexpr int KX_CP = 4160, KX_CH = 4 * KX_CP;
constexpr int L_KX0 = 32768, L_U = L_KX0 + KX_CH, U_CH = 4576, L_KC = L_U + 2 * U_CH;
constexpr int KC0 = 0x3f78, KC_CH = 2 * (0x4548 - KC0);
constexpr int L_T = 65536, T_ROW = 544, T_B = 41, T_CH = 57 * T_ROW;
constexpr int L_RED = 65536;
constexpr int L_KX1 = 131072;
constexpr int L_CNT = L_KX1 + KX_CH;
constexpr int L_ST2 = L_CNT + 128;
constexpr int L_CSW = L_ST2 + 2048;
constexpr int L_INV = L_CSW + 1024;
constexpr int L_HX = L_INV + 16;
constexpr int L_XC = L_HX + 4096, XC_ROW = 1056;
constexpr int L_BYTES = L_XC + 8 * XC_ROW;
static_assert(L_BYTES <= 163840, "lds");
static_assert(L_KC + 2 * KC_CH <= 65536 && 2 * T_CH <= 65536, "lds");
static_assert(L_U % 16 == 0 && L_KC % 16 == 0 && L_CNT % 16 == 0 && T_CH % 16 == 0, "align");
enum { C_S1 = 0, C_S2, C_S3, C_T0, C_T1, C_T2, C_T3, C_M2, C_M3, C_P0, C_P1, C_P2, C_P3 };

struct Params {
  const u16 *x, *logA, *aim, *cre, *cim, *logdt, *dskip, *wg, *wu, *wd;
  u16* out;
  unsigned char* ws;
};

__device__ __forceinline__ float asf(unsigned v) { return __builtin_bit_cast(float, v); }
__device__ __forceinline__ unsigned asu(float v) { return __builtin_bit_cast(unsigned, v); }
__device__ __forceinline__ float bflo(unsigned v) { return asf(v << 16); }
__device__ __forceinline__ float bfhi(unsigned v) { return asf(v & 0xffff0000u); }
__device__ __forceinline__ unsigned pkbf(float lo, float hi) {
  b16x2 r = {(__bf16)lo, (__bf16)hi};
  return __builtin_bit_cast(unsigned, r);
}
__device__ __forceinline__ unsigned pkbf_s(float lo, float hi) {
  asm("" : "+v"(lo));
  asm("" : "+v"(hi));
  return pkbf(lo, hi);
}
__device__ __forceinline__ float rbf(float f) { return bflo(pkbf(f, f)); }
__device__ __forceinline__ unsigned pkh(float lo, float hi) {
  f16x2 r = {(_Float16)lo, (_Float16)hi};
  return __builtin_bit_cast(unsigned, r);
}
template <int C> __device__ __forceinline__ unsigned dppu(unsigned v) {
  return (unsigned)__builtin_amdgcn_update_dpp(0, (int)v, C, 0xf, 0xf, true);
}
template <int C> __device__ __forceinline__ float dppf(float v) { return asf(dppu<C>(asu(v))); }
__device__ __forceinline__ float x16_add(float v) {
  auto s = __builtin_amdgcn_permlane16_swap(asu(v), asu(v), false, false);
  return asf(s[0]) + asf(s[1]);
}
__device__ __forceinline__ float fold32(float a, float b) {
  auto s = __builtin_amdgcn_permlane32_swap(asu(a), asu(b), false, false);
  return asf(s[0]) + asf(s[1]);
}
__device__ __forceinline__ float dot2acc(unsigned a, unsigned b, float c) {
  return __builtin_amdgcn_fdot2_f32_bf16(__builtin_bit_cast(b16x2, a), __builtin_bit_cast(b16x2, b), c, false);
}
__device__ __forceinline__ unsigned umax(unsigned a, unsigned b) { return a > b ? a : b; }
__device__ __forceinline__ unsigned umin(unsigned a, unsigned b) { return a < b ? a : b; }
constexpr unsigned ONES = 0x3f803f80u;
#define BAR() asm volatile("s_waitcnt lgkmcnt(0)\n\ts_barrier" ::: "memory")
#define VMWAIT(N) asm volatile("s_waitcnt vmcnt(" #N ")" ::: "memory")
#define SBAR() __builtin_amdgcn_sched_barrier(0)
#define FENCE() do { asm volatile("" ::: "memory"); __builtin_amdgcn_sched_barrier(0); } while (0)
#define PRIO(n) __builtin_amdgcn_s_setprio(n)

__device__ __forceinline__ void glds1(const void* g, unsigned dst) {
  unsigned sv;
  asm volatile("s_mov_b32 %0, m0\n\ts_mov_b32 m0, %2\n\ts_nop 0\n\tglobal_load_lds_dwordx4 %1, off\n\ts_mov_b32 m0, %0"
               : "=&s"(sv) : "v"(g), "s"(dst) : "memory");
}
__device__ __forceinline__ v2u trd(lptr p) {
  return __builtin_bit_cast(v2u, __builtin_amdgcn_ds_read_tr16_b64_v4i16((LAS s16x4*)p));
}
__device__ __forceinline__ v4u cat(v2u a, v2u b) { return v4u{a.x, a.y, b.x, b.y}; }
__device__ __forceinline__ f32x4 mfma16h(v4u a, v4u b, f32x4 c) {
  return __builtin_amdgcn_mfma_f32_16x16x32_f16(__builtin_bit_cast(f16x8, a), __builtin_bit_cast(f16x8, b), c, 0, 0, 0);
}
__device__ __forceinline__ f32x4 mfma16b(v4u a, v4u b, f32x4 c) {
  return __builtin_amdgcn_mfma_f32_16x16x32_bf16(__builtin_bit_cast(b16x8, a), __builtin_bit_cast(b16x8, b), c, 0, 0, 0);
}
__device__ __forceinline__ f32x16 mfma32b(v4u a, v4u b, f32x16 c) {
  return __builtin_amdgcn_mfma_f32_32x32x16_bf16(__builtin_bit_cast(b16x8, a), __builtin_bit_cast(b16x8, b), c, 0, 0, 0);
}
typedef __amdgpu_buffer_rsrc_t rsrc_t;
__device__ __forceinline__ v4u ld16(rsrc_t rs, int voff, int soff) { return __builtin_amdgcn_raw_buffer_load_b128(rs, voff, soff, SC1); }
__device__ __forceinline__ unsigned ld4(rsrc_t rs, int voff, int soff) { return __builtin_amdgcn_raw_buffer_load_b32(rs, voff, soff, SC1); }
__device__ __forceinline__ v2u ld8(rsrc_t rs, int voff, int soff) { return __builtin_amdgcn_raw_buffer_load_b64(rs, voff, soff, SC1); }
__device__ __forceinline__ void st16(v4u v, rsrc_t rs, int voff, int soff) { __builtin_amdgcn_raw_buffer_store_b128(v, rs, voff + soff, 0, SC1); }
__device__ __forceinline__ void st8(v2u v, rsrc_t rs, int voff, int soff) { __builtin_amdgcn_raw_buffer_store_b64(v, rs, voff, soff, SC1); }
__device__ __forceinline__ void st4(unsigned v, rsrc_t rs, int voff, int soff) { __builtin_amdgcn_raw_buffer_store_b32(v, rs, voff, soff, SC1); }
template <int OFF> __device__ __forceinline__ void dma(rsrc_t rs, int voff, int soff, unsigned dst) {
  asm volatile("s_mov_b32 m0, %3\n\ts_nop 0\n\tbuffer_load_dwordx4 %0, %1, %2 offen offset:%4 sc1 lds"
               :: "v"(voff), "s"(rs), "s"(soff), "s"(dst), "n"(OFF) : "memory");
}
__device__ __forceinline__ f32x16 mfma32k8(v2u a, v2u b, f32x16 c) {
  return __builtin_amdgcn_mfma_f32_32x32x8bf16_1k(__builtin_bit_cast(s16x4, a), __builtin_bit_cast(s16x4, b), c, 0, 0, 0);
}
template <int V> struct ic { static constexpr int v = V; };
template <class T, T... I> struct iseq {
  template <class F> static __device__ __forceinline__ void run(F&& f) { (f(ic<I>{}), ...); }
};
template <int N, class F> __device__ __forceinline__ void sfor(F&& f) { __make_integer_seq<iseq, int, N>::run(f); }
constexpr int wcnt(int n) { return n < 0 ? 0 : (n * WN / WD_ + 1 < 32 ? n * WN / WD_ + 1 : 32); }
constexpr int WC = wcnt(28);
constexpr int every(int g, int a, int d, int n) { return g >= a && (g - a) % d == 0 && (g - a) / d < n ? (g - a) / d : -1; }
constexpr int G_T[4] = {0, GT1, 29, 35}, D_T[4] = {1, DT1, 1, 2};
constexpr int G_V[4] = {0, GV1, GV2, GV3}, G_W[4] = {0, 28, 60, 84};
constexpr int op_tile(int g) { for (int s = 1; s < 4; ++s) if (every(g, G_T[s], D_T[s], 5) >= 0) return 8 * s + every(g, G_T[s], D_T[s], 5); return -1; }
constexpr int op_wa(int g) {
  if (op_tile(g) >= 0) return -1;
  int n = 0;
  for (int i = 0; i < g; ++i) n += op_tile(i) < 0;
  return n < ND ? 32 - ND + n : -1;
}
constexpr int wa_at(int j) { for (int g = 0; g < 32; ++g) if (op_wa(g) == j) return g; return -1; }
constexpr int op_mark(int g) { return every(g, GMK, DMK, 4); }
constexpr int G_FLAG = GFL, G_FH = GFH;
constexpr int op_wd(int g) {
  if (every(g, 55, 2, 3) >= 0) return every(g, 55, 2, 3);
  if (every(g, 69, 2, 7) >= 0) return 3 + every(g, 69, 2, 7);
  if (every(g, 97, 1, 7) >= 0) return 21 + every(g, 97, 1, 7);
  return -1;
}
constexpr int op_stage(int g) {
  if (every(g, 49, 2, 3) >= 0) return 10 + every(g, 49, 2, 3);
  if (every(g, 61, 2, 4) >= 0) return 13 + every(g, 61, 2, 4);
  if (every(g, 85, 2, 4) >= 0) return 17 + every(g, 85, 2, 4);
  return -1;
}
constexpr int op_h(int g) {
  if (every(g, 104, 2, 4) >= 0) return every(g, 104, 2, 4);
  if (every(g, 111, 2, 9) >= 0) return 4 + every(g, 111, 2, 9);
  return -1;
}
constexpr int op_back(int g) {
  if (every(g, 105, 2, 3) >= 0) return 10 + every(g, 105, 2, 3);
  if (every(g, 112, 2, 8) >= 0) return 13 + every(g, 112, 2, 8);
  return -1;
}
constexpr int NH = 13, G_H3 = GH3, D_H3 = DH3, D_H2 = DH2;
constexpr int G_R0 = 124, G_R1 = 126, G_S3 = 121;
constexpr int ES[4] = {1, ESA, ESA, ESB};
constexpr int hstep(int s) { return (EO + 3 + (26 + ES[s] - 1) / ES[s] + 1) & ~1; }
constexpr int nv(int g) {
  return (op_tile(g) >= 0) + (op_wa(g) >= 0) + (op_mark(g) >= 0) + (g == G_FLAG) + (g == G_FH) + (g == G_R0) + (g == G_R1) + (g > 32 && (g & 31) == hstep(g >> 5)) + (op_wd(g) >= 0) + (op_stage(g) >= 0) + (op_h(g) >= 0);
}
constexpr bool single() { for (int g = 0; g < 128; ++g) if (nv(g) > 1) return false; return true; }
static_assert(single(), "schedule");
constexpr int younger(int a, int b) { int n = 0; for (int g = a + 1; g < b; ++g) n += nv(g); return n; }
#define VMW(N) asm volatile("s_waitcnt vmcnt(%0)" :: "n"(N) : "memory")
#define VMWC(N) __builtin_amdgcn_s_waitcnt(0x0f70 | (N))
#define XP 1
#define TAIL (XP ? 3072 : 0)
#define RARE(c) (XP ? __builtin_expect(!!(c), 0) : (c))
#define PIN(x) asm volatile("" : "+v"(x))
template <class T> __device__ __forceinline__ T gld(const void* base, unsigned off) { return *(const GAS T*)((const GAS unsigned char*)base + off); }

template <int C, unsigned N = 1> __device__ __forceinline__ void arrive(unsigned cnt) {
  asm volatile("s_mov_b64 exec, 1\n\tds_add_u32 %0, %1 offset:%2\n\ts_mov_b64 exec, -1"
               :: "v"(cnt), "v"(N), "n"(4 * C) : "memory");
  __builtin_amdgcn_sched_barrier(0);
}
template <int C> __device__ __forceinline__ unsigned peek(lptr L) { return *(volatile LAS unsigned*)(L + L_CNT + 4 * C); }
__device__ __forceinline__ bool below(unsigned v, unsigned n) { return (unsigned)__builtin_amdgcn_readfirstlane(v) < n; }
template <int C> __device__ __forceinline__ void await(lptr L, unsigned n) {
  if (below(peek<C>(L), n)) {
    int it = 0;
    do __builtin_amdgcn_s_sleep(1); while (below(peek<C>(L), n) && ++it < (1 << 22));
  }
  asm volatile("" ::: "memory");
}
__device__ __forceinline__ unsigned neq3(unsigned a, unsigned b, unsigned t) { return __builtin_amdgcn_bitop3_b32(a, b, t, 0x7e); }
__device__ __forceinline__ bool anylane(bool c) { return __builtin_amdgcn_ballot_w64(c) != 0; }

__device__ __forceinline__ void tabaddr(unsigned c, unsigned base, unsigned& lo, unsigned& hi) {
  asm volatile("v_mad_u32_u16 %0, %1, 2, %2" : "=v"(lo) : "v"(c), "s"(base));
  asm volatile("v_mad_u32_u16 %0, %1, 2, %2 op_sel:[1,0,0,0]" : "=v"(hi) : "v"(c), "s"(base));
}
__device__ __forceinline__ f32x2 cmul(f32x2 a, f32x2 b) {
  return __builtin_elementwise_fma(f32x2{a.y, a.y}, f32x2{-b.y, b.x}, f32x2{a.x, a.x} * b);
}
__device__ __forceinline__ f32x2 cexp2(f32x2 z, float p) {
  const f32x2 v = z * p;
  const float f = __builtin_amdgcn_fractf(v.y);
  return f32x2{__builtin_amdgcn_cosf(f), __builtin_amdgcn_sinf(f)} * __builtin_amdgcn_exp2f(v.x);
}
__device__ __forceinline__ void put(lptr t, int row, f32x2 v) { *(volatile LAS unsigned*)(t + row * T_ROW) = pkh(v.x, v.y); }
__device__ __forceinline__ void putc(lptr t, int row, f32x2 v) { *(volatile LAS unsigned*)(t + row * T_ROW) = pkh(v.x, -v.y); }
template <int C> __device__ __forceinline__ unsigned dppmax(unsigned v) {
  return umax(v, (unsigned)__builtin_amdgcn_update_dpp(0, (int)v, C, 0xf, 0xf, true));
}

#define TCLK "s_memtime"
#define TS(i) do {} while (0)
#define TR(i) do {} while (0)
#define TQ(v) do {} while (0)
#define TW(v, i) do {} while (0)

__global__ void __launch_bounds__(512, 2) fused_kernel(Params p) {
  extern __shared__ __attribute__((aligned(16))) unsigned char smem[];
  lptr L = (lptr)smem;
  const unsigned lds0 = (unsigned)(uintptr_t)L;
  const int tid = threadIdx.x, l = tid & 63, w = __builtin_amdgcn_readfirstlane(tid >> 6);
  const int bid = blockIdx.x, xcd = bid & 7, idx = bid >> 3;
  const int cq = (idx >> 1) * 8 + xcd, cp = 2 * cq + (idx & 1), cw = w >> 2, rg = w & 3, ch = 2 * cp + cw;
  const int g4 = l >> 4, q4 = (l >> 2) & 3, p4 = l & 3, n16 = l & 15;
  const int l16 = 16 * l;
  TS(0);
  if (w < 2) {
    u64 pc; unsigned len;
    asm volatile("s_getpc_b64 %0\n.Lcode_here:\n\ts_mov_b32 %1, .Lcode_end-.Lcode_here+%2" : "=s"(pc), "=s"(len) : "n"(TAIL));
    const unsigned o = (unsigned)((idx + 32 * w) * 1024 + l16);
    glds1((const void*)((pc & ~15ull) + (o < len ? o : 0u)), lds0 + L_DUMP);
  }
  FENCE();
  asm volatile("" :: "s"(p.x), "s"(p.ws));
  rsrc_t rs = __builtin_amdgcn_make_buffer_rsrc((void*)p.ws, (short)0, WS_BYTES, 0x00020000);

  TS(32);
  v4u xrow;
  asm volatile("global_load_dwordx4 %0, %1, %2" : "=v"(xrow) : "v"((32 * (bid >> 2) + (bid & 3) + 4 * w) * 1024 + l16), "s"(p.x));
  unsigned ep = *(const CAS unsigned*)(p.ws + WS_EPOCH);
  FENCE();
  const v4u sent4 = {SENT, SENT, SENT, SENT};
  const int hto = (l >> 3) * 32768 + ((l & 7) ^ (cp & 4)) * 16 + WS_HT + w * 8 * 32768 + cp * 128;
  st16(sent4, rs, w < M0 ? hto : OOB, 0);
  FENCE();
  unsigned pA, pB, pCr, pCi;
  asm volatile("global_load_dword %0, %4, %5\n\tglobal_load_dword %1, %4, %6\n\tglobal_load_dword %2, %4, %7\n\tglobal_load_dword %3, %4, %8"
               : "=&v"(pA), "=&v"(pB), "=&v"(pCr), "=&v"(pCi) : "v"(ch * 256 + 4 * l), "s"(p.logA), "s"(p.aim), "s"(p.cre), "s"(p.cim));
  unsigned pdt = *(const CAS unsigned*)(p.logdt + 2 * cp);
  unsigned pds = *(const CAS unsigned*)(p.dskip + 2 * cp);
  FENCE();
  const int rb = (xcd & 1) * 8 + (idx >> 2), cb = (xcd >> 1) * 4 + (idx & 3);
  const int fgW = 7 * cb + (w > 0 ? w - 1 : 0);
  int hfo = (w > 0 ? l16 : OOB) + WS_HF + (rb * 112 + fgW) * 1024;
  const int role = cw ? 3 - rg : rg;
  const bool retile = bid < 224;
  const int rbid = retile ? bid : 0, cg = rbid >> 3;
  int wdr = (16 * (rbid >> 1) + 8 * (l >> 5) + 4 * (rbid & 1) + ((l >> 3) & 3)) * (DM * 2) + 128 * w + 16 * (l & 7);
  auto rawwd = [&]() {
    unsigned sv;
    asm volatile("s_mov_b32 %0, m0\n\ts_mov_b32 m0, %3\n\ts_nop 0\n\tglobal_load_lds_dwordx4 %1, %2\n\ts_mov_b32 m0, %0"
                 : "=&s"(sv) : "v"(wdr), "s"(p.wd), "s"(lds0 + L_RD + w * 1024) : "memory");
  };
  auto rawdma = [&]() {
    const int w1o = (64 * (rbid & 7) + 16 * (w >> 1) + (l >> 3)) * (FF * 2) + 128 * cg + 16 * (l & 7);
    unsigned sv;
    asm volatile("s_mov_b32 %0, m0\n\ts_mov_b32 m0, %4\n\ts_nop 0\n\tglobal_load_lds_dwordx4 %1, %3\n\ts_add_u32 m0, m0, 1024\n\ts_nop 0\n\t"
                 "global_load_lds_dwordx4 %2, %3\n\ts_mov_b32 m0, %0"
                 : "=&s"(sv) : "v"(w1o), "v"(w1o + 8 * FF * 2), "s"((w & 1) ? p.wu : p.wg),
                    "s"(lds0 + L_RT + (w & 1) * 8192 + (w >> 1) * 2048) : "memory", "scc");
  };
  {
    const v4u z = {0u, 0u, 0u, 0u};
    *(LAS v4u*)(L + L_CNT + 16 * (tid & 255)) = z;
    const int za = (cw ? L_KX1 : L_KX0) + 4096 + rg * KX_CP + p4 * 16;
    const int zb = ((rg & 1) ? L_U - 512 + cw * U_CH : L_KC + cw * KC_CH) + (l16 & ((rg & 1) ? 0x3f0 : 0));
    *(LAS v4u*)(L + (l < 32 ? za : zb)) = z;
  }
  unsigned eo[12], kco[4];
  {
    const unsigned kcb = lds0 + L_KC + cw * KC_CH - 2 * KC0;
    float lf = (float)(2047 - 8 * (tid & 255));
    asm("" : "+v"(lf));
    const float Z0 = asf(0x3f7f0000u), ZN = asf(0x3f7e0000u);
#pragma unroll
    for (int j = 0; j < 12; j += 2) {
      float a = lf - (float)j, b = lf - (float)(j + 1);
      if (j == 6) b = fmaxf(b, Z0);
      if (j >= 8) { a = fmaxf(a, ZN); b = j < 10 ? fmaxf(b, ZN) : a; }
      tabaddr(pkbf_s(a, b), kcb, eo[j], eo[j + 1]);
    }
    const u64 pb = role == 0 ? 0x0084008000040000ull : (role == 1 ? 0x0300020001020100ull : 0x0a01080106010401ull);
    const float d1 = role == 0 ? 1.f : (role == 1 ? 4.f : 128.f), d2 = role == 0 ? 2.f : (role == 1 ? 128.f : 256.f);
    const float p0 = (float)(((unsigned)(pb >> (l & 48)) & 0xffffu) + 8 * n16), p2 = p0 + d2;
    tabaddr(pkbf_s(fmaxf(p0, Z0), p0 + d1), kcb, kco[0], kco[1]);
    tabaddr(pkbf_s(p2, p2 + d1), kcb, kco[2], kco[3]);
  }
  lptr xcw = L + L_XC + w * XC_ROW + l16, xcr = L + L_XC + (l & 7) * XC_ROW + 128 * w + 16 * (l >> 3);
  int xto = (l >> 3) * (2 * XT_Q) + (l & 7) * 16 + WS_XT + 16 * w * XT_Q + ((4 * (bid >> 5) + (bid & 3)) * 8 + ((bid >> 2) & 7)) * 128;
  lptr xcs = L + L_XC + w * XC_ROW + 1024, xct = L + L_XC + (l & 7) * XC_ROW + 1024;
  int xto2 = xto + XT_Q;
  lptr rta = L + L_RT + (w & 1) * 8192 + (w >> 1) * 2048 + (8 * (g4 >> 1) + q4) * 128 + 32 * (g4 & 1) + 8 * p4;
  int rto = (retile ? (g4 & 1) * 32768 + ((g4 >> 1) * 32 + n16) * 16 : OOB) + WS_W1F + (4 * cg * 32 + 4 * (rbid & 7) + (w >> 1)) * 1024 + (w & 1) * 256;
  asm volatile("" : "+v"(xcw), "+v"(xcr), "+v"(xto), "+v"(xcs), "+v"(xct), "+v"(xto2), "+v"(rta), "+v"(rto));
  FENCE();
  rawdma();
  FENCE();
  TS(1);
  asm volatile("s_waitcnt vmcnt(%1)" : "+v"(xrow) : "n"(7));
  PRIO(3);
  TS(33);
  {
    float s = dot2acc(xrow.x, ONES, 0.f), q = dot2acc(xrow.x, xrow.x, 0.f);
    s = dot2acc(xrow.y, ONES, s); q = dot2acc(xrow.y, xrow.y, q);
    s = dot2acc(xrow.z, ONES, s); q = dot2acc(xrow.z, xrow.z, q);
    s = dot2acc(xrow.w, ONES, s); q = dot2acc(xrow.w, xrow.w, q);
    f32x2 xf[4] = {{bflo(xrow.x), bfhi(xrow.x)}, {bflo(xrow.y), bfhi(xrow.y)}, {bflo(xrow.z), bfhi(xrow.z)}, {bflo(xrow.w), bfhi(xrow.w)}};
    float v = fold32(s, q);
    v += dppf<0xB1>(v); v += dppf<0x4E>(v); v += dppf<0x141>(v); v += dppf<0x140>(v);
    v = x16_add(v) * (1.f / DM);
    const auto sw = __builtin_amdgcn_permlane32_swap(asu(v), asu(v), false, false);
    const float mean = asf(sw[0]);
    const float var = rbf(fmaxf(asf(sw[1]) - mean * mean, 0.f));
    unsigned pk = pkbf(mean, __builtin_amdgcn_sqrtf(rbf(var + 1e-5f)));
    asm("" : "+v"(pk));
    const float rsd = __builtin_amdgcn_rcpf(bfhi(pk));
    f32x2 rn = {rsd, -bflo(pk) * rsd};
    asm("" : "+v"(rn));
    unsigned us[4];
#pragma unroll
    for (int i = 0; i < 4; ++i) {
      const f32x2 u = __builtin_elementwise_fma(xf[i], f32x2{rn.x, rn.x}, f32x2{rn.y, rn.y});
      us[i] = pkh(u.x, u.y);
    }
    *(LAS v4u*)(xcw) = v4u{us[0], us[1], us[2], us[3]};
    *(LAS unsigned*)(xcs) = pk;
  }
  FENCE();
  TS(34);
  VMW(6);
  TS(35);
  BAR();
  asm volatile("" : "+s"(ep));
  const unsigned tag = ep + 1u;
  TS(36);
  {
    const v2u ua = *(volatile LAS v2u*)(xcr), ub = *(volatile LAS v2u*)(xcr + 8);
    const unsigned sa = *(volatile LAS unsigned*)(xct), sb = *(volatile LAS unsigned*)(xct);
    st16(v4u{ua.x, ua.y, sa, tag}, rs, xto, 0);
    st16(v4u{ub.x, ub.y, sb, tag}, rs, xto2, 0);
  }
  FENCE();
  TS(2);
  PRIO(0);
  asm volatile("s_waitcnt vmcnt(%6)" : "+v"(pA), "+v"(pB), "+v"(pCr), "+v"(pCi), "+s"(pdt), "+s"(pds) : "n"(4));
  TS(44);
  v4u rf[2];
  auto retile_a = [&]() {
    FENCE();
    VMW(2);
    TS(58);
    rf[0] = cat(trd(rta), trd(rta + 512)); rf[1] = cat(trd(rta + 64), trd(rta + 576));
    FENCE();
  };
  auto retile_b = [&]() {
    FENCE();
    st16(rf[0], rs, rto, 0); st16(rf[1], rs, rto, 65536);
    if (M0 < 4 || !MP) st16(sent4, rs, (w < M0 || (MP && cw) || (KO & 128)) ? OOB : hto, 0);
    FENCE(); if (KO & 256) st4(0u, rs, OOB, 0); else rawwd(); FENCE();
    float c[2];
#pragma unroll
    for (int i = 0; i < 2; ++i) c[i] = dot2acc(rf[i].w, ONES, dot2acc(rf[i].z, ONES, dot2acc(rf[i].y, ONES, dot2acc(rf[i].x, ONES, 0.f))));
    *(LAS float*)(L + L_HX + 2048 + 4 * tid) = fold32(c[0], c[1]);
    FENCE();
  };
  {
    const float LOG2E = 1.4426950408889634f, INV2PI = 0.15915494309189535f;
    const unsigned sel = (rg & 1) ? 0x03020c0cu : 0x01000c0cu;
#define HALF(v) asf(__builtin_amdgcn_perm(0u, v, sel))
    const float dt = rbf(__builtin_amdgcn_exp2f((cw ? bfhi(pdt) : bflo(pdt)) * LOG2E));
    const f32x2 z = f32x2{rbf(__builtin_amdgcn_exp2f(HALF(pA) * LOG2E)), HALF(pB)} * dt * f32x2{-LOG2E, INV2PI};
    lptr t = L + L_T + cw * T_CH + 256 * (rg & 1) + 4 * l;
    if (((rg >> 1) ^ cw) == 0) {
      f32x2 e[8];
      e[1] = cexp2(z, 1.f);
      const f32x2 e128 = cexp2(z, 128.f);
      put(t, 0, f32x2{1.f, 0.f}); put(t, 8, e128);
      const f32x2 e256 = cmul(e128, e128);
      e[2] = cmul(e[1], e[1]);
      const f32x2 e384 = cmul(e256, e128), e512 = cmul(e256, e256);
      e[3] = cmul(e[2], e[1]); e[4] = cmul(e[2], e[2]);
      const f32x2 e640 = cmul(e512, e128), e768 = cmul(e512, e256), e896 = cmul(e512, e384);
      e[5] = cmul(e[4], e[1]); e[6] = cmul(e[4], e[2]); e[7] = cmul(e[4], e[3]);
#pragma unroll
      for (int i = 1; i < 8; ++i) {
        put(t, i, e[i]); put(t, 8 + i, cmul(e[i], e128));
        if (i == 2) retile_a();
        if (i == 5) retile_b();
      }
      put(t, 16, e256); put(t, 17, cmul(e256, e[4])); put(t, 18, e384); put(t, 19, cmul(e384, e[4]));
      put(t, 20, cmul(e256, e[2])); put(t, 21, cmul(e256, e[6])); put(t, 22, cmul(e384, e[2])); put(t, 23, cmul(e384, e[6]));
      put(t, 24, e512); put(t, 25, cmul(e512, e[4])); put(t, 26, e640); put(t, 27, cmul(e640, e[4]));
      put(t, 28, e768); put(t, 29, cmul(e768, e[4])); put(t, 30, e896); put(t, 31, cmul(e896, e[4]));
    } else {
      unsigned m = umax(umax(pCr << 1, pCr << 17), umax(pCi << 1, pCi << 17));
      m = dppmax<0xB1>(m); m = dppmax<0x4E>(m); m = dppmax<0x141>(m); m = dppmax<0x140>(m);
      m = dppmax<0x142>(m); m = dppmax<0x143>(m);
      const f32x2 e8 = cexp2(z, 8.f), e64 = cexp2(z, 64.f);
      f32x2 a = cexp2(z, 1024.f);
      const f32x2 e128 = cmul(e64, e64), e16 = cmul(e8, e8), e256 = cmul(e128, e128);
      int ec = (int)((unsigned)__builtin_amdgcn_readlane((int)m, 63) >> 24);
      ec = ec < 5 ? 5 : ec;
      const unsigned db = (unsigned)__builtin_amdgcn_readfirstlane((int)asu(dt));
      int ei = ec + (int)(db >> 23) - 131;
      ei = ei < 0 ? 0 : (ei > 254 ? 254 : ei);
      *(LAS unsigned*)(L + L_INV + 4 * cw) = (unsigned)ei << 23;
      f32x2 v = f32x2{HALF(pCr), HALF(pCi)} * asf((db & 0x007fffffu) | ((unsigned)(259 - ec) << 23));
      f32x2 v1 = cmul(v, e8), a1 = cmul(a, e128);
      retile_a();
#pragma unroll
      for (int j = 0; j < 4; ++j) {
        putc(t, T_B + 2 * j, v); putc(t, T_B + 2 * j + 1, v1);
        putc(t, T_B + 8 + 2 * j, cmul(v, e64)); putc(t, T_B + 9 + 2 * j, cmul(v1, e64));
        put(t, 32 + 2 * j, a); put(t, 33 + 2 * j, a1);
        a = cmul(a, e256);
        if (j < 3) { v = cmul(v, e16); v1 = cmul(v1, e16); a1 = cmul(a1, e256); }
        if (j == 0) retile_b();
      }
      put(t, 40, a);
    }
  }
  FENCE();
  TS(41);
  lptr ta = L + L_T + cw * T_CH + (16 * role + n16) * T_ROW + 16 * g4, tb = L + L_T + cw * T_CH + (T_B + n16) * T_ROW + 16 * g4;
  if (role == 3) tb = L + L_HX + 2048 + (w & 1) * 256 + 4 * l;
  asm volatile("" : "+v"(ta), "+v"(tb));
  TS(4);
  BAR();
  TS(38);
  float csum;
  int f2o = OOB;
  if (role < 3) {
    v4u fr[8], fb[8];
#pragma unroll
    for (int ks = 0; ks < 8; ++ks) { fr[ks] = *(LAS v4u*)(ta + 64 * ks); fb[ks] = *(LAS v4u*)(tb + 64 * ks); }
    SBAR();
    f32x4 acc = {0.f, 0.f, 0.f, 0.f};
#pragma unroll
    for (int ks = 0; ks < 8; ++ks) acc = mfma16h(fr[ks], fb[ks], acc);
#pragma unroll
    for (int r = 0; r < 4; ++r) *(LAS _Float16*)(uintptr_t)kco[r] = (_Float16)acc[r];
    csum = 0.f;
  } else {
    csum = (*(LAS float*)(tb) + *(LAS float*)(tb + 512)) + (*(LAS float*)(tb + 1024) + *(LAS float*)(tb + 1536));
    if (retile) f2o = g4 * 2048 + n16 * 8;
  }
  FENCE();
#define F2ST() st8(v2u{asu(csum), tag}, rs, f2o, WS_F2 + cg * 8192 + (bid & 7) * 256 + (w & 1) * 128)
  FENCE();
  TS(39);
  const int gxs = WS_XT + cq * XT_Q + 4096 * w;
  v4u gx[4];
  asm volatile("buffer_load_dwordx4 %0, %4, %5, %6 offen sc1\n\tbuffer_load_dwordx4 %1, %4, %5, %6 offen offset:1024 sc1\n\t"
               "buffer_load_dwordx4 %2, %4, %5, %6 offen offset:2048 sc1\n\tbuffer_load_dwordx4 %3, %4, %5, %6 offen offset:3072 sc1"
               : "=&v"(gx[0]), "=&v"(gx[1]), "=&v"(gx[2]), "=&v"(gx[3]) : "v"(l16), "s"(rs), "s"(gxs) : "memory");
  FENCE();
  TS(40);
  VMW(5 + (M0 < 4 || !MP));
  TS(37);
  BAR();
  F2ST();
  FENCE();
  TS(5);
  {
    unsigned P[6], Q[5];
#pragma unroll
    for (int j = 0; j < 6; ++j) {
      const f16x2 e = {*(LAS _Float16*)(uintptr_t)eo[2 * j], j < 5 ? *(LAS _Float16*)(uintptr_t)eo[2 * j + 1] : (_Float16)0.f};
      P[j] = __builtin_bit_cast(unsigned, e);
    }
#pragma unroll
    for (int j = 0; j < 5; ++j) Q[j] = __builtin_amdgcn_alignbit(P[j + 1], P[j], 16);
    lptr kx = L + (cw ? L_KX1 : L_KX0) + 16 * (tid & 255);
    *(LAS v4u*)(kx) = v4u{P[0], P[1], P[2], P[3]};
    *(LAS v4u*)(kx + KX_CP) = v4u{Q[0], Q[1], Q[2], Q[3]};
    *(LAS v4u*)(kx + 2 * KX_CP) = v4u{P[1], P[2], P[3], P[4]};
    *(LAS v4u*)(kx + 3 * KX_CP) = v4u{Q[1], Q[2], Q[3], Q[4]};
  }
#define WDF() cat(trd(L + L_RD + w * 1024 + q4 * 128 + 32 * g4 + 8 * p4), trd(L + L_RD + w * 1024 + q4 * 128 + 32 * g4 + 8 * p4 + 512))
  FENCE();
  TS(6);
  asm volatile("s_waitcnt vmcnt(%4)" : "+v"(gx[0]), "+v"(gx[1]), "+v"(gx[2]), "+v"(gx[3]) : "n"(1));
  TS(7);
  FENCE();
  const v4u wdf = WDF();
  FENCE();
#define GMISS() ((neq3(gx[0].w, gx[1].w, tag) | neq3(gx[2].w, gx[3].w, tag)) != 0u)
  if (RARE(anylane(GMISS()))) {
    int it = 0;
    do {
      asm volatile("" ::: "memory");
      if (GMISS()) {
#pragma unroll
        for (int i = 0; i < 4; ++i) gx[i] = ld16(rs, l16 + 1024 * i, gxs);
      }
    } while (anylane(GMISS()) && ++it < SPIN_CAP);
  }
  FENCE();
  TS(8);
  unsigned up[4];
  {
#pragma unroll
    for (int i = 0; i < 4; ++i) up[i] = (cp & 1) ? gx[i].y : gx[i].x;
    lptr uw = L + L_U + 480 + 512 * w + 8 * l;
    *(LAS v2u*)(uw) = v2u{__builtin_amdgcn_perm(up[1], up[0], 0x05040100u), __builtin_amdgcn_perm(up[3], up[2], 0x05040100u)};
    *(LAS v2u*)(uw + U_CH) = v2u{__builtin_amdgcn_perm(up[1], up[0], 0x07060302u), __builtin_amdgcn_perm(up[3], up[2], 0x07060302u)};
  }
  FENCE();
  if (MP == 1 && cw) st16(sent4, rs, hto, 0);
  FENCE();
  st16(wdf, rs, (retile && !(KO & 512) ? g4 * 1024 + n16 * 16 : OOB) + WS_WDF + (w * 56 + (rbid >> 2)) * 4096 + (rbid & 3) * 256, 0);
  FENCE();
  const unsigned cnt = lds0 + L_CNT;
  lptr kA = L + (cw ? L_KX1 : L_KX0) + (3 - (n16 & 3)) * KX_CP + 2 * (12 - 4 * (n16 >> 2) + 8 * g4) + 64 * (6 - 2 * rg);
  lptr uB = L + L_U + cw * U_CH + 32 * n16 + 16 * g4 + 384 - 128 * rg;
  lptr redw = L + L_RED + w * 1024 + 16 * (4 * n16 + (g4 ^ ((n16 >> 1) & 3)));
  lptr redr = L + L_RED + w * 8192 + 16 * ((l & ~3) + (p4 ^ ((l >> 3) & 3)));
  int wvo = (w > 0 ? l16 : OOB) + WS_W1F + fgW * 32768;
  asm volatile("" : "+v"(kA), "+v"(uB), "+v"(redw), "+v"(redr), "+v"(wvo), "+v"(hfo));
  TS(42);
  BAR();
  TS(9); TR(61);
#define AFRAG(a, rr) cat(*(volatile LAS v2u*)(kA + 64 * (57 - 8 * (a) - (rr))), *(volatile LAS v2u*)(kA + 64 * (57 - 8 * (a) - (rr)) + 8))
#define BFRAG(b, rr) (*(LAS v4u*)(uB + 512 * (b) + 64 * (1 - (rr))))
  const int rot = (idx >> 2) * 4096;
  const int tso = WS_HT + rb * 32768;
  auto tile_dma = [&](int s, int c) {
    const int so = tso + s * (16 * 32768) + ((c * 4096) ^ rot);
    const unsigned d = lds0 + s * 32768 + c * 4096;
    dma<0>(rs, l16, so, d); dma<1024>(rs, l16, so, d); dma<2048>(rs, l16, so, d); dma<3072>(rs, l16, so, d);
  };
  v4u wa[32];
#define WAL(j) asm volatile("s_cmp_eq_u32 %3, %4\n\ts_cbranch_scc1 .Lwa%=\n\tbuffer_load_dwordx4 %0, %1, %2, %5 offen offset:%6 sc1\n.Lwa%=:" \
                            : "=v"(wa[j]) : "v"(wvo), "s"(rs), "s"(w), "n"(0), "s"((((j) >> 2) * 4096) ^ rot), "n"(((j) & 3) * 1024) : "scc", "memory")
  float hb0[4], hb1[4];
  auto conv_out = [&](int c) {
    const f32x2 invS = *(LAS f32x2*)(L + L_INV);
    f32x4 y[8];
    int it = 0;
    unsigned done;
    do {
      done = *(volatile LAS unsigned*)(L + L_CNT + 4 * c);
      FENCE();
#pragma unroll
      for (int v = 0; v < 8; ++v) y[v] = *(LAS f32x4*)(redr + 1024 * v);
      FENCE();
    } while (below(done, 8) && ++it < (1 << 22));
    float h0[4], h1[4];
#pragma unroll
    for (int i = 0; i < 4; ++i) {
      h0[i] = __builtin_fmaf(((y[0][i] + y[1][i]) + y[2][i]) + y[3][i], invS.x, hb0[i]);
      h1[i] = __builtin_fmaf(((y[4][i] + y[5][i]) + y[6][i]) + y[7][i], invS.y, hb1[i]);
    }
    st16(v4u{umin(pkbf_s(h0[0], h0[1]), SENT - 1), pkbf_s(h0[2], h0[3]), umin(pkbf_s(h1[0], h1[1]), SENT - 1), pkbf_s(h1[2], h1[3])}, rs, hto, 0);
  };
  {
    v4u A[2][8], B[2][8];
    f32x4 t[8];
#pragma unroll
    for (int i = 0; i < 8; ++i) t[i] = f32x4{0.f, 0.f, 0.f, 0.f};
#define MM(a, b, rr) t[(a) + (b)] = mfma16h(A[rr][a], B[rr][b], t[(a) + (b)])
#define MM2(a, b) do { MM(a, b, 0); MM(a, b, 1); } while (0)
#define LDA(a) do { A[0][a] = AFRAG(a, 0); A[1][a] = AFRAG(a, 1); } while (0)
#define LDB(b) do { B[0][b] = BFRAG(b, 0); B[1][b] = BFRAG(b, 1); } while (0)
    LDB(0); LDA(0); SBAR(); LDB(1); LDA(1); SBAR(); LDB(2); LDA(2); SBAR();
#pragma unroll
    for (int i = 0; i < 4; ++i) {
      const f16x2 u = __builtin_bit_cast(f16x2, up[i]);
      const float m = bflo(gx[i].z), sd = bfhi(gx[i].z);
      hb0[i] = __builtin_fmaf((float)u.x, sd + bflo(pds), m); hb1[i] = __builtin_fmaf((float)u.y, sd + bfhi(pds), m);
      asm volatile("" : "+v"(hb0[i]), "+v"(hb1[i]));
    }
    SBAR();
    v4u f2a, f2b;
#define F2LD() do { f2a = ld16(rs, l16, WS_F2 + fgW * 2048); f2b = ld16(rs, l16, WS_F2 + fgW * 2048 + 1024); } while (0)
    F2LD();
    FENCE();
    if (MP == 2) st16(sent4, rs, cw ? hto : OOB, 0);
    FENCE();
    MM2(0, 0); SBAR();
    LDB(3); SBAR();
    MM2(0, 1); SBAR();
    LDA(3); SBAR();
    MM2(1, 0); SBAR();
    st4(tag, rs, (bid == 0 && tid == 0) ? WS_EPOCH : OOB, 0);
    SBAR();
    MM2(1, 1); SBAR();
    *(LAS f32x4*)(redw) = t[0]; *(LAS f32x4*)(redw + 8192) = t[1];
    FENCE();
    arrive<C_S1>(cnt);
    FENCE();
    MM2(0, 2); MM2(2, 0); SBAR();
    TS(10);
    if (w < 2) conv_out(C_S1);
    FENCE();
    MM2(1, 2); MM2(2, 1); SBAR();
    LDB(4); SBAR();
    TS(43);
#define FMISS() anylane((neq3(f2a.y, f2a.w, tag) | neq3(f2b.y, f2b.w, tag)) != 0u)
    if (RARE(FMISS())) {
      int it = 0;
      do {
        asm volatile("" ::: "memory");
        F2LD();
      } while (FMISS() && ++it < SPIN_CAP);
    }
    FENCE();
    TS(11);
#define WA(n) do { sfor<wcnt(n) - wcnt((n) - 1)>([&](auto J) __attribute__((always_inline)) { WAL(wcnt((n) - 1) + J.v); SBAR(); }); \
                   if ((n) == TDS) { FENCE(); if (cw == 0) { tile_dma(0, 2 * w); tile_dma(0, 2 * w + 1); } FENCE(); } } while (0)
    WA(0);
    MM2(0, 3); SBAR();
    WA(1);
    MM2(3, 0); SBAR();
    WA(2); LDA(4); SBAR();
    MM2(1, 3); SBAR();
    *(LAS f32x4*)(redw + 16384) = t[2]; *(LAS f32x4*)(redw + 24576) = t[3];
    FENCE();
    if (MP == 2) VMW(wcnt(2) + 1);
    arrive<C_S2>(cnt);
    WA(3);
    MM2(2, 2); SBAR();
    WA(4);
    *(LAS float*)(L + L_CSW + w * 128 + 8 * n16 + 4 * (l >> 5)) = x16_add(fold32(asf(f2a.x) + asf(f2b.x), asf(f2a.z) + asf(f2b.z)));
    SBAR();
    MM2(3, 1); SBAR();
    WA(5);
    if (w == 2 || w == 3) conv_out(C_S2);
    FENCE();
    MM2(2, 3); SBAR();
    WA(6);
    MM2(3, 2); SBAR();
    WA(7);
    MM2(3, 3); SBAR();
    WA(8); LDB(5); SBAR();
    MM2(3, 4); SBAR();
    WA(9);
    MM2(2, 4); SBAR();
    WA(10); LDA(5); SBAR();
    MM2(1, 4); SBAR();
    WA(11);
    MM2(0, 4); SBAR();
    WA(12);
    MM2(4, 3); SBAR();
    WA(13); LDB(6); SBAR();
    MM2(4, 2); SBAR();
    WA(14);
    MM2(4, 1); SBAR();
    WA(15); LDA(6); SBAR();
    MM2(4, 0); SBAR();
    WA(16);
    MM2(2, 5); SBAR();
    *(LAS f32x4*)(redw + 32768) = t[4]; WA(17); LDB(7); SBAR();
    MM2(1, 5); SBAR();
    WA(18);
    MM2(0, 5); SBAR();
    WA(19); LDA(7); SBAR();
    MM2(5, 0); SBAR();
    WA(20);
    MM2(5, 1); SBAR();
    *(LAS f32x4*)(redw + 40960) = t[5]; WA(21);
    MM2(5, 2); SBAR();
    WA(22);
    MM2(0, 6); SBAR();
    WA(23);
    MM2(6, 0); SBAR();
    WA(24);
    MM2(1, 6); SBAR();
    *(LAS f32x4*)(redw + 49152) = t[6]; WA(25);
    MM2(6, 1); SBAR();
    WA(26);
    MM2(0, 7); SBAR();
    WA(27);
    MM2(7, 0); SBAR();
    WA(28);
    *(LAS f32x4*)(redw + 57344) = t[7];
  }
  FENCE();
  arrive<C_S3>(cnt);
  TS(12);
  auto tile_peek = [&](int s, int c) {
    lptr q = L + s * 32768 + c * 4096 + l16;
    return v4u{*(volatile LAS unsigned*)(q), *(volatile LAS unsigned*)(q + 1024), *(volatile LAS unsigned*)(q + 2048), *(volatile LAS unsigned*)(q + 3072)};
  };
#define TMISS(m) anylane(umax(umax(m.x, m.y), umax(m.z, m.w)) == SENT)
  auto tile_check = [&](v4u m, int s, int c) {
    if (RARE(TMISS(m))) {
      int it = 0;
      do {
        tile_dma(s, c);
        VMWAIT(0);
        m = tile_peek(s, c);
      } while (TMISS(m) && ++it < SPIN_CAP);
    }
    FENCE();
  };
  if (TDS > 28 && cw == 0) { tile_dma(0, 2 * w); tile_dma(0, 2 * w + 1); }
  if (CO1 && cw) conv_out(C_S3);
  FENCE();
  sfor<32 - ND - WC>([&](auto J) __attribute__((always_inline)) { WAL(WC + J.v); SBAR(); });
  v2u cA;
  {
    const float c = l < 32 ? *(LAS float*)(L + L_CSW + w * 128 + 4 * (l & 31)) : 0.f;
    const float hi = rbf(c), mid = rbf(c - hi);
    cA = v2u{pkbf(hi, mid), pkbf((c - hi) - mid, 0.f)};
    PIN(cA);
  }
  FENCE();
  if (cw) {
    if (!T04) arrive<C_T0>(cnt);
    if (!CO1) conv_out(C_S3);
    FENCE();
    TS(13);
  } else {
    TS(13);
    if (w == 0) VMWAIT(0);
    VMW(32 - ND - wcnt(TDS > 28 ? 28 : TDS));
    FENCE();
    const v4u m0 = tile_peek(0, 2 * w), m1 = tile_peek(0, 2 * w + 1);
    tile_check(m0, 0, 2 * w); tile_check(m1, 0, 2 * w + 1);
    if (T04) await<C_S3>(L, 8);
    arrive<C_T0, 1 + T04>(cnt);
  }
  FENCE();
  TS(14); TR(59);
  const int g2r = xcd * 4 + (idx >> 3), db = idx & 7, tbo = g2r >> 1, th = g2r & 1;
  const int wdo = WS_WDF + (db * 56 + 7 * w) * 4096;
  const int hvo = (g4 >> 1) * 1024 + ((g4 & 1) * 32 + th * 16 + n16) * 16;
  const int hso = WS_HF + (tbo * 112 + 14 * w) * 1024;
  const int rch = 64 * db + 16 * rg + (l >> 2);
  const int hro = WS_HT + (16 * cw + tbo) * 32768 + (rch >> 1) * 128 + (((4 * th) ^ ((rch >> 1) & 4)) + p4) * 16 + (rch & 1) * 8;
#define WDSO(j) (wdo + ((j) >> 2) * 4096 + ((((j) & 3) + rg) & 3) * 1024)
#define WDLD(j) ld16(rs, (KO & 2) ? OOB : l16, WDSO(j))
#define SLOT(j) ((j) < 12 ? L_KX1 - 2048 + w * 2048 + ((j) - 10) * 1024 : ((j) == 12 ? L_XC - 1024 + w * 1024 : ((j) - 13) / 4 * 32768 + w * 4096 + ((j) - 13) % 4 * 1024))
#define HLD(s, ks) ld16(rs, (KO & 4) ? OOB : hvo, hso + (s) * HF_S + (ks) * 2048)
#define FHLD() ld4(rs, 4 * (n16 < 14 ? n16 : 13), WS_FH + tbo * 512 + 56 * w)
#define FHST() st4(tag, rs, l == 0 ? 0 : OOB, WS_FH + rb * 512 + 4 * fgW)
  v4u wd[28], hb[3][7];
  v2u hres[2];
  unsigned fh;
  auto fh_check = [&]() {
    if (RARE(anylane(fh != tag))) {
      int it = 0;
      do {
        asm volatile("" ::: "memory");
        fh = FHLD();
      } while (anylane(fh != tag) && ++it < SPIN_CAP);
    }
    FENCE();
  };
  auto tile_wait = [&](unsigned v, int s) {
    lptr c = L + L_CNT + 4 * (C_T0 + s);
    if (RARE(below(v, 8))) {
      int it = 0;
      do __builtin_amdgcn_s_sleep(1); while (below(*(volatile LAS unsigned*)c, 8) && ++it < (1 << 22));
    }
    FENCE();
  };
#define TCNT(s) (*(volatile LAS unsigned*)(L + L_CNT + 4 * (C_T0 + (s))))
  if (w > 0) {
    lptr bt = L + (4 * (g4 >> 1) + (q4 >> 1)) * 128 + (4 * ((g4 ^ (g4 >> 1)) & 1) + p4) * 16 + (q4 & 1) * 8, bt2 = bt + 65536;
    lptr stp = L + L_ST2 + 16 * (l & 31);
    PIN(bt); PIN(bt2); PIN(stp);
#define BLD(g) cat(trd(((g) < 64 ? bt : bt2) + ((g) & 63) * 1024), trd(((g) < 64 ? bt : bt2) + ((g) & 63) * 1024 + 256))
    PRIO(1);
    if ((PA & 1) && cw) PRIO(2);
    { await<C_T0>(L, 8); FENCE(); }
    TS(15);
    f32x16 acc[2];
    v4u bf[4];
    f32x4 st;
    float gt[2], gu[2], ge[2], go[2];
    unsigned ho[4], tc, t5;
#define DMA5(t) dma<0>(rs, l16, tso + (t) * (16 * 32768) + rot + rg * 1024, lds0 + (t) * 32768 + rg * 1024)
#define PEEK5(t) (*(volatile LAS unsigned*)(L + (t) * 32768 + rg * 1024 + l16))
    v4u tm;
    u64 tq[8];
    auto st_check = [&](int s) {
      if (RARE(anylane(st.w == 0.f))) {
        int it = 0;
        do {
          __builtin_amdgcn_s_sleep(1);
          st = *(volatile LAS f32x4*)(stp + s * 512);
        } while (anylane(st.w == 0.f) && ++it < (1 << 22));
      }
      FENCE();
      acc[s & 1] = mfma32k8(cA, v2u{asu(st.x), asu(st.x)}, acc[s & 1]);
    };
    auto epi = [&](int s, int j, int stage) {
      const float g0 = acc[s & 1][2 * j], g1 = acc[s & 1][2 * j + 1];
      if (stage == 0) { gt[0] = __builtin_fmaf(g0 * g0, st.z, st.y); gt[1] = __builtin_fmaf(g1 * g1, st.z, st.y); PIN(gt[0]); PIN(gt[1]); }
      if (stage == 1) { gt[0] *= g0; gt[1] *= g1; gu[0] = g0 * acc[s & 1][8 + 2 * j]; PIN(gt[0]); PIN(gt[1]); PIN(gu[0]); }
      if (stage == 2) { ge[0] = __builtin_amdgcn_exp2f(gt[0]); gu[1] = g1 * acc[s & 1][9 + 2 * j]; PIN(ge[0]); PIN(gu[1]); }
      if (stage == 3) { ge[1] = __builtin_amdgcn_exp2f(gt[1]); ge[0] = __builtin_fmaf(ge[0], st.w, st.w); PIN(ge[0]); PIN(ge[1]); }
      if (stage == 4) { ge[0] = __builtin_amdgcn_rcpf(ge[0]); ge[1] = __builtin_fmaf(ge[1], st.w, st.w); PIN(ge[0]); PIN(ge[1]); }
      if (stage == 5) { ge[1] = __builtin_amdgcn_rcpf(ge[1]); go[0] = gu[0] * ge[0]; PIN(go[0]); PIN(ge[1]); }
      if (stage == 6) { go[1] = gu[1] * ge[1]; PIN(go[1]); }
      if (stage == 7) { ho[j] = pkbf(go[0], go[1]); PIN(ho[j]); }
    };
#define HST(s) st16(v4u{umin(ho[0], SENT - 1), ho[1], ho[2], ho[3]}, rs, hfo, (s) * HF_S)
#pragma unroll
    for (int g = 0; g < 3; ++g) bf[g] = BLD(g);
    SBAR();
    sfor<128>([&](auto G) __attribute__((always_inline)) {
      constexpr int g = G.v, s = g >> 5, ks = g & 31;
      if constexpr (ks == 0) {
#pragma unroll
        for (int r = 0; r < 16; ++r) acc[s & 1][r] = 0.f;
      }
      if constexpr (g + 3 < 128) bf[(g + 3) & 3] = BLD(g + 3);
      if constexpr (!(KO & 16) && g < 32 - ND && g <= G_V[1]) asm volatile("s_waitcnt vmcnt(%1)" : "+v"(wa[ks]) : "n"(31 - ND - g + younger(-1, g)));
      if constexpr (!(KO & 16) && g >= 32 - ND && g < 32) asm volatile("s_waitcnt vmcnt(%1)" : "+v"(wa[ks]) : "n"(younger(wa_at(g), g)));
      acc[s & 1] = mfma32b(wa[ks], bf[g & 3], acc[s & 1]);
      SBAR();
      if constexpr (s > 0) {
        constexpr int ke = ks - EO;
        if constexpr (ke == (EO ? -1 : 0)) st = *(volatile LAS f32x4*)(stp + (s - 1) * 512);
        if constexpr (ke == 1) st_check(s - 1);
        constexpr int n = ES[s];
        if constexpr (ke >= 3 && ke < hstep(s) - EO) {
          sfor<n>([&](auto R) __attribute__((always_inline)) {
            constexpr int e = (ke - 3) * n + R.v, j = e / 6, q = e % 6;
            if constexpr (q < 2 && j > 0 && j < 5) epi(s - 1, j - 1, 6 + q);
            if constexpr (j < 4) epi(s - 1, j, q);
          });
        }
        if constexpr (ks == hstep(s)) HST(s - 1);
      }
      constexpr int v = g < G_V[1] + 3 ? 1 : (g < G_V[2] + 3 ? 2 : 3), u = g <= G_W[1] ? 1 : (g <= G_W[2] ? 2 : 3);
      if constexpr (g == G_V[v]) { if (!(KO & 32)) VMW(younger(G_T[v] + 4 * D_T[v], g)); tm = tile_peek(v, w); t5 = PEEK5(v); }
      if constexpr (g == G_V[v] + 2) {
        if (RARE(anylane(umax(umax(umax(tm.x, tm.y), umax(tm.z, tm.w)), t5) == SENT))) {
          int it = 0;
          do {
            tile_dma(v, w); DMA5(v);
            VMWAIT(0);
            tm = tile_peek(v, w); t5 = PEEK5(v);
          } while (anylane(umax(umax(umax(tm.x, tm.y), umax(tm.z, tm.w)), t5) == SENT) && ++it < SPIN_CAP);
        }
        FENCE();
        arrive<C_T0 + v>(cnt);
      }
      if constexpr (g == G_W[u] - 2) tc = TCNT(u);
      if constexpr (g == G_W[u]) tile_wait(tc, u);
      if constexpr (op_tile(g) >= 0) {
        constexpr int t = op_tile(g) >> 3;
        if constexpr ((op_tile(g) & 7) < 4) dma<(op_tile(g) & 3) * 1024>(rs, l16, tso + t * (16 * 32768) + ((w * 4096) ^ rot), lds0 + t * 32768 + w * 4096);
        else DMA5(t);
      }
      if constexpr (op_wa(g) >= 0) asm volatile("buffer_load_dwordx4 %0, %1, %2, %3 offen offset:%4 sc1"
                                                : "=v"(wa[op_wa(g)]) : "v"((KO & 64) ? OOB : wvo), "s"(rs), "s"(((op_wa(g) >> 2) * 4096) ^ rot), "n"((op_wa(g) & 3) * 1024) : "memory");
      if constexpr (op_mark(g) >= 0) st16(sent4, rs, (KO & 1) ? OOB : hfo, op_mark(g) * HF_S);
      if constexpr (g == G_FLAG) { VMW(younger(GMK + 3 * DMK, g)); FHST(); }
      if constexpr (op_wd(g) >= 0) wd[op_wd(g)] = WDLD(op_wd(g));
      if constexpr (op_stage(g) >= 0) dma<0>(rs, (KO & 2) ? OOB : l16, WDSO(op_stage(g)), lds0 + SLOT(op_stage(g)));
      if constexpr (g == G_FH) fh = FHLD();
      if constexpr (g == 103) fh_check();
      if constexpr (op_h(g) >= 0) hb[op_h(g) / 7][op_h(g) % 7] = HLD(op_h(g) / 7, op_h(g) % 7);
      if constexpr (g == 105) VMW(younger(91, g));
      if constexpr (op_back(g) >= 0) wd[op_back(g)] = *(LAS v4u*)(L + SLOT(op_back(g)) + l16);
      if constexpr (g == G_S3) st = *(volatile LAS f32x4*)(stp + 3 * 512);
      if constexpr (g == G_S3 + 2) st_check(3);
      if constexpr (g == G_R0) hres[0] = ld8(rs, hro, 0);
      if constexpr (g == G_R1) hres[1] = ld8(rs, hro, 32 * 32768);
      if constexpr (g == 95) arrive<C_M2>(cnt);
      if constexpr ((g & 3) == 3 && g < 127 && (((PA >> (g >> 2)) ^ (PA >> ((g + 1) >> 2))) & 1)) { if (cw) PRIO(((PA >> ((g + 1) >> 2)) & 1) ? 2 : 1); }
      if constexpr (ks == 31) TS(16 + 2 * s);
      if constexpr (g == 63) TR(62);
      if constexpr (s == LS && (ks & 3) == 3) TQ(tq[ks >> 2]);
      if constexpr (s == LS && ks == 31) { TW(tq[0], 50); TW(tq[1], 51); TW(tq[2], 52); TW(tq[3], 53); TW(tq[4], 54); TW(tq[5], 55); TW(tq[6], 56); TW(tq[7], 57); }
      FENCE();
    });
    arrive<C_M3>(cnt);
    {
      float t[8], u[8];
#define EP(x) _Pragma("unroll") for (int j = 0; j < 8; ++j) { x; } SBAR()
      EP(t[j] = acc[1][j] * acc[1][j]; PIN(t[j]));
      EP(t[j] = __builtin_fmaf(t[j], st.z, st.y); PIN(t[j]));
      EP(t[j] *= acc[1][j]; PIN(t[j]));
      EP(t[j] = __builtin_amdgcn_exp2f(t[j]); u[j] = acc[1][j] * acc[1][8 + j]; PIN(t[j]); PIN(u[j]));
      EP(t[j] = __builtin_fmaf(t[j], st.w, st.w); PIN(t[j]));
      EP(t[j] = __builtin_amdgcn_rcpf(t[j]); PIN(t[j]));
      EP(t[j] *= u[j]; PIN(t[j]));
#pragma unroll
      for (int j = 0; j < 4; ++j) ho[j] = pkbf(t[2 * j], t[2 * j + 1]);
      HST(3);
    }
    FENCE();
    TS(21);
  } else {
    lptr b0 = L + g4 * 512 + (q4 >> 1) * 128 + (g4 & 1) * 64 + p4 * 16 + (q4 & 1) * 8, b1 = L + g4 * 512 + (q4 >> 1) * 128 + ((g4 & 1) ^ 1) * 64 + p4 * 16 + (q4 & 1) * 8;
    v4u ones = {ONES, ONES, ONES, ONES};
    int sel = (l & 1) ? 0xeeeeeeee : 0x44444444;
    PIN(ones); PIN(sel);
    v2u f[4][8];
#define GLD(o, G, i) f[(G) & 3][i] = trd(((i) < 4 ? b0 : b1) + (o) + (G) * 4096 + ((i) & 2) * 1024 + ((i) & 1) * 256)
    if (HP) PRIO(HP);
    arrive<C_T1>(cnt);
    { await<C_T0>(L, 8); FENCE(); }
    TS(15);
#pragma unroll
    for (int i = 0; i < 16; ++i) GLD(0, i >> 3, i & 7);
    FENCE();
#pragma unroll 1
    for (int s = 0; s < 4; ++s) {
      const int ot = s * 32768, on = ((s + 1) & 3) * 32768;
      f32x4 gq[2] = {{0.f, 0.f, 0.f, 0.f}, {0.f, 0.f, 0.f, 0.f}}, gs[2] = {{0.f, 0.f, 0.f, 0.f}, {0.f, 0.f, 0.f, 0.f}};
      unsigned tc;
#pragma unroll
      for (int G = 0; G < 8; ++G) {
        const v2u* e = f[G & 3];
#define SQ(h, j) do { const v4u t_ = cat(e[4 * (h) + 2 * (j)], e[4 * (h) + 2 * (j) + 1]); gq[h] = mfma16b(t_, t_, gq[h]); SBAR(); } while (0)
#define SM(h) do { const v8u t_ = {e[4 * (h)].x, e[4 * (h)].y, e[4 * (h) + 1].x, e[4 * (h) + 1].y, e[4 * (h) + 2].x, e[4 * (h) + 2].y, e[4 * (h) + 3].x, e[4 * (h) + 3].y}; \
        if (!(KO & 8)) gs[h] = __builtin_amdgcn_smfmac_f32_16x16x64_bf16(__builtin_bit_cast(b16x8, ones), __builtin_bit_cast(b16x16, t_), gs[h], sel, 0, 0); SBAR(); } while (0)
#define NX(i) do { if (G < 6) GLD(ot, G + 2, i); else GLD(on, G - 6, i); SBAR(); } while (0)
        SQ(0, 0); NX(0);
        SQ(1, 0); NX(1);
        SQ(0, 1); NX(4); NX(5);
        SM(0); NX(2);
        SQ(1, 1); NX(3);
        SM(1); NX(6); NX(7);
        if (G == 5) { FENCE(); arrive<C_T2>(cnt + 4 * s); tc = TCNT((s + 1) & 3); FENCE(); }
        if ((G & 1) == 0 && s == HWS) {
          FENCE();
#pragma unroll
          for (int j = 0; j < 7; ++j) wd[7 * (G >> 1) + j] = WDLD(7 * (G >> 1) + j);
          FENCE();
        }
        SBAR();
      }
      const float C1 = -2.f * 0.7978845608028654f * 1.4426950408889634f, C2c = C1 * 0.044715f;
      float q[2];
#pragma unroll
      for (int hh = 0; hh < 2; ++hh) q[hh] = (p4 & 2) ? ((p4 & 1) ? gq[hh][3] : gq[hh][2]) : ((p4 & 1) ? gq[hh][1] : gq[hh][0]);
      const auto sw = __builtin_amdgcn_permlane32_swap(asu(q[1]), asu(q[1]), false, false);
      const bool first = q4 == g4;
      const float mean = (first ? gs[0][0] + gs[0][1] : gs[1][0] + gs[1][1]) * (1.f / DM);
      const float var = rbf(fmaxf((first ? q[0] : asf(l < 32 ? sw[1] : sw[0])) * (1.f / DM) - mean * mean, 0.f));
      const float sd = rbf(__builtin_amdgcn_sqrtf(rbf(var + 1e-5f)));
      const float r = __builtin_amdgcn_rcpf(sd);
      if (((q4 ^ g4) & 1) == 0) *(LAS f32x4*)(L + L_ST2 + s * 512 + (first ? 0 : 256) + 16 * n16) = f32x4{asf(pkbf(-mean, -mean)), C1 * r, C2c * r * r * r, sd * sd};
      FENCE();
      if (s == 0) TS(16); else if (s == 1) TS(18); else if (s == 2) TS(20); else TS(22);
      if (below(tc, 8)) {
        tile_wait(tc, s + 1);
#pragma unroll
        for (int i = 0; i < 16; ++i) GLD(on, i >> 3, i & 7);
      }
      FENCE();
    }
    if (HP) PRIO(0);
    fh = FHLD();
    await<C_M2>(L, 8);
    fh_check();
#pragma unroll
    for (int j = 0; j < NH; ++j) hb[j / 7][j % 7] = HLD(j / 7, j % 7);
    hres[0] = ld8(rs, hro, 0);
    hres[1] = ld8(rs, hro, 32 * 32768);
  }
  FENCE();
  PRIO(0);
  {
    unsigned pw[4];
#pragma unroll
    for (int j = 0; j < 4; ++j) pw[j] = lds0 + w * 4096 + n16 * 256 + (((((j + rg) & 3) * 4 + g4) ^ n16) * 16);
    lptr hx = L + L_HX + w * 512 + 8 * l;
    PIN(hx); PIN(pw[0]); PIN(pw[1]); PIN(pw[2]); PIN(pw[3]);
    lptr pr3;
    unsigned oo3;
    f32x4 acc[4];
    v2u ht[2];
#define HMAX(h) umax(umax(umax(h[0].x, h[1].x), umax(h[2].x, h[3].x)), umax(umax(h[4].x, h[5].x), h[6].x))
    auto h_check = [&](unsigned m, int s) {
      v4u* h = hb[s % 3];
      if (RARE(anylane(m == SENT))) {
        int it = 0;
        do {
          asm volatile("" ::: "memory");
          if (m == SENT) {
#pragma unroll
            for (int ks = 0; ks < 7; ++ks) h[ks] = HLD(s, ks);
          }
          m = HMAX(h);
        } while (anylane(m == SENT) && ++it < SPIN_CAP);
      }
      FENCE();
    };
#define PWR(c) *(LAS f32x4*)(uintptr_t)(pw[3 - ((c) & 3)] + ((c) >> 2) * 32768) = acc[(c) & 3]
    unsigned hm = HMAX(hb[0]), mc;
    TS(23); TR(60);
    auto stream = [&](auto M) __attribute__((always_inline)) {
      constexpr int m = M.v, q = m % 14, c = m / 14 * 2 + (m & 1), ks = q / 2, s = c >> 2, dt = 3 - (c & 3);
      if constexpr (m % 28 == 0) { h_check(hm, s); TS(45 + s); }
      if constexpr (ks == 0 && dt > 0) asm volatile("v_mfma_f32_16x16x32_bf16 %0, %1, %2, 0" : "=v"(acc[c & 3]) : "v"(wd[dt]), "v"(hb[s % 3][0]));
      else asm volatile("v_mfma_f32_16x16x32_bf16 %0, %1, %2, %0" : "+v"(acc[c & 3]) : "v"(wd[4 * ks + dt]), "v"(hb[s % 3][ks]));
      SBAR();
      if constexpr (m % 28 == 10) {
        const unsigned lo = cw == (s & 1) ? 0x01000c0cu : 0x0c0c0c0cu, hi = cw == (s & 1) ? 0x03020c0cu : 0x0c0c0c0cu;
        const v2u t = ht[s >> 1];
        acc[3] = f32x4{asf(__builtin_amdgcn_perm(0u, t.x, lo)), asf(__builtin_amdgcn_perm(0u, t.x, hi)), asf(__builtin_amdgcn_perm(0u, t.y, lo)), asf(__builtin_amdgcn_perm(0u, t.y, hi))};
        PIN(acc[3]);
      }
      if constexpr (m == 64) mc = peek<C_M2>(L);
      if constexpr (m == 71) { if (RARE(below(mc, 8))) await<C_M2>(L, 8); FENCE(); }
      if constexpr (m == 80) mc = peek<C_M3>(L);
      if constexpr (m == 85) { const int tk = 2 * w + (l >> 5); pr3 = L + 3 * 32768 + tk * 256 + ((((l & 31) >> 1) ^ tk) * 16) + (l & 1) * 8; PIN(pr3); }
      if constexpr (m == 88) { oo3 = (1536 + 16 * g2r + 2 * w + (l >> 5)) * 1024 + 128 * db + 4 * (l & 31); PIN(oo3); }
      if constexpr ((q == 2 || q == 5) && m >= 14 && m / 14 != 6) PWR(c - 2);
      if constexpr (m == 34) { FENCE(); arrive<C_P0>(cnt); }
      if constexpr (m == 62) { FENCE(); arrive<C_P1>(cnt); }
      if constexpr (every(m, 1, D_H2, 21 - NH) >= 0) { constexpr int j = NH + every(m, 1, D_H2, 21 - NH); hb[j / 7][j % 7] = HLD(j / 7, j % 7); }
      if constexpr (m == 2) {
        if (RARE(anylane(umax(hres[0].x, hres[1].x) == SENT))) {
          int it = 0;
          do {
            asm volatile("" ::: "memory");
            hres[0] = ld8(rs, hro, 0);
            hres[1] = ld8(rs, hro, 32 * 32768);
          } while (anylane(umax(hres[0].x, hres[1].x) == SENT) && ++it < SPIN_CAP);
        }
        FENCE();
        *(LAS v2u*)(hx) = hres[0];
        FENCE();
        ht[0] = trd(hx);
      }
      if constexpr (m == 3) {
        *(LAS v2u*)(hx) = hres[1];
        FENCE();
        ht[1] = trd(hx);
      }
      if constexpr (every(m, G_H3, D_H3, 7) >= 0) hb[0][every(m, G_H3, D_H3, 7)] = HLD(3, every(m, G_H3, D_H3, 7));
      if constexpr (m == 26 || m == 54) { hm = HMAX(hb[(s + 1) % 3]); PIN(hm); }
      FENCE();
    };
    sfor<84>(stream);
    asm volatile("s_nop 7");
    FENCE();
    PWR(10); PWR(11);
    FENCE();
    arrive<C_P2>(cnt);
    TS(24);
    const int tk = 4 * rg + g4;
    lptr prd = L + tk * 256 + ((n16 ^ tk) * 16);
    GAS v2u* op = (GAS v2u*)((GAS unsigned char*)p.out + (unsigned)((16 * g2r + tk) * 1024 + 128 * db + 8 * n16));
    auto reduce = [&](int s) {
      f32x4 v[8];
#pragma unroll
      for (int j = 0; j < 8; ++j) v[j] = *(LAS f32x4*)(prd + s * 32768 + j * 4096);
      float a[4];
#pragma unroll
      for (int i = 0; i < 4; ++i) {
        a[i] = v[0][i];
#pragma unroll
        for (int j = 1; j < 8; ++j) a[i] += v[j][i];
      }
      op[s * (512 * 128)] = v2u{pkbf_s(a[0], a[1]), pkbf_s(a[2], a[3])};
      FENCE();
    };
    if (cw == 0) {
      await<C_P0>(L, 8);
      reduce(0);
      await<C_P1>(L, 8);
      reduce(1);
      hm = HMAX(hb[0]);
      PIN(hm);
    } else {
      if (RARE(below(mc, 8))) await<C_M3>(L, 8);
      hm = HMAX(hb[0]);
      PIN(hm);
    }
    FENCE();
    TS(25);
    sfor<28>([&](auto M) __attribute__((always_inline)) { stream(ic<84 + M.v>{}); });
    asm volatile("s_nop 7");
    FENCE();
    PWR(14); PWR(15);
    FENCE();
    arrive<C_P3>(cnt);
    TS(26);
    if (cw == 0) { await<C_P2>(L, 8); reduce(2); }
    TS(49);
    {
      f32x2 v[8];
      int it = 0;
      unsigned done;
      do {
        done = peek<C_P3>(L);
        FENCE();
#pragma unroll
        for (int j = 0; j < 8; ++j) v[j] = *(volatile LAS f32x2*)(pr3 + j * 4096);
        FENCE();
      } while (below(done, 8) && ++it < (1 << 22));
      const f32x2 a = ((v[0] + v[1]) + (v[2] + v[3])) + ((v[4] + v[5]) + (v[6] + v[7]));
      *(GAS unsigned*)((GAS unsigned char*)p.out + oo3) = pkbf(a.x, a.y);
    }
    TS(27);
  }
  asm volatile(".Lcode_end:");
}

extern "C" void kernel_launch(void* const* d_in, const int* in_sizes, int n_in, void* d_out, int out_size, void* d_ws,
                              size_t ws_size, hipStream_t stream) {
  Params p;
  p.x = (const u16*)d_in[0]; p.logA = (const u16*)d_in[1]; p.aim = (const u16*)d_in[2]; p.cre = (const u16*)d_in[3];
  p.cim = (const u16*)d_in[4]; p.logdt = (const u16*)d_in[5]; p.dskip = (const u16*)d_in[6];
  p.wg = (const u16*)d_in[8]; p.wu = (const u16*)d_in[9]; p.wd = (const u16*)d_in[10];
  p.out = (u16*)d_out; p.ws = (unsigned char*)d_ws;
  (void)hipFuncSetAttribute((const void*)fused_kernel, hipFuncAttributeMaxDynamicSharedMemorySize, L_BYTES);
  fused_kernel<<<dim3(256), dim3(512), L_BYTES, stream>>>(p);
}
```

```cpp
#define HR 1
#define T1H 3
#define GV1 16
#define XB 9
#define RL 2
#define P_EO 5
#define P_KC 4
#define P_RT 2
#define P_IN 2
#define KS 1
#define P_A1 6
#define MP 3
#define ND 10
#define GT1 6
#define GMK 16
#define CO1 1
#define M0 4
#define OPQ 1
#define P_A0 3
#define P_A2 8
#define P_A3 7
#define P_A4 6
#define P_SC 9
#define CPR 0
#define T0F 0
#define NTX 0
#define NTW 0
#define NTP 0
#define OST 0
#define RDE 0
#define NTXS ""
#define NTWS ""
#define NTPS ""
#include <hip/hip_runtime.h>
#include <stdint.h>

typedef unsigned short u16;
typedef unsigned long long u64;
typedef unsigned v4u __attribute__((ext_vector_type(4)));
typedef unsigned v2u __attribute__((ext_vector_type(2)));
typedef float f32x2 __attribute__((ext_vector_type(2)));
typedef float f32x4 __attribute__((ext_vector_type(4)));
typedef float f32x16 __attribute__((ext_vector_type(16)));
typedef short s16x4 __attribute__((ext_vector_type(4)));
typedef _Float16 f16x8 __attribute__((ext_vector_type(8)));
typedef _Float16 f16x2 __attribute__((ext_vector_type(2)));
typedef __bf16 b16x8 __attribute__((ext_vector_type(8)));
typedef __bf16 b16x2 __attribute__((ext_vector_type(2)));
typedef __bf16 b16x16 __attribute__((ext_vector_type(16)));
typedef unsigned v8u __attribute__((ext_vector_type(8)));
#define LAS __attribute__((address_space(3)))
#define GAS __attribute__((address_space(1)))
typedef LAS unsigned char* lptr;

#define CAS __attribute__((address_space(4)))

#define STAMP 0
#define LS 2
#define EO 0
#define HWS 2
#define ESA 1
#define ESB 2
#define KO 0
#define HP 1
#define GH3 48
#define DH3 4
#define DH2 5
#define DT1 1
#define GV2 54
#define GV3 66
#define GFL 45
#define GFH 83
#define DMK 2
#define WN 1
#define WD_ 2
#define T04 1
#define PA 0u
#define FE 0
#define RA 0
#define FL 0
#define FC 0
#define W0 0
#define HCK 3
#define GT2 29
#define GT3 35
#define DT3 2
#define GW1 28
#define RR 0
#define R2A 90
#define R2B 97
#define RS 0
#define RI 2
#define RJ -1
#define TDE 0
#define TDS 99

constexpr int SEQ = 2048, DM = 512, NMODE = 128, FF = 1792;
constexpr int SC1 = 16;
constexpr int SPIN_CAP = 20000;
constexpr int OOB = 0x40000000;
constexpr unsigned SENT = 0xffffffffu;

constexpr int WS_EPOCH = 0;
constexpr int WS_FH = 4096;
constexpr int WS_F2 = 65536;
constexpr int WS_HT = 1 << 20;
constexpr int WS_W1F = 3 << 20;
constexpr int WS_WDF = 7 << 20;
constexpr int WS_HF = 10 << 20;
constexpr int HF_S = 16 * 112 * 1024;
constexpr int WS_XT = 17 << 20;
constexpr int XT_Q = 32768 + 256;
constexpr int WS_DBG = 22 << 20;
constexpr int WS_BYTES = 24 << 20;
static_assert(WS_XT + 128 * XT_Q <= WS_DBG, "ws map");

constexpr int L_RT = 0, L_RD = 16384, L_DUMP = 28672;
constexpr int KX_CP = 4160, KX_CH = 4 * KX_CP;
constexpr int L_KX0 = 32768, L_U = L_KX0 + KX_CH, U_CH = 4576, L_KC = L_U + 2 * U_CH;
constexpr int KC0 = 0x3f78, KC_CH = 2 * (0x4548 - KC0);
constexpr int L_T = 65536, T_ROW = 544, T_B = 41, T_CH = 57 * T_ROW;
constexpr int L_RED = 65536;
constexpr int L_KX1 = 131072;
constexpr int L_CNT = L_KX1 + KX_CH;
constexpr int L_ST2 = L_CNT + 128;
constexpr int L_CSW = L_ST2 + 2048;
constexpr int L_INV = L_CSW + 1024;
constexpr int L_HX = L_INV + 16;
constexpr int L_XC = L_HX + 4096, XC_ROW = 1056;
constexpr int L_BYTES = L_XC + 8 * XC_ROW;
static_assert(L_BYTES <= 163840, "lds");
static_assert(L_KC + 2 * KC_CH <= 65536 && 2 * T_CH <= 65536, "lds");
static_assert(L_U % 16 == 0 && L_KC % 16 == 0 && L_CNT % 16 == 0 && T_CH % 16 == 0, "align");
enum { C_S1 = 0, C_S2, C_S3, C_T0, C_T1, C_T2, C_T3, C_M2, C_M3, C_P0, C_P1, C_P2, C_P3 };

struct Params {
  const u16 *x, *logA, *aim, *cre, *cim, *logdt, *dskip, *wg, *wu, *wd;
  u16* out;
  unsigned char* ws;
};

__device__ __forceinline__ float asf(unsigned v) { return __builtin_bit_cast(float, v); }
__device__ __forceinline__ unsigned asu(float v) { return __builtin_bit_cast(unsigned, v); }
__device__ __forceinline__ float bflo(unsigned v) { return asf(v << 16); }
__device__ __forceinline__ float bfhi(unsigned v) { return asf(v & 0xffff0000u); }
__device__ __forceinline__ unsigned pkbf(float lo, float hi) {
  b16x2 r = {(__bf16)lo, (__bf16)hi};
  return __builtin_bit_cast(unsigned, r);
}
__device__ __forceinline__ unsigned pkbf_s(float lo, float hi) {
  asm("" : "+v"(lo));
  asm("" : "+v"(hi));
  return pkbf(lo, hi);
}
__device__ __forceinline__ float rbf(float f) { return bflo(pkbf(f, f)); }
__device__ __forceinline__ unsigned pkh(float lo, float hi) {
  f16x2 r = {(_Float16)lo, (_Float16)hi};
  return __builtin_bit_cast(unsigned, r);
}
template <int C> __device__ __forceinline__ unsigned dppu(unsigned v) {
  return (unsigned)__builtin_amdgcn_update_dpp(0, (int)v, C, 0xf, 0xf, true);
}
template <int C> __device__ __forceinline__ float dppf(float v) { return asf(dppu<C>(asu(v))); }
__device__ __forceinline__ float x16_add(float v) {
  auto s = __builtin_amdgcn_permlane16_swap(asu(v), asu(v), false, false);
  return asf(s[0]) + asf(s[1]);
}
__device__ __forceinline__ float fold32(float a, float b) {
  auto s = __builtin_amdgcn_permlane32_swap(asu(a), asu(b), false, false);
  return asf(s[0]) + asf(s[1]);
}
__device__ __forceinline__ float dot2acc(unsigned a, unsigned b, float c) {
  return __builtin_amdgcn_fdot2_f32_bf16(__builtin_bit_cast(b16x2, a), __builtin_bit_cast(b16x2, b), c, false);
}
__device__ __forceinline__ unsigned umax(unsigned a, unsigned b) { return a > b ? a : b; }
__device__ __forceinline__ unsigned umin(unsigned a, unsigned b) { return a < b ? a : b; }
constexpr unsigned ONES = 0x3f803f80u;
#define BAR() asm volatile("s_waitcnt lgkmcnt(0)\n\ts_barrier" ::: "memory")
#define VMWAIT(N) asm volatile("s_waitcnt vmcnt(" #N ")" ::: "memory")
#define SBAR() __builtin_amdgcn_sched_barrier(0)
#define FENCE() do { asm volatile("" ::: "memory"); __builtin_amdgcn_sched_barrier(0); } while (0)
#define PRIO(n) __builtin_amdgcn_s_setprio(n)

__device__ __forceinline__ void glds1(const void* g, unsigned dst) {
  unsigned sv;
  asm volatile("s_mov_b32 %0, m0\n\ts_mov_b32 m0, %2\n\ts_nop 0\n\tglobal_load_lds_dwordx4 %1, off\n\ts_mov_b32 m0, %0"
               : "=&s"(sv) : "v"(g), "s"(dst) : "memory");
}
__device__ __forceinline__ v2u trd(lptr p) {
  return __builtin_bit_cast(v2u, __builtin_amdgcn_ds_read_tr16_b64_v4i16((LAS s16x4*)p));
}
__device__ __forceinline__ v4u cat(v2u a, v2u b) { return v4u{a.x, a.y, b.x, b.y}; }
__device__ __forceinline__ f32x4 mfma16h(v4u a, v4u b, f32x4 c) {
  return __builtin_amdgcn_mfma_f32_16x16x32_f16(__builtin_bit_cast(f16x8, a), __builtin_bit_cast(f16x8, b), c, 0, 0, 0);
}
__device__ __forceinline__ f32x4 mfma16b(v4u a, v4u b, f32x4 c) {
  return __builtin_amdgcn_mfma_f32_16x16x32_bf16(__builtin_bit_cast(b16x8, a), __builtin_bit_cast(b16x8, b), c, 0, 0, 0);
}
__device__ __forceinline__ f32x16 mfma32b(v4u a, v4u b, f32x16 c) {
  return __builtin_amdgcn_mfma_f32_32x32x16_bf16(__builtin_bit_cast(b16x8, a), __builtin_bit_cast(b16x8, b), c, 0, 0, 0);
}
typedef __amdgpu_buffer_rsrc_t rsrc_t;
__device__ __forceinline__ v4u ld16(rsrc_t rs, int voff, int soff) { return __builtin_amdgcn_raw_buffer_load_b128(rs, voff, soff, SC1); }
__device__ __forceinline__ unsigned ld4(rsrc_t rs, int voff, int soff) { return __builtin_amdgcn_raw_buffer_load_b32(rs, voff, soff, SC1); }
__device__ __forceinline__ v2u ld8(rsrc_t rs, int voff, int soff) { return __builtin_amdgcn_raw_buffer_load_b64(rs, voff, soff, SC1); }
__device__ __forceinline__ void st16(v4u v, rsrc_t rs, int voff, int soff) { __builtin_amdgcn_raw_buffer_store_b128(v, rs, voff + soff, 0, SC1); }
__device__ __forceinline__ void st8(v2u v, rsrc_t rs, int voff, int soff) { __builtin_amdgcn_raw_buffer_store_b64(v, rs, voff, soff, SC1); }
__device__ __forceinline__ void st4(unsigned v, rsrc_t rs, int voff, int soff) { __builtin_amdgcn_raw_buffer_store_b32(v, rs, voff, soff, SC1); }
template <int OFF> __device__ __forceinline__ void dma(rsrc_t rs, int voff, int soff, unsigned dst) {
  asm volatile("s_mov_b32 m0, %3\n\ts_nop 0\n\tbuffer_load_dwordx4 %0, %1, %2 offen offset:%4 sc1 lds"
               :: "v"(voff), "s"(rs), "s"(soff), "s"(dst), "n"(OFF) : "memory");
}
__device__ __forceinline__ f32x16 mfma32k8(v2u a, v2u b, f32x16 c) {
  return __builtin_amdgcn_mfma_f32_32x32x8bf16_1k(__builtin_bit_cast(s16x4, a), __builtin_bit_cast(s16x4, b), c, 0, 0, 0);
}
template <int V> struct ic { static constexpr int v = V; };
template <class T, T... I> struct iseq {
  template <class F> static __device__ __forceinline__ void run(F&& f) { (f(ic<I>{}), ...); }
};
template <int N, class F> __device__ __forceinline__ void sfor(F&& f) { __make_integer_seq<iseq, int, N>::run(f); }
constexpr int wcnt(int n) { return n < 0 ? W0 : (W0 + n * WN / WD_ + 1 < 32 - ND ? W0 + n * WN / WD_ + 1 : 32 - ND); }
constexpr int WC = wcnt(28);
constexpr int every(int g, int a, int d, int n) { return g >= a && (g - a) % d == 0 && (g - a) / d < n ? (g - a) / d : -1; }
constexpr int G_T[4] = {0, GT1, GT2, GT3}, D_T[4] = {1, DT1, 1, DT3};
constexpr int G_V[4] = {0, GV1, GV2, GV3}, G_W[4] = {0, GW1, 60, 84};
static_assert(GT2 > GW1 && GT3 > GW1 && GW1 <= 28 && GW1 >= GV1 + 4, "order");
constexpr int op_tile(int g) { for (int s = 1 + !!T1H; s < 4; ++s) if (every(g, G_T[s], D_T[s], 5) >= 0) return 8 * s + every(g, G_T[s], D_T[s], 5); return -1; }
constexpr int op_wa(int g) {
  if (op_tile(g) >= 0) return -1;
  int n = 0;
  for (int i = 0; i < g; ++i) n += op_tile(i) < 0;
  return n < ND ? 32 - ND + n : -1;
}
constexpr int wa_at(int j) { for (int g = 0; g < 32; ++g) if (op_wa(g) == j) return g; return -1; }
constexpr int op_mark(int g) { return every(g, GMK, DMK, 4); }
constexpr int G_FLAG = GFL, G_FH = GFH;
constexpr int op_wd(int g) {
  if (every(g, 55, 2, 3) >= 0) return every(g, 55, 2, 3);
  if (every(g, 69, 2, 7) >= 0) return 3 + every(g, 69, 2, 7);
  if (every(g, 97, 1, 7) >= 0) return 21 + every(g, 97, 1, 7);
  return -1;
}
constexpr int op_stage(int g) {
  if (every(g, 49, 2, 3) >= 0) return 10 + every(g, 49, 2, 3);
  if (every(g, 61, 2, 4) >= 0) return 13 + every(g, 61, 2, 4);
  if (every(g, 85, 2, 4) >= 0) return 17 + every(g, 85, 2, 4);
  return -1;
}
constexpr int op_h(int g) {
  if (every(g, 104, 2, 4) >= 0) return every(g, 104, 2, 4);
  if (every(g, 111, 2, 9) >= 0) return 4 + every(g, 111, 2, 9);
  return -1;
}
constexpr int op_back(int g) {
  if (every(g, 105, 2, 3) >= 0) return 10 + every(g, 105, 2, 3);
  if (every(g, 112, 2, 8) >= 0) return 13 + every(g, 112, 2, 8);
  return -1;
}
constexpr int NH = 13, G_H3 = GH3, D_H3 = DH3, D_H2 = DH2;
constexpr int G_R0 = 124, G_R1 = 126, G_S3 = 121;
constexpr int ES[4] = {1, ESA, ESA, ESB};
constexpr int hstep(int s) { return (EO + 3 + (26 + ES[s] - 1) / ES[s] + 1) & ~1; }
constexpr int nv(int g) {
  return (op_tile(g) >= 0) + (op_wa(g) >= 0) + (op_mark(g) >= 0) + (g == G_FLAG) + (g == G_FH) + (g == G_R0) + (g == G_R1) + (g > 32 && (g & 31) == hstep(g >> 5)) + (op_wd(g) >= 0) + (op_stage(g) >= 0) + (op_h(g) >= 0);
}
constexpr bool single() { for (int g = 0; g < 128; ++g) if (nv(g) > 1) return false; return true; }
static_assert(single(), "schedule");
constexpr int younger(int a, int b) { int n = 0; for (int g = a + 1; g < b; ++g) n += nv(g); return n; }
#define VMW(N) asm volatile("s_waitcnt vmcnt(%0)" :: "n"(N) : "memory")
#define VMWC(N) __builtin_amdgcn_s_waitcnt(0x0f70 | (N))
#define XP 1
#define TAIL (XP ? 3072 : 0)
#define RARE(c) (XP ? __builtin_expect(!!(c), 0) : (c))
#define PIN(x) asm volatile("" : "+v"(x))
template <class T> __device__ __forceinline__ void ost(GAS T* q, T v, int m) {
  if (m == 0) *q = v;
  else if (m == 1) __hip_atomic_store(q, v, __ATOMIC_RELAXED, __HIP_MEMORY_SCOPE_AGENT);
  else if (m == 2) __hip_atomic_store(q, v, __ATOMIC_RELAXED, __HIP_MEMORY_SCOPE_SYSTEM);
  else __builtin_nontemporal_store(v, q);
}
template <class T> __device__ __forceinline__ T gld(const void* base, unsigned off) { return *(const GAS T*)((const GAS unsigned char*)base + off); }

template <int C, unsigned N = 1> __device__ __forceinline__ void arrive(unsigned cnt) {
  asm volatile("s_mov_b64 exec, 1\n\tds_add_u32 %0, %1 offset:%2\n\ts_mov_b64 exec, -1"
               :: "v"(cnt), "v"(N), "n"(4 * C) : "memory");
  __builtin_amdgcn_sched_barrier(0);
}
template <int C> __device__ __forceinline__ unsigned peek(lptr L) { return *(volatile LAS unsigned*)(L + L_CNT + 4 * C); }
__device__ __forceinline__ bool below(unsigned v, unsigned n) { return (unsigned)__builtin_amdgcn_readfirstlane(v) < n; }
template <int C> __device__ __forceinline__ void await(lptr L, unsigned n) {
  if (below(peek<C>(L), n)) {
    int it = 0;
    do __builtin_amdgcn_s_sleep(1); while (below(peek<C>(L), n) && ++it < (1 << 22));
  }
  asm volatile("" ::: "memory");
}
__device__ __forceinline__ unsigned neq3(unsigned a, unsigned b, unsigned t) { return __builtin_amdgcn_bitop3_b32(a, b, t, 0x7e); }
__device__ __forceinline__ bool anylane(bool c) { return __builtin_amdgcn_ballot_w64(c) != 0; }

__device__ __forceinline__ void tabaddr(unsigned c, unsigned base, unsigned& lo, unsigned& hi) {
  asm volatile("v_mad_u32_u16 %0, %1, 2, %2" : "=v"(lo) : "v"(c), "s"(base));
  asm volatile("v_mad_u32_u16 %0, %1, 2, %2 op_sel:[1,0,0,0]" : "=v"(hi) : "v"(c), "s"(base));
}
__device__ __forceinline__ f32x2 cmul(f32x2 a, f32x2 b) {
  return __builtin_elementwise_fma(f32x2{a.y, a.y}, f32x2{-b.y, b.x}, f32x2{a.x, a.x} * b);
}
__device__ __forceinline__ f32x2 cexp2(f32x2 z, float p) {
  const f32x2 v = z * p;
  const float f = __builtin_amdgcn_fractf(v.y);
  return f32x2{__builtin_amdgcn_cosf(f), __builtin_amdgcn_sinf(f)} * __builtin_amdgcn_exp2f(v.x);
}
__device__ __forceinline__ void put(lptr t, int row, f32x2 v) { *(volatile LAS unsigned*)(t + row * T_ROW) = pkh(v.x, v.y); }
__device__ __forceinline__ void putc(lptr t, int row, f32x2 v) { *(volatile LAS unsigned*)(t + row * T_ROW) = pkh(v.x, -v.y); }
template <int C> __device__ __forceinline__ unsigned dppmax(unsigned v) {
  return umax(v, (unsigned)__builtin_amdgcn_update_dpp(0, (int)v, C, 0xf, 0xf, true));
}

#define TCLK "s_memtime"
#define TS(i) do {} while (0)
#define TR(i) do {} while (0)
#define TQ(v) do {} while (0)
#define TW(v, i) do {} while (0)

__global__ void __launch_bounds__(512, 2) fused_kernel(Params p) {
  extern __shared__ __attribute__((aligned(16))) unsigned char smem[];
  lptr L = (lptr)smem;
  const unsigned lds0 = (unsigned)(uintptr_t)L;
  const int tid = threadIdx.x, l = tid & 63;
  int w = __builtin_amdgcn_readfirstlane(tid >> 6), bid = blockIdx.x;
  int cp = 2 * ((bid >> 4) * 8 + (bid & 7)) + ((bid >> 3) & 1);
  const int g4 = l >> 4, q4 = (l >> 2) & 3, p4 = l & 3, n16 = l & 15;
  const int l16 = 16 * l;
  TS(0);
  auto pull = [&]() {
  if (w < 2) {
    u64 pc; unsigned len;
    asm volatile("s_getpc_b64 %0\n.Lcode_here:\n\ts_mov_b32 %1, .Lcode_end-.Lcode_here+%2" : "=s"(pc), "=s"(len) : "n"(TAIL));
    const unsigned o = (unsigned)(((bid >> 3) + 32 * w) * 1024 + l16);
    glds1((const void*)((pc & ~15ull) + (o < len ? o : 0u)), lds0 + L_DUMP);
  }
  };
  if (!(XB & 248)) pull();
  FENCE();
  v4u sent4 = {SENT, SENT, SENT, SENT};
  int hto = (l >> 3) * 32768 + ((l & 7) ^ (cp & 4)) * 16 + WS_HT + w * 8 * 32768 + cp * 128;
  int xro = (32 * (bid >> 2) + (bid & 3) + 4 * w) * 1024 + l16, mko = w < M0 ? hto : OOB, pro = (2 * cp + (w >> 2)) * 256 + 4 * l;
  lptr xcw, xcr, xcs, xct;
  int xto, xto2, w1o, w1o2;
  unsigned w1d;
  auto adrx = [&]() {
    xcw = L + L_XC + w * XC_ROW + l16; xcr = L + L_XC + (l & 7) * XC_ROW + 128 * w + 16 * (l >> 3);
    xto = (l >> 3) * (2 * XT_Q) + (l & 7) * 16 + WS_XT + 16 * w * XT_Q + ((4 * (bid >> 5) + (bid & 3)) * 8 + ((bid >> 2) & 7)) * 128;
    xcs = L + L_XC + w * XC_ROW + 1024; xct = L + L_XC + (l & 7) * XC_ROW + 1024;
    xto2 = xto + XT_Q;
    asm volatile("" : "+v"(xcw), "+v"(xcr), "+v"(xto), "+v"(xcs), "+v"(xct), "+v"(xto2));
  };
  auto adrw = [&]() {
    const int rbid = bid < 224 ? bid : 0;
    w1o = (64 * (rbid & 7) + 16 * (w >> 1) + (l >> 3)) * (FF * 2) + 128 * (rbid >> 3) + 16 * (l & 7);
    w1o2 = w1o + 8 * FF * 2;
    w1d = lds0 + L_RT + (w & 1) * 8192 + (w >> 1) * 2048;
    asm volatile("" : "+v"(w1o), "+v"(w1o2), "+s"(w1d));
  };
  v4u sent0 = sent4;
  if (KS) asm volatile("" : "+v"(xro), "+v"(hto), "+v"(mko), "+v"(pro), "+v"(sent0));
  if (KS & 2) adrw();
  if (KS & 4) adrx();
  FENCE();
  asm volatile("" :: "s"(p.x), "s"(p.ws));
  rsrc_t rs = __builtin_amdgcn_make_buffer_rsrc((void*)p.ws, (short)0, WS_BYTES, 0x00020000);

  TS(32);
  v4u xrow;
  asm volatile("global_load_dwordx4 %0, %1, %2" NTXS : "=v"(xrow) : "v"(xro), "s"(p.x));
  unsigned ep = *(const CAS unsigned*)(p.ws + WS_EPOCH);
  FENCE();
  if (XB & 1) asm volatile("s_barrier" ::: "memory");
  if (XB & 8) pull();
  FENCE();
  st16(sent0, rs, mko, 0);
  FENCE();
  if (XB & 2) asm volatile("s_barrier" ::: "memory");
  FENCE();
  unsigned pA, pB, pCr, pCi;
  asm volatile("global_load_dword %0, %4, %5" NTPS "\n\tglobal_load_dword %1, %4, %6" NTPS "\n\tglobal_load_dword %2, %4, %7" NTPS "\n\tglobal_load_dword %3, %4, %8" NTPS
               : "=&v"(pA), "=&v"(pB), "=&v"(pCr), "=&v"(pCi) : "v"(pro), "s"(p.logA), "s"(p.aim), "s"(p.cre), "s"(p.cim));
  FENCE();
  if (XB & 16) pull();
  FENCE();
  if (OPQ) asm volatile("" : "+s"(w), "+s"(bid), "+s"(cp));
  __builtin_assume(w >= 0 && w < 8); __builtin_assume(bid >= 0 && bid < 256); __builtin_assume(cp >= 0 && cp < 256);
  const int xcd = bid & 7, idx = bid >> 3, cq = cp >> 1, cw = w >> 2, rg = w & 3;
  unsigned pdt = *(const CAS unsigned*)(p.logdt + 2 * cp);
  unsigned pds = *(const CAS unsigned*)(p.dskip + 2 * cp);
  FENCE();
  const int rb = (xcd & 1) * 8 + (idx >> 2), cb = (xcd >> 1) * 4 + (idx & 3);
  const int fgW = 7 * cb + (w > 0 ? w - 1 : 0);
  const int role = cw ? 3 - rg : rg;
  const bool retile = bid < 224;
  const int rbid = retile ? bid : 0, cg = rbid >> 3;
  int wdr = (16 * (rbid >> 1) + 8 * (l >> 5) + 4 * (rbid & 1) + ((l >> 3) & 3)) * (DM * 2) + 128 * w + 16 * (l & 7);
  auto rawwd = [&]() {
    unsigned sv;
    asm volatile("s_mov_b32 %0, m0\n\ts_mov_b32 m0, %3\n\ts_nop 0\n\tglobal_load_lds_dwordx4 %1, %2" NTWS "\n\ts_mov_b32 m0, %0"
                 : "=&s"(sv) : "v"(wdr), "s"(p.wd), "s"(lds0 + L_RD + w * 1024) : "memory");
  };
  auto rawdma = [&]() {
    if (!(KS & 2) && !RL) adrw();
    unsigned sv;
    asm volatile("s_mov_b32 %0, m0\n\ts_mov_b32 m0, %4\n\ts_nop 0\n\tglobal_load_lds_dwordx4 %1, %3" NTWS "\n\ts_add_u32 m0, m0, 1024\n\ts_nop 0\n\t"
                 "global_load_lds_dwordx4 %2, %3" NTWS "\n\ts_mov_b32 m0, %0"
                 : "=&s"(sv) : "v"(w1o), "v"(w1o2), "s"((w & 1) ? p.wu : p.wg), "s"(w1d) : "memory", "scc");
  };
  const float Z0 = asf(0x3f7f0000u), ZN = asf(0x3f7e0000u);
  unsigned eo[12], kco[4], upsel = (cp & 1) ? 0x07060504u : 0x03020100u;
  lptr rta, ta, tb, kA, uB, redw, redr, uw, wda, kx;
  int rto, wvo, wds, hfo;
  int gxs = WS_XT + cq * XT_Q + 4096 * w, f2s = WS_F2 + cg * 8192 + (bid & 7) * 256 + (w & 1) * 128, f2l = WS_F2 + fgW * 2048;
  auto slot = [&](auto N) __attribute__((always_inline)) {
    constexpr int n = N.v;
    if constexpr (P_IN == n || P_EO == n || P_KC == n || P_RT == n || P_A0 == n || P_A1 == n || P_A2 == n || P_A3 == n || P_A4 == n || P_SC == n) {
    SBAR();
    int t = tid;
    if (n > 1) asm volatile("" : "+v"(t));
    const int l = t & 63, g4 = l >> 4, q4 = (l >> 2) & 3, p4 = l & 3, n16 = l & 15;
    if constexpr (P_IN == n) {
      const v4u z = {0u, 0u, 0u, 0u};
      *(LAS v4u*)(L + L_CNT + 16 * (n > 1 ? (t < 135 ? t : 135) : (t & 255))) = z;
      const int za = (cw ? L_KX1 : L_KX0) + 4096 + rg * KX_CP + p4 * 16;
      const int zb = ((rg & 1) ? L_U - 512 + cw * U_CH : L_KC + cw * KC_CH) + (l16 & ((rg & 1) ? 0x3f0 : 0));
      *(LAS v4u*)(L + (l < 32 ? za : zb)) = z;
    }
    const unsigned kcb = lds0 + L_KC + cw * KC_CH - 2 * KC0;
    if constexpr (P_EO == n) {
      float lf = (float)(2047 - 8 * (t & 255));
      asm("" : "+v"(lf));
#pragma unroll
      for (int j = 0; j < 12; j += 2) {
        float a = lf - (float)j, b = lf - (float)(j + 1);
        if (j == 6) b = fmaxf(b, Z0);
        if (j >= 8) { a = fmaxf(a, ZN); b = j < 10 ? fmaxf(b, ZN) : a; }
        tabaddr(pkbf_s(a, b), kcb, eo[j], eo[j + 1]);
      }
    }
    if constexpr (P_KC == n) {
      const u64 pb = role == 0 ? 0x0084008000040000ull : (role == 1 ? 0x0300020001020100ull : 0x0a01080106010401ull);
      const float d1 = role == 0 ? 1.f : (role == 1 ? 4.f : 128.f), d2 = role == 0 ? 2.f : (role == 1 ? 128.f : 256.f);
      const float p0 = (float)(((unsigned)(pb >> (l & 48)) & 0xffffu) + 8 * n16), p2 = p0 + d2;
      tabaddr(pkbf_s(fmaxf(p0, Z0), p0 + d1), kcb, kco[0], kco[1]);
      tabaddr(pkbf_s(p2, p2 + d1), kcb, kco[2], kco[3]);
    }
    if constexpr (P_RT == n) {
      rta = L + L_RT + (w & 1) * 8192 + (w >> 1) * 2048 + (8 * (g4 >> 1) + q4) * 128 + 32 * (g4 & 1) + 8 * p4;
      rto = (retile ? (g4 & 1) * 32768 + ((g4 >> 1) * 32 + n16) * 16 : OOB) + WS_W1F + (4 * cg * 32 + 4 * (rbid & 7) + (w >> 1)) * 1024 + (w & 1) * 256;
      asm volatile("" : "+v"(rta), "+v"(rto));
    }
    if constexpr (P_A0 == n) {
      ta = L + L_T + cw * T_CH + (16 * role + n16) * T_ROW + 16 * g4; tb = L + L_T + cw * T_CH + (T_B + n16) * T_ROW + 16 * g4;
      if (role == 3) tb = L + L_HX + 2048 + (w & 1) * 256 + 4 * l;
      asm volatile("" : "+v"(ta), "+v"(tb));
    }
    if constexpr (P_A1 == n) {
      kA = L + (cw ? L_KX1 : L_KX0) + (3 - (n16 & 3)) * KX_CP + 2 * (12 - 4 * (n16 >> 2) + 8 * g4) + 64 * (6 - 2 * rg);
      uB = L + L_U + cw * U_CH + 32 * n16 + 16 * g4 + 384 - 128 * rg;
      redw = L + L_RED + w * 1024 + 16 * (4 * n16 + (g4 ^ ((n16 >> 1) & 3)));
      redr = L + L_RED + w * 8192 + 16 * ((l & ~3) + (p4 ^ ((l >> 3) & 3)));
      asm volatile("" : "+v"(kA), "+v"(uB), "+v"(redw), "+v"(redr));
    }
    if constexpr (P_A2 == n) {
      const int o = w > 0 ? 16 * l : OOB;
      wvo = o + WS_W1F + fgW * 32768;
      hfo = o + WS_HF + (rb * 112 + fgW) * 1024;
      asm volatile("" : "+v"(wvo), "+v"(hfo));
    }
    if constexpr (P_A3 == n) {
      wds = (retile && !(KO & 512) ? g4 * 1024 + n16 * 16 : OOB) + WS_WDF + (w * 56 + (rbid >> 2)) * 4096 + (rbid & 3) * 256;
      uw = L + L_U + 480 + 512 * w + 8 * l;
      wda = L + L_RD + w * 1024 + q4 * 128 + 32 * g4 + 8 * p4;
      asm volatile("" : "+v"(wds), "+v"(uw), "+v"(wda));
    }
    if constexpr (P_A4 == n) {
      kx = L + (cw ? L_KX1 : L_KX0) + 16 * (t & 255);
      asm volatile("" : "+v"(kx));
    }
    if constexpr (P_SC == n) asm volatile("" : "+s"(gxs), "+s"(f2s), "+s"(f2l), "+s"(upsel));
    SBAR();
    }
  };
  slot(ic<0>{});
  if (!(KS & 4) && !RDE) adrx();
  FENCE();
  if (XB & 4) asm volatile("s_barrier" ::: "memory");
  FENCE();
  if (XB & 32) pull();
  FENCE();
  if (RL == 0) rawdma(); else adrw();
  FENCE();
  if (!(KS & 4) && RDE) adrx();
  slot(ic<1>{});
  TS(1);
  asm volatile("s_waitcnt vmcnt(%1)" : "+v"(xrow) : "n"(RL ? 5 : 7));
  FENCE();
  if (RL == 1) rawdma();
  FENCE();
  PRIO(3);
  TS(33);
  {
    float s = dot2acc(xrow.x, ONES, 0.f), q = dot2acc(xrow.x, xrow.x, 0.f);
    s = dot2acc(xrow.y, ONES, s); q = dot2acc(xrow.y, xrow.y, q);
    s = dot2acc(xrow.z, ONES, s); q = dot2acc(xrow.z, xrow.z, q);
    s = dot2acc(xrow.w, ONES, s); q = dot2acc(xrow.w, xrow.w, q);
    f32x2 xf[4] = {{bflo(xrow.x), bfhi(xrow.x)}, {bflo(xrow.y), bfhi(xrow.y)}, {bflo(xrow.z), bfhi(xrow.z)}, {bflo(xrow.w), bfhi(xrow.w)}};
    float v = fold32(s, q);
    v += dppf<0xB1>(v); v += dppf<0x4E>(v); v += dppf<0x141>(v); v += dppf<0x140>(v);
    v = x16_add(v) * (1.f / DM);
    const auto sw = __builtin_amdgcn_permlane32_swap(asu(v), asu(v), false, false);
    const float mean = asf(sw[0]);
    const float var = rbf(fmaxf(asf(sw[1]) - mean * mean, 0.f));
    unsigned pk = pkbf(mean, __builtin_amdgcn_sqrtf(rbf(var + 1e-5f)));
    asm("" : "+v"(pk));
    const float rsd = __builtin_amdgcn_rcpf(bfhi(pk));
    f32x2 rn = {rsd, -bflo(pk) * rsd};
    asm("" : "+v"(rn));
    unsigned us[4];
#pragma unroll
    for (int i = 0; i < 4; ++i) {
      const f32x2 u = __builtin_elementwise_fma(xf[i], f32x2{rn.x, rn.x}, f32x2{rn.y, rn.y});
      us[i] = pkh(u.x, u.y);
    }
    *(LAS v4u*)(xcw) = v4u{us[0], us[1], us[2], us[3]};
    *(LAS unsigned*)(xcs) = pk;
  }
  FENCE();
  if (RL == 2) rawdma();
  if (XB & 128) pull();
  FENCE();
  TS(34);
  VMW(RL == 3 ? 4 : 6);
  TS(35);
  BAR();
  asm volatile("" : "+s"(ep));
  const unsigned tag = ep + 1u;
  TS(36);
  {
    const v2u ua = *(volatile LAS v2u*)(xcr), ub = *(volatile LAS v2u*)(xcr + 8);
    const unsigned sa = *(volatile LAS unsigned*)(xct), sb = *(volatile LAS unsigned*)(xct);
    st16(v4u{ua.x, ua.y, sa, tag}, rs, xto, 0);
    st16(v4u{ub.x, ub.y, sb, tag}, rs, xto2, 0);
  }
  FENCE();
  if (RL == 3) rawdma();
  FENCE();
  TS(2);
  PRIO(0);
  slot(ic<2>{});
  asm volatile("s_waitcnt vmcnt(%6)" : "+v"(pA), "+v"(pB), "+v"(pCr), "+v"(pCi), "+s"(pdt), "+s"(pds) : "n"(4));
  TS(44);
  v4u rf[2];
  auto retile_a = [&]() {
    FENCE();
    VMW(RL == 3 ? 0 : 2);
    TS(58);
    rf[0] = cat(trd(rta), trd(rta + 512)); rf[1] = cat(trd(rta + 64), trd(rta + 576));
    FENCE();
  };
  auto retile_b = [&]() {
    FENCE();
    st16(rf[0], rs, rto, 0); st16(rf[1], rs, rto, 65536);
    if (M0 < 4 || !MP) st16(sent4, rs, (w < M0 || (MP && cw) || (KO & 128)) ? OOB : hto, 0);
    FENCE(); if (KO & 256) st4(0u, rs, OOB, 0); else rawwd(); FENCE();
    float c[2];
#pragma unroll
    for (int i = 0; i < 2; ++i) c[i] = dot2acc(rf[i].w, ONES, dot2acc(rf[i].z, ONES, dot2acc(rf[i].y, ONES, dot2acc(rf[i].x, ONES, 0.f))));
    *(LAS float*)(L + L_HX + 2048 + 4 * tid) = fold32(c[0], c[1]);
    FENCE();
  };
  {
    const float LOG2E = 1.4426950408889634f, INV2PI = 0.15915494309189535f;
    if (RA) retile_a();
    const unsigned sel = (rg & 1) ? 0x03020c0cu : 0x01000c0cu;
#define HALF(v) asf(__builtin_amdgcn_perm(0u, v, sel))
    const float dt = rbf(__builtin_amdgcn_exp2f((cw ? bfhi(pdt) : bflo(pdt)) * LOG2E));
    const f32x2 z = f32x2{rbf(__builtin_amdgcn_exp2f(HALF(pA) * LOG2E)), HALF(pB)} * dt * f32x2{-LOG2E, INV2PI};
    lptr t = L + L_T + cw * T_CH + 256 * (rg & 1) + 4 * l;
    if (((rg >> 1) ^ cw) == 0) {
      f32x2 e[8];
      e[1] = cexp2(z, 1.f);
      const f32x2 e128 = cexp2(z, 128.f);
      if (RA == 1) retile_b();
      put(t, 0, f32x2{1.f, 0.f}); put(t, 8, e128);
      const f32x2 e256 = cmul(e128, e128);
      e[2] = cmul(e[1], e[1]);
      const f32x2 e384 = cmul(e256, e128), e512 = cmul(e256, e256);
      e[3] = cmul(e[2], e[1]); e[4] = cmul(e[2], e[2]);
      const f32x2 e640 = cmul(e512, e128), e768 = cmul(e512, e256), e896 = cmul(e512, e384);
      e[5] = cmul(e[4], e[1]); e[6] = cmul(e[4], e[2]); e[7] = cmul(e[4], e[3]);
#pragma unroll
      for (int i = 1; i < 8; ++i) {
        put(t, i, e[i]); put(t, 8 + i, cmul(e[i], e128));
        if (!RA && i == RI) retile_a();
        if (RA == 0 ? i == (RI + 3 < 7 ? RI + 3 : 7) : (RA == 2 && i == 1)) retile_b();
      }
      put(t, 16, e256); put(t, 17, cmul(e256, e[4])); put(t, 18, e384); put(t, 19, cmul(e384, e[4]));
      put(t, 20, cmul(e256, e[2])); put(t, 21, cmul(e256, e[6])); put(t, 22, cmul(e384, e[2])); put(t, 23, cmul(e384, e[6]));
      put(t, 24, e512); put(t, 25, cmul(e512, e[4])); put(t, 26, e640); put(t, 27, cmul(e640, e[4]));
      put(t, 28, e768); put(t, 29, cmul(e768, e[4])); put(t, 30, e896); put(t, 31, cmul(e896, e[4]));
    } else {
      unsigned m = umax(umax(pCr << 1, pCr << 17), umax(pCi << 1, pCi << 17));
      m = dppmax<0xB1>(m); m = dppmax<0x4E>(m); m = dppmax<0x141>(m); m = dppmax<0x140>(m);
      m = dppmax<0x142>(m); m = dppmax<0x143>(m);
      if (RA == 1) retile_b();
      const f32x2 e8 = cexp2(z, 8.f), e64 = cexp2(z, 64.f);
      f32x2 a = cexp2(z, 1024.f);
      const f32x2 e128 = cmul(e64, e64), e16 = cmul(e8, e8), e256 = cmul(e128, e128);
      int ec = (int)((unsigned)__builtin_amdgcn_readlane((int)m, 63) >> 24);
      ec = ec < 5 ? 5 : ec;
      const unsigned db = (unsigned)__builtin_amdgcn_readfirstlane((int)asu(dt));
      int ei = ec + (int)(db >> 23) - 131;
      ei = ei < 0 ? 0 : (ei > 254 ? 254 : ei);
      *(LAS unsigned*)(L + L_INV + 4 * cw) = (unsigned)ei << 23;
      f32x2 v = f32x2{HALF(pCr), HALF(pCi)} * asf((db & 0x007fffffu) | ((unsigned)(259 - ec) << 23));
      f32x2 v1 = cmul(v, e8), a1 = cmul(a, e128);
      if (!RA && RJ < 0) retile_a();
      if (RA == 2) retile_b();
#pragma unroll
      for (int j = 0; j < 4; ++j) {
        putc(t, T_B + 2 * j, v); putc(t, T_B + 2 * j + 1, v1);
        putc(t, T_B + 8 + 2 * j, cmul(v, e64)); putc(t, T_B + 9 + 2 * j, cmul(v1, e64));
        put(t, 32 + 2 * j, a); put(t, 33 + 2 * j, a1);
        a = cmul(a, e256);
        if (j < 3) { v = cmul(v, e16); v1 = cmul(v1, e16); a1 = cmul(a1, e256); }
        if (!RA && j == RJ + 1) retile_b();
        if (!RA && j == RJ) retile_a();
      }
      put(t, 40, a);
    }
  }
  FENCE();
  TS(41);
  slot(ic<3>{});
  if (FE) VMW(1 + (M0 < 4 || !MP));
  TS(4);
  BAR();
  TS(38);
  float csum;
  int f2o = OOB;
  if (role < 3) {
    v4u fr[8], fb[8];
#pragma unroll
    for (int ks = 0; ks < 8; ++ks) { fr[ks] = *(LAS v4u*)(ta + 64 * ks); fb[ks] = *(LAS v4u*)(tb + 64 * ks); }
    SBAR();
    slot(ic<4>{});
    f32x4 acc = {0.f, 0.f, 0.f, 0.f};
#pragma unroll
    for (int ks = 0; ks < 8; ++ks) acc = mfma16h(fr[ks], fb[ks], acc);
#pragma unroll
    for (int r = 0; r < 4; ++r) *(LAS _Float16*)(uintptr_t)kco[r] = (_Float16)acc[r];
    csum = 0.f;
  } else {
    csum = (*(LAS float*)(tb) + *(LAS float*)(tb + 512)) + (*(LAS float*)(tb + 1024) + *(LAS float*)(tb + 1536));
    if (retile) f2o = g4 * 2048 + n16 * 8;
    slot(ic<4>{});
  }
  FENCE();
#define F2ST() st8(v2u{asu(csum), tag}, rs, f2o, f2s)
  if (FE && role == 3) F2ST();
  FENCE();
  TS(39);
  v4u gx[4];
  asm volatile("buffer_load_dwordx4 %0, %4, %5, %6 offen sc1\n\tbuffer_load_dwordx4 %1, %4, %5, %6 offen offset:1024 sc1\n\t"
               "buffer_load_dwordx4 %2, %4, %5, %6 offen offset:2048 sc1\n\tbuffer_load_dwordx4 %3, %4, %5, %6 offen offset:3072 sc1"
               : "=&v"(gx[0]), "=&v"(gx[1]), "=&v"(gx[2]), "=&v"(gx[3]) : "v"(l16), "s"(rs), "s"(gxs) : "memory");
  FENCE();
  TS(40);
  slot(ic<5>{});
  if (!FE) VMW(5 + (M0 < 4 || !MP));
  TS(37);
  BAR();
  if (!FE) F2ST();
  FENCE();
  TS(5);
  {
    unsigned P[6], Q[5];
    _Float16 ee[11];
#pragma unroll
    for (int j = 0; j < 11; ++j) ee[j] = *(LAS _Float16*)(uintptr_t)eo[j];
    slot(ic<6>{});
#pragma unroll
    for (int j = 0; j < 6; ++j) {
      const f16x2 e = {ee[2 * j], j < 5 ? ee[j < 5 ? 2 * j + 1 : 0] : (_Float16)0.f};
      P[j] = __builtin_bit_cast(unsigned, e);
    }
#pragma unroll
    for (int j = 0; j < 5; ++j) Q[j] = __builtin_amdgcn_alignbit(P[j + 1], P[j], 16);
    *(LAS v4u*)(kx) = v4u{P[0], P[1], P[2], P[3]};
    *(LAS v4u*)(kx + KX_CP) = v4u{Q[0], Q[1], Q[2], Q[3]};
    *(LAS v4u*)(kx + 2 * KX_CP) = v4u{P[1], P[2], P[3], P[4]};
    *(LAS v4u*)(kx + 3 * KX_CP) = v4u{Q[1], Q[2], Q[3], Q[4]};
  }
#define WDF() cat(trd(wda), trd(wda + 512))
  slot(ic<7>{});
  FENCE();
  TS(6);
  asm volatile("s_waitcnt vmcnt(%4)" : "+v"(gx[0]), "+v"(gx[1]), "+v"(gx[2]), "+v"(gx[3]) : "n"(FE ? 0 : 1));
  TS(7);
  FENCE();
  v4u f2a, f2b;
#define F2LD() do { f2a = ld16(rs, l16, f2l); f2b = ld16(rs, l16 + 1024, f2l); } while (0)
  if (FL == 1) F2LD();
  FENCE();
  v4u wdf = WDF();
  FENCE();
#define GMISS() ((neq3(gx[0].w, gx[1].w, tag) | neq3(gx[2].w, gx[3].w, tag)) != 0u)
  if (RARE(anylane(GMISS()))) {
    int it = 0;
    do {
      asm volatile("" ::: "memory");
      if (GMISS()) {
#pragma unroll
        for (int i = 0; i < 4; ++i) gx[i] = ld16(rs, l16 + 1024 * i, gxs);
      }
    } while (anylane(GMISS()) && ++it < SPIN_CAP);
  }
  FENCE();
  TS(8);
  unsigned up[4];
  {
#pragma unroll
    for (int i = 0; i < 4; ++i) up[i] = __builtin_amdgcn_perm(gx[i].y, gx[i].x, upsel);
    *(LAS v2u*)(uw) = v2u{__builtin_amdgcn_perm(up[1], up[0], 0x05040100u), __builtin_amdgcn_perm(up[3], up[2], 0x05040100u)};
    *(LAS v2u*)(uw + U_CH) = v2u{__builtin_amdgcn_perm(up[1], up[0], 0x07060302u), __builtin_amdgcn_perm(up[3], up[2], 0x07060302u)};
  }
  FENCE();
  if (MP == 1 && cw) st16(sent4, rs, hto, 0);
  FENCE();
  if (MP != 3) st16(wdf, rs, wds, 0); else PIN(wdf);
  FENCE();
  const unsigned cnt = lds0 + L_CNT;
  if (FL == 2) F2LD();
  FENCE();
  slot(ic<8>{});
  if (CPR) PRIO(1);
  TS(42);
  BAR();
  TS(9); TR(61);
#define AFRAG(a, rr) cat(*(volatile LAS v2u*)(kA + 64 * (57 - 8 * (a) - (rr))), *(volatile LAS v2u*)(kA + 64 * (57 - 8 * (a) - (rr)) + 8))
#define BFRAG(b, rr) (*(LAS v4u*)(uB + 512 * (b) + 64 * (1 - (rr))))
  const int rot = (idx >> 2) * 4096;
  const int tso = WS_HT + rb * 32768;
  auto tile_dma = [&](int s, int c) {
    const int so = tso + s * (16 * 32768) + ((c * 4096) ^ rot);
    const unsigned d = lds0 + s * 32768 + c * 4096;
    dma<0>(rs, l16, so, d); dma<1024>(rs, l16, so, d); dma<2048>(rs, l16, so, d); dma<3072>(rs, l16, so, d);
  };
  v4u wa[32];
#define WAL(j) asm volatile("s_cmp_eq_u32 %3, %4\n\ts_cbranch_scc1 .Lwa%=\n\tbuffer_load_dwordx4 %0, %1, %2, %5 offen offset:%6 sc1\n.Lwa%=:" \
                            : "=v"(wa[j]) : "v"(wvo), "s"(rs), "s"(w), "n"(0), "s"((((j) >> 2) * 4096) ^ rot), "n"(((j) & 3) * 1024) : "scc", "memory")
  float hb0[4], hb1[4];
  auto conv_out = [&](int c) {
    const f32x2 invS = *(LAS f32x2*)(L + L_INV);
    f32x4 y[8];
    int it = 0;
    unsigned done;
    do {
      done = *(volatile LAS unsigned*)(L + L_CNT + 4 * c);
      FENCE();
#pragma unroll
      for (int v = 0; v < 8; ++v) y[v] = *(LAS f32x4*)(redr + 1024 * v);
      FENCE();
    } while (below(done, 8) && ++it < (1 << 22));
    float h0[4], h1[4];
#pragma unroll
    for (int i = 0; i < 4; ++i) {
      h0[i] = __builtin_fmaf(((y[0][i] + y[1][i]) + y[2][i]) + y[3][i], invS.x, hb0[i]);
      h1[i] = __builtin_fmaf(((y[4][i] + y[5][i]) + y[6][i]) + y[7][i], invS.y, hb1[i]);
    }
    st16(v4u{umin(pkbf_s(h0[0], h0[1]), SENT - 1), pkbf_s(h0[2], h0[3]), umin(pkbf_s(h1[0], h1[1]), SENT - 1), pkbf_s(h1[2], h1[3])}, rs, hto, 0);
  };
  {
    v4u A[2][8], B[2][8];
    f32x4 t[8];
#pragma unroll
    for (int i = 0; i < 8; ++i) t[i] = f32x4{0.f, 0.f, 0.f, 0.f};
#define MM(a, b, rr) t[(a) + (b)] = mfma16h(A[rr][a], B[rr][b], t[(a) + (b)])
#define MM2(a, b) do { MM(a, b, 0); MM(a, b, 1); } while (0)
#define LDA(a) do { A[0][a] = AFRAG(a, 0); A[1][a] = AFRAG(a, 1); } while (0)
#define LDB(b) do { B[0][b] = BFRAG(b, 0); B[1][b] = BFRAG(b, 1); } while (0)
    LDB(0); LDA(0); SBAR(); LDB(1); LDA(1); SBAR(); LDB(2); LDA(2); SBAR();
    if (MP == 3) {
      FENCE();
      if (FL == 3) F2LD();
      FENCE();
      if (cw) st16(sent4, rs, hto, 0);
      FENCE();
      st16(wdf, rs, wds, 0);
      FENCE();
    }
#pragma unroll
    for (int i = 0; i < 4; ++i) {
      const f16x2 u = __builtin_bit_cast(f16x2, up[i]);
      const float m = bflo(gx[i].z), sd = bfhi(gx[i].z);
      hb0[i] = __builtin_fmaf((float)u.x, sd + bflo(pds), m); hb1[i] = __builtin_fmaf((float)u.y, sd + bfhi(pds), m);
      asm volatile("" : "+v"(hb0[i]), "+v"(hb1[i]));
    }
    SBAR();
    if (FL == 0) F2LD();
    FENCE();
    if (MP == 2) st16(sent4, rs, cw ? hto : OOB, 0);
    FENCE();
#define FMISS() anylane((neq3(f2a.y, f2a.w, tag) | neq3(f2b.y, f2b.w, tag)) != 0u)
    auto f2_check = [&]() {
      FENCE();
      if (RARE(FMISS())) {
        int it = 0;
        do {
          asm volatile("" ::: "memory");
          F2LD();
        } while (FMISS() && ++it < SPIN_CAP);
      }
      FENCE();
    };
    constexpr int NE = FC == 2 ? 7 : 3;
#define WE(k) do { constexpr int k_ = (k) - (7 - NE); if constexpr (FC && k_ >= 0) sfor<(k_ + 1) * W0 / NE - k_ * W0 / NE>([&](auto J) __attribute__((always_inline)) { WAL(k_ * W0 / NE + J.v); SBAR(); }); } while (0)
    if (FC == 2) f2_check();
    WE(0);
    MM2(0, 0); SBAR();
    LDB(3); SBAR();
    WE(1);
    MM2(0, 1); SBAR();
    LDA(3); SBAR();
    WE(2);
    MM2(1, 0); SBAR();
    WE(3);
    st4(tag, rs, (bid == 0 && tid == 0) ? WS_EPOCH : OOB, 0);
    SBAR();
    MM2(1, 1); SBAR();
    *(LAS f32x4*)(redw) = t[0]; *(LAS f32x4*)(redw + 8192) = t[1];
    FENCE();
    arrive<C_S1>(cnt);
    if (CPR) PRIO(0);
    FENCE();
    if (FC == 1) f2_check();
    WE(4);
    MM2(0, 2); SBAR();
    WE(5);
    MM2(2, 0); SBAR();
    TS(10);
    if (w < 2) { if (CPR == 2) PRIO(1); conv_out(C_S1); if (CPR == 2) PRIO(0); }
    FENCE();
    MM2(1, 2); SBAR();
    WE(6);
    MM2(2, 1); SBAR();
    LDB(4); SBAR();
    TS(43);
    if (FC == 0) f2_check();
    TS(11);
#define WA(n) do { sfor<wcnt(n) - wcnt((n) - 1)>([&](auto J) __attribute__((always_inline)) { WAL(wcnt((n) - 1) + J.v); SBAR(); }); \
                   if ((n) == TDS) { FENCE(); if (cw == 0) { tile_dma(0, 2 * w); tile_dma(0, 2 * w + 1); } FENCE(); } } while (0)
    WA(0);
    MM2(0, 3); SBAR();
    WA(1);
    MM2(3, 0); SBAR();
    WA(2); LDA(4); SBAR();
    MM2(1, 3); SBAR();
    *(LAS f32x4*)(redw + 16384) = t[2]; *(LAS f32x4*)(redw + 24576) = t[3];
    FENCE();
    if (MP == 2) VMW(wcnt(2) + 1);
    if (FL) { if (w == 0) VMW(2); VMW(wcnt(2) + 1); }
    arrive<C_S2>(cnt);
    WA(3);
    MM2(2, 2); SBAR();
    WA(4);
    *(LAS float*)(L + L_CSW + w * 128 + 8 * n16 + 4 * (l >> 5)) = x16_add(fold32(asf(f2a.x) + asf(f2b.x), asf(f2a.z) + asf(f2b.z)));
    SBAR();
    MM2(3, 1); SBAR();
    WA(5);
    if (w == 2 || w == 3) conv_out(C_S2);
    FENCE();
    MM2(2, 3); SBAR();
    WA(6);
    MM2(3, 2); SBAR();
    WA(7);
    MM2(3, 3); SBAR();
    WA(8); LDB(5); SBAR();
    MM2(3, 4); SBAR();
    WA(9);
    MM2(2, 4); SBAR();
    WA(10); LDA(5); SBAR();
    MM2(1, 4); SBAR();
    WA(11);
    MM2(0, 4); SBAR();
    WA(12);
    MM2(4, 3); SBAR();
    WA(13); LDB(6); SBAR();
    MM2(4, 2); SBAR();
    WA(14);
    MM2(4, 1); SBAR();
    WA(15); LDA(6); SBAR();
    MM2(4, 0); SBAR();
    WA(16);
    MM2(2, 5); SBAR();
    *(LAS f32x4*)(redw + 32768) = t[4]; WA(17); LDB(7); SBAR();
    MM2(1, 5); SBAR();
    WA(18);
    MM2(0, 5); SBAR();
    WA(19); LDA(7); SBAR();
    MM2(5, 0); SBAR();
    WA(20);
    MM2(5, 1); SBAR();
    *(LAS f32x4*)(redw + 40960) = t[5]; WA(21);
    MM2(5, 2); SBAR();
    WA(22);
    MM2(0, 6); SBAR();
    WA(23);
    MM2(6, 0); SBAR();
    WA(24);
    MM2(1, 6); SBAR();
    *(LAS f32x4*)(redw + 49152) = t[6]; WA(25);
    MM2(6, 1); SBAR();
    WA(26);
    MM2(0, 7); SBAR();
    WA(27);
    MM2(7, 0); SBAR();
    WA(28);
    *(LAS f32x4*)(redw + 57344) = t[7];
  }
  FENCE();
  arrive<C_S3>(cnt);
  TS(12);
  auto tile_peek = [&](int s, int c) {
    lptr q = L + s * 32768 + c * 4096 + l16;
    return v4u{*(volatile LAS unsigned*)(q), *(volatile LAS unsigned*)(q + 1024), *(volatile LAS unsigned*)(q + 2048), *(volatile LAS unsigned*)(q + 3072)};
  };
#define TMISS(m) anylane(umax(umax(m.x, m.y), umax(m.z, m.w)) == SENT)
  auto tile_check = [&](v4u m, int s, int c) {
    if (RARE(TMISS(m))) {
      int it = 0;
      do {
        tile_dma(s, c);
        VMWAIT(0);
        m = tile_peek(s, c);
      } while (TMISS(m) && ++it < SPIN_CAP);
    }
    FENCE();
  };
  bool again = true;
  if (TDE && cw == 0) {
    if (w == 0) VMWAIT(0);
    VMW(WC - wcnt(TDS));
    FENCE();
    const v4u m0 = tile_peek(0, 2 * w), m1 = tile_peek(0, 2 * w + 1);
    again = anylane(umax(umax(umax(m0.x, m0.y), umax(m0.z, m0.w)), umax(umax(m1.x, m1.y), umax(m1.z, m1.w))) == SENT);
    FENCE();
  }
  if ((TDS > 28 || TDE) && cw == 0 && again) { tile_dma(0, 2 * w); tile_dma(0, 2 * w + 1); }
  if (CO1 && cw) conv_out(C_S3);
  FENCE();
  sfor<32 - ND - WC>([&](auto J) __attribute__((always_inline)) { WAL(WC + J.v); SBAR(); });
  v2u cA;
  {
    const float c = l < 32 ? *(LAS float*)(L + L_CSW + w * 128 + 4 * (l & 31)) : 0.f;
    const float hi = rbf(c), mid = rbf(c - hi);
    cA = v2u{pkbf(hi, mid), pkbf((c - hi) - mid, 0.f)};
    PIN(cA);
  }
  FENCE();
  if (cw) {
    if (!T04) arrive<C_T0>(cnt);
    if (!CO1) conv_out(C_S3);
    FENCE();
    TS(13);
  } else {
    TS(13);
    if (again) {
      if (w == 0) VMWAIT(0);
      VMW(32 - ND - wcnt(TDS > 28 || TDE ? 28 : TDS));
      FENCE();
      const v4u m0 = tile_peek(0, 2 * w), m1 = tile_peek(0, 2 * w + 1);
      tile_check(m0, 0, 2 * w); tile_check(m1, 0, 2 * w + 1);
    }
    if (T04) await<C_S3>(L, 8);
    arrive<C_T0, 1 + T04>(cnt);
    if (T1H == 3 && w == 1) { tile_dma(1, 0); tile_dma(1, 1); }
    if (T1H == 2 || (T1H == 3 && w > 0)) { tile_dma(1, 2 * w); tile_dma(1, 2 * w + 1); }
  }
  FENCE();
  TS(14); TR(59);
  const int g2r = xcd * 4 + (idx >> 3), db = idx & 7, tbo = g2r >> 1, th = g2r & 1;
  const int wdo = WS_WDF + (db * 56 + 7 * w) * 4096;
  const int hvo = (g4 >> 1) * 1024 + ((g4 & 1) * 32 + th * 16 + n16) * 16;
  const int hso = WS_HF + (tbo * 112 + 14 * w) * 1024;
  const int rch = 64 * db + 16 * rg + (l >> 2);
  const int hro = WS_HT + (16 * cw + tbo) * 32768 + (rch >> 1) * 128 + (((4 * th) ^ ((rch >> 1) & 4)) + p4) * 16 + (rch & 1) * 8;
#define WDSO(j) (wdo + ((j) >> 2) * 4096 + ((((j) & 3) + rg) & 3) * 1024)
#define WDLD(j) ld16(rs, (KO & 2) ? OOB : l16, WDSO(j))
#define SLOT(j) ((j) < 12 ? L_KX1 - 2048 + w * 2048 + ((j) - 10) * 1024 : ((j) == 12 ? L_XC - 1024 + w * 1024 : ((j) - 13) / 4 * 32768 + w * 4096 + ((j) - 13) % 4 * 1024))
#define HLD(s, ks) ld16(rs, (KO & 4) ? OOB : hvo, hso + (s) * HF_S + (ks) * 2048)
#define FHLD() ld4(rs, 4 * (n16 < 14 ? n16 : 13), WS_FH + tbo * 512 + 56 * w)
#define FHST() st4(tag, rs, l == 0 ? 0 : OOB, WS_FH + rb * 512 + 4 * fgW)
  v4u wd[28], hb[3][7];
  v2u hres[2];
  unsigned fh;
  auto fh_check = [&]() {
    if (RARE(anylane(fh != tag))) {
      int it = 0;
      do {
        asm volatile("" ::: "memory");
        fh = FHLD();
      } while (anylane(fh != tag) && ++it < SPIN_CAP);
    }
    FENCE();
  };
  auto tile_wait = [&](unsigned v, int s) {
    lptr c = L + L_CNT + 4 * (C_T0 + s);
    if (RARE(below(v, 8))) {
      int it = 0;
      do __builtin_amdgcn_s_sleep(1); while (below(*(volatile LAS unsigned*)c, 8) && ++it < (1 << 22));
    }
    FENCE();
  };
#define TCNT(s) (*(volatile LAS unsigned*)(L + L_CNT + 4 * (C_T0 + (s))))
  if (w > 0) {
    lptr bt = L + (4 * (g4 >> 1) + (q4 >> 1)) * 128 + (4 * ((g4 ^ (g4 >> 1)) & 1) + p4) * 16 + (q4 & 1) * 8, bt2 = bt + 65536;
    lptr stp = L + L_ST2 + 16 * (l & 31);
    PIN(bt); PIN(bt2); PIN(stp);
#define BLD(g) cat(trd(((g) < 64 ? bt : bt2) + ((g) & 63) * 1024), trd(((g) < 64 ? bt : bt2) + ((g) & 63) * 1024 + 256))
    PRIO(1);
    if ((PA & 1) && cw) PRIO(2);
    if (T1H == 1 || (T1H >= 2 && cw)) arrive<C_T1>(cnt);
    { await<C_T0>(L, 8); FENCE(); }
    TS(15);
    f32x16 acc[2];
    v4u bf[4];
    f32x4 st;
    float gt[2], gu[2], ge[2], go[2];
    unsigned ho[4], tc, t5;
#define DMA5(t) dma<0>(rs, l16, tso + (t) * (16 * 32768) + rot + rg * 1024, lds0 + (t) * 32768 + rg * 1024)
#define PEEK5(t) (*(volatile LAS unsigned*)(L + (t) * 32768 + rg * 1024 + l16))
    v4u tm, tm2;
    u64 tq[8];
    auto st_check = [&](int s) {
      if (RARE(anylane(st.w == 0.f))) {
        int it = 0;
        do {
          __builtin_amdgcn_s_sleep(1);
          st = *(volatile LAS f32x4*)(stp + s * 512);
        } while (anylane(st.w == 0.f) && ++it < (1 << 22));
      }
      FENCE();
      acc[s & 1] = mfma32k8(cA, v2u{asu(st.x), asu(st.x)}, acc[s & 1]);
    };
    auto epi = [&](int s, int j, int stage) {
      const float g0 = acc[s & 1][2 * j], g1 = acc[s & 1][2 * j + 1];
      if (stage == 0) { gt[0] = __builtin_fmaf(g0 * g0, st.z, st.y); gt[1] = __builtin_fmaf(g1 * g1, st.z, st.y); PIN(gt[0]); PIN(gt[1]); }
      if (stage == 1) { gt[0] *= g0; gt[1] *= g1; gu[0] = g0 * acc[s & 1][8 + 2 * j]; PIN(gt[0]); PIN(gt[1]); PIN(gu[0]); }
      if (stage == 2) { ge[0] = __builtin_amdgcn_exp2f(gt[0]); gu[1] = g1 * acc[s & 1][9 + 2 * j]; PIN(ge[0]); PIN(gu[1]); }
      if (stage == 3) { ge[1] = __builtin_amdgcn_exp2f(gt[1]); ge[0] = __builtin_fmaf(ge[0], st.w, st.w); PIN(ge[0]); PIN(ge[1]); }
      if (stage == 4) { ge[0] = __builtin_amdgcn_rcpf(ge[0]); ge[1] = __builtin_fmaf(ge[1], st.w, st.w); PIN(ge[0]); PIN(ge[1]); }
      if (stage == 5) { ge[1] = __builtin_amdgcn_rcpf(ge[1]); go[0] = gu[0] * ge[0]; PIN(go[0]); PIN(ge[1]); }
      if (stage == 6) { go[1] = gu[1] * ge[1]; PIN(go[1]); }
      if (stage == 7) { ho[j] = pkbf(go[0], go[1]); PIN(ho[j]); }
    };
#define HST(s) st16(v4u{umin(ho[0], SENT - 1), ho[1], ho[2], ho[3]}, rs, hfo, (s) * HF_S)
#pragma unroll
    for (int g = 0; g < 3; ++g) bf[g] = BLD(g);
    SBAR();
    sfor<128>([&](auto G) __attribute__((always_inline)) {
      constexpr int g = G.v, s = g >> 5, ks = g & 31;
      if constexpr (ks == 0) {
#pragma unroll
        for (int r = 0; r < 16; ++r) acc[s & 1][r] = 0.f;
      }
      if constexpr (g + 3 < 128) bf[(g + 3) & 3] = BLD(g + 3);
      if constexpr (!(KO & 16) && g < 32 - ND && (T1H || g <= G_V[1])) asm volatile("s_waitcnt vmcnt(%1)" : "+v"(wa[ks]) : "n"(31 - ND - g + younger(-1, g)));
      if constexpr (!(KO & 16) && g >= 32 - ND && g < 32) asm volatile("s_waitcnt vmcnt(%1)" : "+v"(wa[ks]) : "n"(younger(wa_at(g), g)));
      acc[s & 1] = mfma32b(wa[ks], bf[g & 3], acc[s & 1]);
      SBAR();
      if constexpr (s > 0) {
        constexpr int ke = ks - EO;
        if constexpr (ke == (EO ? -1 : 0)) st = *(volatile LAS f32x4*)(stp + (s - 1) * 512);
        if constexpr (ke == 1) st_check(s - 1);
        constexpr int n = ES[s];
        if constexpr (ke >= 3 && ke < hstep(s) - EO) {
          sfor<n>([&](auto R) __attribute__((always_inline)) {
            constexpr int e = (ke - 3) * n + R.v, j = e / 6, q = e % 6;
            if constexpr (q < 2 && j > 0 && j < 5) epi(s - 1, j - 1, 6 + q);
            if constexpr (j < 4) epi(s - 1, j, q);
          });
        }
        if constexpr (ks == hstep(s)) HST(s - 1);
      }
      constexpr int v = g < G_V[1] + 3 ? 1 : (g < G_V[2] + 3 ? 2 : 3), u = g <= G_W[1] ? 1 : (g <= G_W[2] ? 2 : 3);
      if constexpr (T1H == 3 && g == G_V[1] - 3) { if (w == 1) { VMW(younger(-1, g) + 8); tm = tile_peek(1, 0); tm2 = tile_peek(1, 1); } }
      if constexpr (T1H == 3 && g == G_V[1] - 1) { if (w == 1) { tile_check(tm, 1, 0); tile_check(tm2, 1, 1); } }
      if constexpr (T1H >= 2 && g == G_V[1]) { if (!cw) { VMW(younger(-1, g)); tm = tile_peek(1, 2 * w); tm2 = tile_peek(1, 2 * w + 1); } }
      if constexpr (T1H >= 2 && g == G_V[1] + 2) { if (!cw) { tile_check(tm, 1, 2 * w); tile_check(tm2, 1, 2 * w + 1); arrive<C_T1>(cnt); } }
      if constexpr (g == G_V[v] && v > !!T1H) { if (!(KO & 32)) VMW(younger(G_T[v] + 4 * D_T[v], g)); tm = tile_peek(v, w); t5 = PEEK5(v); }
      if constexpr (g == G_V[v] + 2 && v > !!T1H) {
        if (RARE(anylane(umax(umax(umax(tm.x, tm.y), umax(tm.z, tm.w)), t5) == SENT))) {
          int it = 0;
          do {
            tile_dma(v, w); DMA5(v);
            VMWAIT(0);
            tm = tile_peek(v, w); t5 = PEEK5(v);
          } while (anylane(umax(umax(umax(tm.x, tm.y), umax(tm.z, tm.w)), t5) == SENT) && ++it < SPIN_CAP);
        }
        FENCE();
        arrive<C_T0 + v>(cnt);
      }
      if constexpr (g == G_W[u] - 2) tc = TCNT(u);
      if constexpr (g == G_W[u]) tile_wait(tc, u);
      if constexpr (op_tile(g) >= 0) {
        constexpr int t = op_tile(g) >> 3;
        if constexpr ((op_tile(g) & 7) < 4) dma<(op_tile(g) & 3) * 1024>(rs, l16, tso + t * (16 * 32768) + ((w * 4096) ^ rot), lds0 + t * 32768 + w * 4096);
        else DMA5(t);
      }
      if constexpr (op_wa(g) >= 0) asm volatile("buffer_load_dwordx4 %0, %1, %2, %3 offen offset:%4 sc1"
                                                : "=v"(wa[op_wa(g)]) : "v"((KO & 64) ? OOB : wvo), "s"(rs), "s"(((op_wa(g) >> 2) * 4096) ^ rot), "n"((op_wa(g) & 3) * 1024) : "memory");
      if constexpr (op_mark(g) >= 0) st16(sent4, rs, (KO & 1) ? OOB : hfo, op_mark(g) * HF_S);
      if constexpr (g == G_FLAG) { VMW(younger(GMK + 3 * DMK, g)); FHST(); }
      if constexpr (op_wd(g) >= 0) wd[op_wd(g)] = WDLD(op_wd(g));
      if constexpr (op_stage(g) >= 0) dma<0>(rs, (KO & 2) ? OOB : l16, WDSO(op_stage(g)), lds0 + SLOT(op_stage(g)));
      if constexpr (g == G_FH) fh = FHLD();
      if constexpr (g == 103) fh_check();
      if constexpr (op_h(g) >= 0) hb[op_h(g) / 7][op_h(g) % 7] = HLD(op_h(g) / 7, op_h(g) % 7);
      if constexpr (g == 105) VMW(younger(91, g));
      if constexpr (op_back(g) >= 0) wd[op_back(g)] = *(LAS v4u*)(L + SLOT(op_back(g)) + l16);
      if constexpr (g == G_S3) st = *(volatile LAS f32x4*)(stp + 3 * 512);
      if constexpr (g == G_S3 + 2) st_check(3);
      if constexpr (g == G_R0) hres[0] = ld8(rs, hro, 0);
      if constexpr (g == G_R1) hres[1] = ld8(rs, hro, 32 * 32768);
      if constexpr (g == 95) arrive<C_M2>(cnt);
      if constexpr ((g & 3) == 3 && g < 127 && (((PA >> (g >> 2)) ^ (PA >> ((g + 1) >> 2))) & 1)) { if (cw) PRIO(((PA >> ((g + 1) >> 2)) & 1) ? 2 : 1); }
      if constexpr (ks == 31) TS(16 + 2 * s);
      if constexpr (g == 63) TR(62);
      if constexpr (s == LS && (ks & 3) == 3) TQ(tq[ks >> 2]);
      if constexpr (s == LS && ks == 31) { TW(tq[0], 50); TW(tq[1], 51); TW(tq[2], 52); TW(tq[3], 53); TW(tq[4], 54); TW(tq[5], 55); TW(tq[6], 56); TW(tq[7], 57); }
      FENCE();
    });
    arrive<C_M3>(cnt);
    {
      float t[8], u[8];
#define EP(x) _Pragma("unroll") for (int j = 0; j < 8; ++j) { x; } SBAR()
      EP(t[j] = acc[1][j] * acc[1][j]; PIN(t[j]));
      EP(t[j] = __builtin_fmaf(t[j], st.z, st.y); PIN(t[j]));
      EP(t[j] *= acc[1][j]; PIN(t[j]));
      EP(t[j] = __builtin_amdgcn_exp2f(t[j]); u[j] = acc[1][j] * acc[1][8 + j]; PIN(t[j]); PIN(u[j]));
      EP(t[j] = __builtin_fmaf(t[j], st.w, st.w); PIN(t[j]));
      EP(t[j] = __builtin_amdgcn_rcpf(t[j]); PIN(t[j]));
      EP(t[j] *= u[j]; PIN(t[j]));
#pragma unroll
      for (int j = 0; j < 4; ++j) ho[j] = pkbf(t[2 * j], t[2 * j + 1]);
      HST(3);
    }
    FENCE();
    TS(21);
  } else {
    lptr b0 = L + g4 * 512 + (q4 >> 1) * 128 + (g4 & 1) * 64 + p4 * 16 + (q4 & 1) * 8, b1 = L + g4 * 512 + (q4 >> 1) * 128 + ((g4 & 1) ^ 1) * 64 + p4 * 16 + (q4 & 1) * 8;
    v4u ones = {ONES, ONES, ONES, ONES};
    int sel = (l & 1) ? 0xeeeeeeee : 0x44444444;
    PIN(ones); PIN(sel);
    v2u f[4][8];
#define GLD(o, G, i) f[(G) & 3][i] = trd(((i) < 4 ? b0 : b1) + (o) + (G) * 4096 + ((i) & 2) * 1024 + ((i) & 1) * 256)
    if (HP) PRIO(HP);
    if (T1H == 1) {
#pragma unroll
      for (int c = 0; c < 8; ++c) tile_dma(1, c);
    } else if (!T1H || T1H == 3) arrive<C_T1>(cnt);
    { await<C_T0>(L, 8); FENCE(); }
    TS(15);
#pragma unroll
    for (int i = 0; i < 16; ++i) GLD(0, i >> 3, i & 7);
    FENCE();
#pragma unroll 1
    for (int s = 0; s < 4; ++s) {
      const int ot = s * 32768, on = ((s + 1) & 3) * 32768;
      f32x4 gq[2] = {{0.f, 0.f, 0.f, 0.f}, {0.f, 0.f, 0.f, 0.f}}, gs[2] = {{0.f, 0.f, 0.f, 0.f}, {0.f, 0.f, 0.f, 0.f}};
      unsigned tc;
#pragma unroll
      for (int G = 0; G < 8; ++G) {
        const v2u* e = f[G & 3];
#define SQ(h, j) do { const v4u t_ = cat(e[4 * (h) + 2 * (j)], e[4 * (h) + 2 * (j) + 1]); gq[h] = mfma16b(t_, t_, gq[h]); SBAR(); } while (0)
#define SM(h) do { const v8u t_ = {e[4 * (h)].x, e[4 * (h)].y, e[4 * (h) + 1].x, e[4 * (h) + 1].y, e[4 * (h) + 2].x, e[4 * (h) + 2].y, e[4 * (h) + 3].x, e[4 * (h) + 3].y}; \
        if (!(KO & 8)) gs[h] = __builtin_amdgcn_smfmac_f32_16x16x64_bf16(__builtin_bit_cast(b16x8, ones), __builtin_bit_cast(b16x16, t_), gs[h], sel, 0, 0); SBAR(); } while (0)
#define NX(i) do { if (G < 6) GLD(ot, G + 2, i); else GLD(on, G - 6, i); SBAR(); } while (0)
        SQ(0, 0); NX(0);
        SQ(1, 0); NX(1);
        SQ(0, 1); NX(4); NX(5);
        SM(0); NX(2);
        SQ(1, 1); NX(3);
        SM(1); NX(6); NX(7);
        if (T1H == 2 && G == HCK && s == 0) {
          VMWAIT(0);
          FENCE();
          const v4u m0 = tile_peek(1, 0), m1 = tile_peek(1, 1);
          tile_check(m0, 1, 0); tile_check(m1, 1, 1);
          arrive<C_T1>(cnt);
          TS(17);
        }
        if (T1H == 1 && G == HCK && s == 0) {
          int it = 0;
          for (;;) {
            VMWAIT(0);
            FENCE();
            unsigned m[32];
#pragma unroll
            for (int i = 0; i < 32; ++i) m[i] = *(LAS unsigned*)(L + 32768 + 1024 * i + l16);
#pragma unroll
            for (int i = 1; i < 32; ++i) m[0] = umax(m[0], m[i]);
            if (!RARE(anylane(m[0] == SENT)) || ++it >= SPIN_CAP) break;
            FENCE();
#pragma unroll
            for (int c = 0; c < 8; ++c) tile_dma(1, c);
          }
          FENCE();
          arrive<C_T1>(cnt);
          TS(17);
        }
        if (G == 5) { FENCE(); arrive<C_T2>(cnt + 4 * s); tc = TCNT((s + 1) & 3); FENCE(); }
        if ((G & 1) == 0 && s == HWS) {
          FENCE();
#pragma unroll
          for (int j = 0; j < 7; ++j) wd[7 * (G >> 1) + j] = WDLD(7 * (G >> 1) + j);
          FENCE();
        }
        SBAR();
      }
      const float C1 = -2.f * 0.7978845608028654f * 1.4426950408889634f, C2c = C1 * 0.044715f;
      float q[2];
#pragma unroll
      for (int hh = 0; hh < 2; ++hh) q[hh] = (p4 & 2) ? ((p4 & 1) ? gq[hh][3] : gq[hh][2]) : ((p4 & 1) ? gq[hh][1] : gq[hh][0]);
      const auto sw = __builtin_amdgcn_permlane32_swap(asu(q[1]), asu(q[1]), false, false);
      const bool first = q4 == g4;
      const float mean = (first ? gs[0][0] + gs[0][1] : gs[1][0] + gs[1][1]) * (1.f / DM);
      const float var = rbf(fmaxf((first ? q[0] : asf(l < 32 ? sw[1] : sw[0])) * (1.f / DM) - mean * mean, 0.f));
      const float sd = rbf(__builtin_amdgcn_sqrtf(rbf(var + 1e-5f)));
      const float r = __builtin_amdgcn_rcpf(sd);
      if (((q4 ^ g4) & 1) == 0) *(LAS f32x4*)(L + L_ST2 + s * 512 + (first ? 0 : 256) + 16 * n16) = f32x4{asf(pkbf(-mean, -mean)), C1 * r, C2c * r * r * r, sd * sd};
      FENCE();
      if (s == 0) TS(16); else if (s == 1) TS(18); else if (s == 2) TS(20); else TS(22);
      if (below(tc, 8)) {
        tile_wait(tc, s + 1);
#pragma unroll
        for (int i = 0; i < 16; ++i) GLD(on, i >> 3, i & 7);
      }
      FENCE();
    }
    if (HP) PRIO(0);
    fh = FHLD();
    await<C_M2>(L, 8);
    fh_check();
#pragma unroll
    for (int j = 0; j < NH; ++j) {
      hb[j / 7][j % 7] = HLD(j / 7, j % 7);
      if (j == (HR ? NH - 4 : NH - 1)) {
        FENCE();
        hres[0] = ld8(rs, hro, 0);
        hres[1] = ld8(rs, hro, 32 * 32768);
        FENCE();
      }
    }
  }
  FENCE();
  PRIO(0);
  {
    unsigned pw[4];
#pragma unroll
    for (int j = 0; j < 4; ++j) pw[j] = lds0 + w * 4096 + n16 * 256 + (((((j + rg) & 3) * 4 + g4) ^ n16) * 16);
    lptr hx = L + L_HX + w * 512 + 8 * l;
    PIN(hx); PIN(pw[0]); PIN(pw[1]); PIN(pw[2]); PIN(pw[3]);
    lptr pr3;
    GAS v2u* op;
    v2u ov[2];
    unsigned oo3;
    f32x4 acc[4];
    v2u ht[2];
    f32x2 r2[8];
#define HMAX(h) umax(umax(umax(h[0].x, h[1].x), umax(h[2].x, h[3].x)), umax(umax(h[4].x, h[5].x), h[6].x))
    auto h_check = [&](unsigned m, int s) {
      v4u* h = hb[s % 3];
      if (RARE(anylane(m == SENT))) {
        int it = 0;
        do {
          asm volatile("" ::: "memory");
          if (m == SENT) {
#pragma unroll
            for (int ks = 0; ks < 7; ++ks) h[ks] = HLD(s, ks);
          }
          m = HMAX(h);
        } while (anylane(m == SENT) && ++it < SPIN_CAP);
      }
      FENCE();
    };
#define PWR(c) *(LAS f32x4*)(uintptr_t)(pw[3 - ((c) & 3)] + ((c) >> 2) * 32768) = acc[(c) & 3]
    unsigned hm = HMAX(hb[0]), mc;
    TS(23); TR(60);
    auto stream = [&](auto M) __attribute__((always_inline)) {
      constexpr int m = M.v, q = m % 14, c = m / 14 * 2 + (m & 1), ks = q / 2, s = c >> 2, dt = 3 - (c & 3);
      if constexpr (m % 28 == 0) { h_check(hm, s); TS(45 + s); }
      if constexpr (ks == 0 && dt > 0) asm volatile("v_mfma_f32_16x16x32_bf16 %0, %1, %2, 0" : "=v"(acc[c & 3]) : "v"(wd[dt]), "v"(hb[s % 3][0]));
      else asm volatile("v_mfma_f32_16x16x32_bf16 %0, %1, %2, %0" : "+v"(acc[c & 3]) : "v"(wd[4 * ks + dt]), "v"(hb[s % 3][ks]));
      SBAR();
      if constexpr (m % 28 == 10) {
        const unsigned lo = cw == (s & 1) ? 0x01000c0cu : 0x0c0c0c0cu, hi = cw == (s & 1) ? 0x03020c0cu : 0x0c0c0c0cu;
        const v2u t = ht[s >> 1];
        acc[3] = f32x4{asf(__builtin_amdgcn_perm(0u, t.x, lo)), asf(__builtin_amdgcn_perm(0u, t.x, hi)), asf(__builtin_amdgcn_perm(0u, t.y, lo)), asf(__builtin_amdgcn_perm(0u, t.y, hi))};
        PIN(acc[3]);
      }
      if constexpr (m == 64) mc = peek<C_M2>(L);
      if constexpr (m == 71) { if (RARE(below(mc, 8))) await<C_M2>(L, 8); FENCE(); }
      if constexpr (m == 80) mc = peek<C_M3>(L);
      if constexpr (m == 85) { const int tk = 2 * w + (l >> 5); pr3 = L + 2 * 32768 + tk * 256 + ((((l & 31) >> 1) ^ tk) * 16) + (l & 1) * 8; PIN(pr3); }
      if constexpr (m == 88) { oo3 = (1536 + 16 * g2r + 2 * w + (l >> 5)) * 1024 + 128 * db + 4 * (l & 31); PIN(oo3); }
      if constexpr (RS && m == 84 + RS) { if (cw == 0) { *op = ov[0]; op[512 * 128] = ov[1]; } }
      if constexpr (RR == 4) {
        if constexpr (m == R2A - 4) mc = peek<C_P2>(L);
        if constexpr (m == R2A) { if (RARE(below(mc, 8))) await<C_P2>(L, 8); FENCE(); }
        if constexpr (m >= R2A && m < R2A + 4) {
          r2[2 * (m - R2A)] = *(volatile LAS f32x2*)(pr3 + 2 * (m - R2A) * 4096);
          r2[2 * (m - R2A) + 1] = *(volatile LAS f32x2*)(pr3 + (2 * (m - R2A) + 1) * 4096);
        }
        if constexpr (m >= R2B && m < R2B + 7) {
          constexpr int i = m - R2B, a = i < 4 ? 2 * i : (i < 6 ? 4 * (i - 4) : 0), b = a + (i < 4 ? 1 : (i < 6 ? 2 : 4));
          float x = r2[a].x + r2[b].x, y = r2[a].y + r2[b].y;
          PIN(x); PIN(y);
          r2[a] = f32x2{x, y};
        }
        if constexpr (m == R2B + 8) ost((GAS unsigned*)((GAS unsigned char*)p.out + (oo3 - 512 * 1024)), pkbf(r2[0].x, r2[0].y), OST & 3);
      }
      if constexpr ((q == 2 || q == 5) && m >= 14 && m / 14 != 6) PWR(c - 2);
      if constexpr (m == 34) { FENCE(); arrive<C_P0>(cnt); }
      if constexpr (m == 62) { FENCE(); arrive<C_P1>(cnt); }
      if constexpr (every(m, 1, D_H2, 21 - NH) >= 0) { constexpr int j = NH + every(m, 1, D_H2, 21 - NH); hb[j / 7][j % 7] = HLD(j / 7, j % 7); }
      if constexpr (m == 2) {
        if (RARE(anylane(umax(hres[0].x, hres[1].x) == SENT))) {
          int it = 0;
          do {
            asm volatile("" ::: "memory");
            hres[0] = ld8(rs, hro, 0);
            hres[1] = ld8(rs, hro, 32 * 32768);
          } while (anylane(umax(hres[0].x, hres[1].x) == SENT) && ++it < SPIN_CAP);
        }
        FENCE();
        *(LAS v2u*)(hx) = hres[0];
        FENCE();
        ht[0] = trd(hx);
      }
      if constexpr (m == 3) {
        *(LAS v2u*)(hx) = hres[1];
        FENCE();
        ht[1] = trd(hx);
      }
      if constexpr (every(m, G_H3, D_H3, 7) >= 0) hb[0][every(m, G_H3, D_H3, 7)] = HLD(3, every(m, G_H3, D_H3, 7));
      if constexpr (m == 26 || m == 54) { hm = HMAX(hb[(s + 1) % 3]); PIN(hm); }
      FENCE();
    };
    sfor<84>(stream);
    asm volatile("s_nop 7");
    FENCE();
    PWR(10); PWR(11);
    FENCE();
    arrive<C_P2>(cnt);
    TS(24);
    const int tk = 4 * rg + g4;
    lptr prd = L + tk * 256 + ((n16 ^ tk) * 16);
    op = (GAS v2u*)((GAS unsigned char*)p.out + (unsigned)((16 * g2r + tk) * 1024 + 128 * db + 8 * n16));
    auto reduce = [&](int s) {
      f32x4 v[8];
#pragma unroll
      for (int j = 0; j < 8; ++j) v[j] = *(LAS f32x4*)(prd + s * 32768 + j * 4096);
      float a[4];
#pragma unroll
      for (int i = 0; i < 4; ++i) {
        a[i] = v[0][i];
#pragma unroll
        for (int j = 1; j < 8; ++j) a[i] += v[j][i];
      }
      const v2u o = {pkbf_s(a[0], a[1]), pkbf_s(a[2], a[3])};
      if (RS && s < 2) { ov[s] = o; PIN(ov[s]); }
      else ost((GAS u64*)(op + s * (512 * 128)), __builtin_bit_cast(u64, o), OST & 3);
      FENCE();
    };
    if (cw == 0) {
      await<C_P0>(L, 8);
      reduce(0);
      await<C_P1>(L, 8);
      reduce(1);
      if (RR == 2) { await<C_P2>(L, 8); reduce(2); }
      if (!RS) { hm = HMAX(hb[0]); PIN(hm); }
    } else {
      if (RR == 1) { await<C_P2>(L, 8); reduce(2); }
      else if (RARE(below(mc, 8))) await<C_M3>(L, 8);
      if (!RS) { hm = HMAX(hb[0]); PIN(hm); }
    }
    if (RS) { FENCE(); hm = HMAX(hb[0]); PIN(hm); }
    FENCE();
    TS(25);
    sfor<28>([&](auto M) __attribute__((always_inline)) { stream(ic<84 + M.v>{}); });
    asm volatile("s_nop 7");
    FENCE();
    PWR(14); PWR(15);
    FENCE();
    arrive<C_P3>(cnt);
    TS(26);
    if (RR == 0 && cw == 0) { await<C_P2>(L, 8); reduce(2); }
    if (RR == 3 && cw) { await<C_P2>(L, 8); reduce(2); }
    TS(49);
    {
      f32x2 v[8];
      int it = 0;
      unsigned done;
      do {
        done = peek<C_P3>(L);
        FENCE();
#pragma unroll
        for (int j = 0; j < 8; ++j) v[j] = *(volatile LAS f32x2*)(pr3 + 32768 + j * 4096);
        FENCE();
      } while (below(done, 8) && ++it < (1 << 22));
      const f32x2 a = ((v[0] + v[1]) + (v[2] + v[3])) + ((v[4] + v[5]) + (v[6] + v[7]));
      ost((GAS unsigned*)((GAS unsigned char*)p.out + oo3), pkbf(a.x, a.y), OST >> 2);
    }
    TS(27);
  }
  asm volatile(".Lcode_end:");
}

extern "C" void kernel_launch(void* const* d_in, const int* in_sizes, int n_in, void* d_out, int out_size, void* d_ws,
                              size_t ws_size, hipStream_t stream) {
  Params p;
  p.x = (const u16*)d_in[0]; p.logA = (const u16*)d_in[1]; p.aim = (const u16*)d_in[2]; p.cre = (const u16*)d_in[3];
  p.cim = (const u16*)d_in[4]; p.logdt = (const u16*)d_in[5]; p.dskip = (const u16*)d_in[6];
  p.wg = (const u16*)d_in[8]; p.wu = (const u16*)d_in[9]; p.wd = (const u16*)d_in[10];
  p.out = (u16*)d_out; p.ws = (unsigned char*)d_ws;
  (void)hipFuncSetAttribute((const void*)fused_kernel, hipFuncAttributeMaxDynamicSharedMemorySize, L_BYTES);
  fused_kernel<<<dim3(256), dim3(512), L_BYTES, stream>>>(p);
}
```
